```python
import torch

def _softmax(x, axis=-1):
    return torch.softmax(x, dim=axis)
def _gelu(x):
    return torch.nn.functional.gelu(x, approximate="tanh")
def _silu(x):
    return torch.nn.functional.silu(x)
def _sigmoid(x):
    return torch.sigmoid(x)
def _layernorm(x, axis=-1, eps=1e-5):
    m = x.mean(dim=axis, keepdim=True)
    v = x.var(dim=axis, keepdim=True, unbiased=False)
    return (x - m) / torch.sqrt(v + eps)
def _scatter_add(tgt, idx, src, axis=0):
    return tgt.clone().index_add_(axis, idx, src)
def _segment_sum(src, seg, n):
    out = torch.zeros((n,) + src.shape[1:], dtype=src.dtype)
    return out.index_add_(0, seg, src)
def _pad(x, pw):
    flat = tuple(v for pair in reversed(pw) for v in pair)
    return torch.nn.functional.pad(x, flat)

kernel_name = 'interleave_layers__sliding_local_dilated__glu_gated_bilinear__prompt'

def setup_inputs():

    _w = (lambda a: (lambda t: t.to(torch.bfloat16) if t.is_floating_point() else t)(torch.as_tensor(a)))
    def _A_setup():
        B, S, D = 1, 2048, 576
        return {
            "x": _w((lambda *s: torch.randn(*s, dtype=torch.bfloat16) * 0.05)(B, S, D)),
            "wqkv": _w((lambda *s: torch.randn(*s, dtype=torch.bfloat16) * 0.05)(D, 3 * D)),
            "wo": _w((lambda *s: torch.randn(*s, dtype=torch.bfloat16) * 0.05)(D, D)),
        }
    _A_in = _A_setup()

    def _B_setup():
        B, S, D, F = 1, 2048, 576, 4608
        return {
            "x": _w((lambda *s: torch.randn(*s, dtype=torch.bfloat16) * 0.05)(B, S, D)),
            "w_gate": _w((lambda *s: torch.randn(*s, dtype=torch.bfloat16) * 0.8)(D, F) * (D ** -0.5)),
            "w_up": _w((lambda *s: torch.randn(*s, dtype=torch.bfloat16) * 0.8)(D, F) * (D ** -0.5)),
            "w_down": _w((lambda *s: torch.randn(*s, dtype=torch.bfloat16) * 0.8)(F, D) * (F ** -0.5)),
        }
    _B_in = _B_setup()

    return {
        "A_x": _A_in["x"],
        "A_wqkv": _A_in["wqkv"],
        "A_wo": _A_in["wo"],
        "B_x": _B_in["x"],
        "B_w_gate": _B_in["w_gate"],
        "B_w_up": _B_in["w_up"],
        "B_w_down": _B_in["w_down"],
    }

def reference(A_x, A_wqkv, A_wo, B_x, B_w_gate, B_w_up, B_w_down):
    def _A_ref(x, wqkv, wo):
        B, S, D = x.shape
        H = 32; Dh = D // H
        W, dil = 8, 2
        qkv = (lambda x, *d: x.permute(*d))((x @ wqkv).reshape(B, S, 3, H, Dh), 2, 0, 3, 1, 4)
        q, k, v = qkv[0], qkv[1], qkv[2]
        scale = Dh ** -0.5
        pos = torch.arange(S)
        off = torch.arange(W) * dil
        idx = pos[:, None] - off[None, :]
        mask = idx >= 0
        idx_c = torch.where(mask, idx, 0).reshape(-1)
        kg = k[:, :, idx_c, :].reshape(B, H, S, W, Dh)
        vg = v[:, :, idx_c, :].reshape(B, H, S, W, Dh)
        logits = torch.einsum("bhsd,bhswd->bhsw", q, kg) * scale
        logits = torch.where(mask[None, None, :, :], logits, -1e9)
        attn = _softmax(logits, axis=-1)
        o = torch.einsum("bhsw,bhswd->bhsd", attn, vg)
        return (lambda x, *d: x.permute(*d))(o, 0, 2, 1, 3).reshape(B, S, D) @ wo

    def _B_ref(x, w_gate, w_up, w_down):
        g = torch.einsum("bsd,df->bsf", x, w_gate)
        u = torch.einsum("bsd,df->bsf", x, w_up)
        h = g * u
        return torch.einsum("bsf,fd->bsd", h, w_down)

    x = A_x
    x = x + _A_ref(_layernorm(x, axis=-1), A_wqkv, A_wo)
    x = x + _B_ref(_layernorm(x, axis=-1), B_w_gate, B_w_up, B_w_down)
    x = x + _A_ref(_layernorm(x, axis=-1), A_wqkv, A_wo)
    x = x + _B_ref(_layernorm(x, axis=-1), B_w_gate, B_w_up, B_w_down)
    return x
```

```cpp
#include <hip/hip_runtime.h>
#include <stdint.h>
#include <type_traits>

#if defined(__HIP_DEVICE_COMPILE__)
#pragma clang attribute push(__attribute__((target("no-packed-fp32-ops"))), apply_to = function)
#endif
typedef __attribute__((ext_vector_type(8))) short bf16x8;
typedef __attribute__((ext_vector_type(4))) short bf16x4;
typedef __attribute__((ext_vector_type(4))) float f32x4;
typedef __attribute__((ext_vector_type(2))) float f32x2;
typedef __attribute__((ext_vector_type(4))) unsigned u32x4;
typedef __attribute__((ext_vector_type(2))) unsigned u32x2;
typedef __bf16 bf2v __attribute__((ext_vector_type(2)));
typedef unsigned short u16;

#define MFMA(b, a, c) __builtin_amdgcn_mfma_f32_16x16x32_bf16(b, a, c, 0, 0, 0)
constexpr int DM = 576, FD = 4608, NQKV = 3 * DM;
constexpr int TB = 18432;
constexpr int HB = 24576;
constexpr int PB = 6144;
#define FSTR 2048
constexpr int FGRP = 256 * FSTR, F2ROW = 19 * 128;
constexpr size_t WS_FL = 4096;
constexpr size_t WS_IS = WS_FL + 4 * FGRP;
constexpr size_t WS_F2 = WS_IS + 4 * 64 * 128;
constexpr size_t WS_O = WS_F2 + 2 * 72 * F2ROW;
constexpr size_t WS_X1 = WS_O + (size_t)4 * 68 * TB;
constexpr size_t WS_X2 = WS_X1 + (size_t)256 * TB;
constexpr size_t WS_X0 = WS_X2 + (size_t)128 * TB;
constexpr size_t WS_P = WS_X0 + (size_t)128 * TB;
constexpr size_t WS_H = WS_P + (size_t)72 * 64 * PB;
constexpr size_t WS_END = WS_H + (size_t)(24 * 64 + 4) * HB;
static_assert(WS_O % 1024 == 0 && WS_END <= ((size_t)256 << 20), "workspace");
constexpr int F_H = 0, F_P = 144, F_O = 216, F_X1 = 240, F_X0 = 18;
constexpr int OLAG = 4;
constexpr int SMEM_BYTES = 163840;
#define PDG 6
#define PDD 4
#define BARG 14
#define BARD 10
#define PR_A 2
#define PR_B 1
#define PR_T 3
#define PR_V 3
#define PR_VA 3
#define PR_QA 0
#define PUTK 10
#define PDB 8
#define PMOD "sc1"
#define DLY_G 400
#define DLY_D 800
#define SLP "s_sleep 8\n\t"
#define DD 2
#define LAGF 8

__device__ __forceinline__ float u2f(unsigned u) { return __builtin_bit_cast(float, u); }
__device__ __forceinline__ int f2i(float f) { return __builtin_bit_cast(int, f); }
__device__ __forceinline__ float rbf(float f) { __bf16 b = (__bf16)f; return u2f((unsigned)__builtin_bit_cast(u16, b) << 16); }
__device__ __forceinline__ unsigned cvtpk(f32x2 v) { return __builtin_bit_cast(unsigned, __builtin_convertvector(v, bf2v)); }
__device__ __forceinline__ unsigned cvt2(float a, float b) { return cvtpk(f32x2{a, b}); }
__device__ __forceinline__ float blo(unsigned w) { return u2f(w << 16); }
__device__ __forceinline__ float bhi(unsigned w) { return u2f(w & 0xffff0000u); }
__device__ __forceinline__ float vmax(float a, float b) { float r; asm("v_max_f32 %0, %1, %2" : "=v"(r) : "v"(a), "v"(b)); return r; }
__device__ __forceinline__ float vmax3(float a, float b, float c) { float r; asm("v_max3_f32 %0, %1, %2, %3" : "=v"(r) : "v"(a), "v"(b), "v"(c)); return r; }
typedef __attribute__((address_space(3))) bf16x4* lds_b4p;
__device__ __forceinline__ bf16x4 tr_read(const char* p) {
  return __builtin_amdgcn_ds_read_tr16_b64_v4i16((lds_b4p)(__attribute__((address_space(3))) void*)(p));
}
__device__ __forceinline__ bf16x8 tr_frag(const char* lo, const char* hi) {
  return __builtin_shufflevector(tr_read(lo), tr_read(hi), 0, 1, 2, 3, 4, 5, 6, 7);
}
__device__ __forceinline__ unsigned lds_addr(const void* p) { return (unsigned)(size_t)(__attribute__((address_space(3))) const char*)p; }
template <int I, int N, class F> __device__ __forceinline__ void static_for(F&& f) {
  if constexpr (I < N) { f(std::integral_constant<int, I>{}); static_for<I + 1, N>(f); }
}
template <int N> using IC = std::integral_constant<int, N>;
template <int N> __device__ __forceinline__ void wait_vm() { asm volatile("s_waitcnt vmcnt(%0)" ::"n"(N) : "memory"); }
template <int N> __device__ __forceinline__ void wait_lg() { asm volatile("s_waitcnt lgkmcnt(%0)" ::"n"(N) : "memory"); }
__device__ __forceinline__ void bar_raw() { asm volatile("s_waitcnt lgkmcnt(0)\n\ts_barrier" ::: "memory"); }
__device__ __forceinline__ void bar_bare() { asm volatile("s_barrier" ::: "memory"); }
__device__ __forceinline__ int sopq(int v) { asm volatile("" : "+s"(v)); return v; }
__device__ __forceinline__ unsigned vopq(unsigned v) { asm volatile("" : "+v"(v)); return v; }

template <bool DEV> __device__ __forceinline__ void dma1(const void* s, unsigned v, unsigned m) {
  if (DEV) asm volatile("s_mov_b32 m0, %2\n\ts_nop 0\n\tglobal_load_lds_dwordx4 %1, %0 sc1" :: "s"(s), "v"(v), "s"(m) : "memory");
  else asm volatile("s_mov_b32 m0, %2\n\ts_nop 0\n\tglobal_load_lds_dwordx4 %1, %0" :: "s"(s), "v"(v), "s"(m) : "memory");
}
__device__ __forceinline__ void touch(const void* p, unsigned lds_dummy) {
  asm volatile("s_mov_b32 m0, %1\n\ts_nop 0\n\tglobal_load_lds_dword %0, off" :: "v"(p), "s"(lds_dummy) : "memory");
}
template <class T> __device__ __forceinline__ void gld16(T& d, const void* s, unsigned v) { asm volatile("global_load_dwordx4 %0, %1, %2 sc1" : "=v"(d) : "v"(v), "s"(s) : "memory"); }
template <class T> __device__ __forceinline__ void gld8(T& d, const void* s, unsigned v) { asm volatile("global_load_dwordx2 %0, %1, %2 sc1" : "+v"(d) : "v"(v), "s"(s) : "memory"); }
__device__ __forceinline__ void gld4(unsigned& d, const void* s, unsigned v) { asm volatile("global_load_dword %0, %1, %2 sc1" : "+v"(d) : "v"(v), "s"(s) : "memory"); }
__device__ __forceinline__ void gst16(const void* s, unsigned v, u32x4 d) {
  asm volatile("global_store_dwordx4 %0, %1, %2 sc1\n\ts_nop 1" :: "v"(v), "v"(d), "s"(s) : "memory");
}
template <int LO> __device__ __forceinline__ void gst4m(const void* s, unsigned v, unsigned d) {
  asm volatile("s_mov_b32 exec_lo, %3\n\ts_mov_b32 exec_hi, 0\n\tglobal_store_dword %0, %1, %2 sc1\n\ts_mov_b64 exec, -1" :: "v"(v), "v"(d), "s"(s), "n"(LO) : "memory");
}
template <int OFF> __device__ __forceinline__ void gld4o(unsigned& d, const void* s, unsigned v) { asm volatile("global_load_dword %0, %1, %2 offset:%3 " PMOD "" : "=v"(d) : "v"(v), "s"(s), "n"(OFF) : "memory"); }
#define LWAIT0() wait_vm<0>()
#define LWAIT(N) wait_vm<N>()
#define VMBX(N) N
template <int OFF> __device__ __forceinline__ void gld16o(u32x4& d, const void* s, unsigned v) { asm volatile("global_load_dwordx4 %0, %1, %2 offset:%3 sc1" : "+v"(d) : "v"(v), "s"(s), "n"(OFF) : "memory"); }
__device__ __forceinline__ void flag_put(const void* s, unsigned voff, unsigned v) {
  asm volatile("s_mov_b64 exec, 1\n\tglobal_store_dword %0, %1, %2 sc1\n\ts_mov_b64 exec, -1" :: "v"(voff), "v"(v), "s"(s) : "memory");
}
struct Cnt { unsigned v, o; };
__device__ __forceinline__ void flag_up(const void* s, Cnt& c) {
  asm volatile("s_mov_b64 exec, 1\n\tglobal_store_dword %1, %0, %2 sc1\n\ts_mov_b64 exec, -1\n\t"
               "v_add_u32 %1, 0x80, %1\n\tv_add_u32 %0, 1, %0\n\tv_and_b32 %1, 0x7ff, %1" : "+v"(c.v), "+v"(c.o) : "s"(s) : "memory");
}
template <int OFF> __device__ __forceinline__ void gst16h(const void* s, unsigned v, u32x4 d, unsigned long long half) {
  asm volatile("s_mov_b64 exec, %3\n\tglobal_store_dwordx4 %0, %1, %2 offset:%4 sc1\n\ts_mov_b64 exec, -1\n\ts_nop 0" :: "v"(v), "v"(d), "s"(s), "s"(half), "n"(OFF) : "memory");
}
template <int MO> __device__ __forceinline__ void dma3(const void* s, unsigned v0, unsigned v1, unsigned v2, unsigned m) {
  asm volatile("s_add_u32 m0, %4, %5\n\ts_nop 0\n\tglobal_load_lds_dwordx4 %1, %0 sc1\n\tglobal_load_lds_dwordx4 %2, %0 offset:1024 sc1\n\t"
               "global_load_lds_dwordx4 %3, %0 offset:2048 sc1" :: "s"(s), "v"(v0), "v"(v1), "v"(v2), "s"(m), "n"(MO) : "memory", "scc");
}
template <int MO, int N> __device__ __forceinline__ void dma4(const void* s, unsigned v, unsigned m) {
  if constexpr (N == 4)
    asm volatile("s_add_u32 m0, %2, %3\n\ts_nop 0\n\tglobal_load_lds_dwordx4 %1, %0 sc1\n\tglobal_load_lds_dwordx4 %1, %0 offset:1024 sc1\n\t"
                 "global_load_lds_dwordx4 %1, %0 offset:2048 sc1\n\tglobal_load_lds_dwordx4 %1, %0 offset:3072 sc1" :: "s"(s), "v"(v), "s"(m), "n"(MO) : "memory", "scc");
  else
    asm volatile("s_add_u32 m0, %2, %3\n\ts_nop 0\n\tglobal_load_lds_dwordx4 %1, %0 sc1\n\tglobal_load_lds_dwordx4 %1, %0 offset:1024 sc1" :: "s"(s), "v"(v), "s"(m), "n"(MO) : "memory", "scc");
}
template <int MO> __device__ __forceinline__ void dma1c(const void* s, unsigned v, unsigned m) {
  asm volatile("s_add_u32 m0, %2, %3\n\ts_nop 0\n\tglobal_load_lds_dwordx4 %1, %0 sc1" :: "s"(s), "v"(v), "s"(m), "n"(MO) : "memory", "scc");
}
template <int MO> __device__ __forceinline__ void dma45c(const void* s, unsigned v, unsigned v4, unsigned m) {
  asm volatile("s_add_u32 m0, %3, %4\n\ts_nop 0\n\tglobal_load_lds_dwordx4 %1, %0 sc1\n\tglobal_load_lds_dwordx4 %1, %0 offset:1024 sc1\n\t"
               "global_load_lds_dwordx4 %1, %0 offset:2048 sc1\n\tglobal_load_lds_dwordx4 %1, %0 offset:3072 sc1\n\t"
               "s_add_u32 m0, %3, %5\n\ts_mov_b32 exec_hi, 0\n\tglobal_load_lds_dwordx4 %2, %0 sc1\n\ts_mov_b32 exec_hi, -1"
               :: "s"(s), "v"(v), "v"(v4), "s"(m), "n"(MO), "n"(MO + 4096) : "memory", "scc");
}
#define REACHED(V, N) "v_subrev_u32 " V ", " N ", " V "\n\tv_cmp_lt_u32 vcc, 0x7f, " V "\n\t"
template <int N> __device__ __forceinline__ void check_flags(unsigned& v, const void* s, unsigned voff, unsigned need, unsigned deadline) {
  asm volatile("s_waitcnt vmcnt(%5)\n\t" REACHED("%0", "%4") "s_cbranch_vccz .Lok%=\n\ts_cmp_eq_u32 %1, 0\n\ts_cbranch_scc1 .Lok%=\n"
               ".Lsp%=:\n\t" SLP "s_memtime vcc\n\ts_waitcnt lgkmcnt(0)\n\ts_sub_u32 vcc_lo, vcc_lo, %1\n\ts_cmp_ge_i32 vcc_lo, 0\n\ts_cbranch_scc1 .Lok%=\n\t"
               "global_load_dword %0, %2, %3 " PMOD "\n\ts_waitcnt vmcnt(0)\n\t" REACHED("%0", "%4") "s_cbranch_vccnz .Lsp%=\n.Lok%=:"
               : "+v"(v) : "s"(deadline), "v"(voff), "s"(s), "s"(need), "n"(N) : "memory", "vcc", "scc");
}
__device__ __forceinline__ void poll(const void* s, unsigned voff, unsigned need, unsigned deadline) {
  unsigned v; gld4o<0>(v, s, voff); check_flags<0>(v, s, voff, need, deadline);
}
__device__ __forceinline__ unsigned make_tag(unsigned nonce) { return nonce * 2654435761u + 0x7F4A7C15u; }
template <class A, class B, class C, class D> __device__ __forceinline__ void tie(A& a, B& b, C& c, D& d) {
  asm volatile("" : "+v"(a)); asm volatile("" : "+v"(b)); asm volatile("" : "+v"(c)); asm volatile("" : "+v"(d));
}
__device__ __forceinline__ void reduce_ask(u32x4 (&pp)[6], u32x4& xp, unsigned& i2, unsigned& fw, const void* rp, const void* rx, const void* ri, const void* rf,
                                           unsigned l16, unsigned iv, unsigned pv, unsigned tag, unsigned deadline, int step) {
  asm volatile("s_cmp_gt_u32 %18, 1\n\ts_cbranch_scc1 .Lz%=\n\ts_cmp_eq_u32 %18, 1\n\ts_cbranch_scc1 .Lc%=\n\t"
               "global_load_dword %8, %15, %12 " PMOD "\n\ts_branch .Lz%=\n"
               ".Lc%=:\n\t"
               REACHED("%8", "%16") "s_cbranch_vccz .Lr%=\n\t"
               SLP "s_memtime vcc\n\ts_waitcnt lgkmcnt(0)\n\ts_sub_u32 vcc_lo, vcc_lo, %17\n\ts_cmp_ge_i32 vcc_lo, 0\n\ts_cbranch_scc1 .Lr%=\n\t"
               "global_load_dword %8, %15, %12 " PMOD "\n\ts_waitcnt vmcnt(0)\n\ts_branch .Lc%=\n"
               ".Lr%=:\n\tglobal_load_dwordx4 %0, %13, %9 offset:-3072 sc1\n\tglobal_load_dwordx4 %1, %13, %9 offset:-2048 sc1\n\t"
               "global_load_dwordx4 %2, %13, %9 offset:-1024 sc1\n\tglobal_load_dwordx4 %3, %13, %9 sc1\n\tglobal_load_dwordx4 %4, %13, %9 offset:1024 sc1\n\t"
               "global_load_dwordx4 %5, %13, %9 offset:2048 sc1\n\tglobal_load_dwordx4 %6, %13, %10 sc1\n\tglobal_load_dword %7, %14, %11 sc1\n.Lz%=:"
               : "+v"(pp[0]), "+v"(pp[1]), "+v"(pp[2]), "+v"(pp[3]), "+v"(pp[4]), "+v"(pp[5]), "+v"(xp), "+v"(i2), "+v"(fw)
               : "s"(rp), "s"(rx), "s"(ri), "s"(rf), "v"(l16), "v"(iv), "v"(pv), "s"(tag), "s"(deadline), "s"(step) : "memory", "vcc", "scc");
}
__device__ __forceinline__ Cnt flag_init(const char* s, unsigned l16, unsigned tag) {
  const unsigned z = tag - 4096u;
  gst16(s, l16, u32x4{z, z, z, z}); gst16(s + 1024, l16, u32x4{z, z, z, z});
  wait_vm<0>();
  return Cnt{vopq(tag), vopq(0)};
}
__device__ __forceinline__ const char* line_of(const char* fl, unsigned n) { const char* p = fl + (n & 15) * 128; asm volatile("" : "+s"(p)); return p; }
__device__ __forceinline__ void adv(const char*& p, long d) { p += d; asm volatile("" : "+s"(p)); }

struct Prof {};
#define PT(I)
#define PTQ(I)
#define PROF_INIT()
#define PROF_OUT()
#define TR(J, I)
#define TR_OUT()
template <bool Z> __device__ __forceinline__ void mf(f32x4& c, const bf16x8& a, const bf16x8& b) {
  if (Z) asm volatile("v_mfma_f32_16x16x32_bf16 %0, %1, %2, 0" : "=&v"(c) : "v"(a), "v"(b));
  else asm volatile("v_mfma_f32_16x16x32_bf16 %0, %1, %2, %0" : "+v"(c) : "v"(a), "v"(b));
}
template <int OFF, class T> __device__ __forceinline__ void lds_rd(T& d, unsigned a) { asm volatile("ds_read_b128 %0, %1 offset:%2" : "=v"(d) : "v"(a), "n"(OFF)); }
template <int OFF, class T> __device__ __forceinline__ void lds_rd8(T& d, unsigned a) { asm volatile("ds_read_b64 %0, %1 offset:%2" : "=v"(d) : "v"(a), "n"(OFF)); }
template <int OFF, class T> __device__ __forceinline__ void lds_wr(unsigned a, const T& d) { asm volatile("ds_write_b128 %0, %1 offset:%2" :: "v"(a), "v"(d), "n"(OFF) : "memory"); }
__device__ __forceinline__ void mfc(f32x4& d, const bf16x8& a, const bf16x8& b, const f32x4& c) {
  asm volatile("v_mfma_f32_16x16x32_bf16 %0, %1, %2, %3" : "=&v"(d) : "v"(a), "v"(b), "v"(c));
}
__device__ __forceinline__ void max4(float (&s)[4], const f32x4& a, const f32x4& b) {
  asm volatile("v_max_f32 %0, %4, %8\n\tv_max_f32 %1, %5, %9\n\tv_max_f32 %2, %6, %10\n\tv_max_f32 %3, %7, %11" : "=&v"(s[0]), "=&v"(s[1]), "=&v"(s[2]), "=&v"(s[3])
               : "v"(a[0]), "v"(a[1]), "v"(a[2]), "v"(a[3]), "v"(b[0]), "v"(b[1]), "v"(b[2]), "v"(b[3]));
}
struct Soft { float s[4], t0, t1; unsigned own[2], oth[2], mo[2][2]; };
#define QK_M(D, W, X, C) "v_mfma_f32_16x16x32_bf16 " D ", " W ", " X ", " C "\n\t"
#define QK_G0 "v_permlane32_swap_b32 %8, %9\n\t"
#define QK_G1 "v_add_f32 %8, %8, %9\n\t"
#define QK_G2 "v_rcp_f32 %8, %8\n\t"
#define QK_G3 "v_mul_f32 %4, %4, %8\n\t"
#define QK_G4 "v_mul_f32 %5, %5, %8\n\tv_mul_f32 %6, %6, %8\n\t"
#define QK_G5 "v_mul_f32 %7, %7, %8\n\t"
#define QK_G6 "v_cvt_pk_bf16_f32 %4, %4, %5\n\tv_cvt_pk_bf16_f32 %6, %6, %7\n\t"
#define QK_G7 "v_and_b32 %10, %4, %21\n\t"
#define QK_G8 "v_and_b32 %11, %6, %22\n\tv_xor_b32 %12, %4, %10\n\t"
#define QK_G9 "v_xor_b32 %13, %6, %11\n\t"
#define QK_OUT "+v"(f.s[0]), "+v"(f.s[1]), "+v"(f.s[2]), "+v"(f.s[3]), "+v"(f.t0), "+v"(f.t1), "+v"(f.own[0]), "+v"(f.own[1]), "+v"(f.oth[0]), "+v"(f.oth[1])
#define QK_PAIR(C0, C1, GA, GB) asm volatile(QK_M("%0", "%14", "%18", C0) "ds_read_b128 %2, %20 offset:%23\n\t" QK_M("%1", "%15", "%18", C1) GA \
    QK_M("%0", "%16", "%19", "%0") "ds_read_b128 %3, %20 offset:%24\n\t" QK_M("%1", "%17", "%19", "%1") GB \
    : "+v"(a0), "+v"(a1), "=&v"(y0), "=&v"(y1), QK_OUT \
    : "v"(w0[0]), "v"(w0[1]), "v"(w1[0]), "v"(w1[1]), "v"(x0), "v"(x1), "v"(la), "v"(f.mo[E][0]), "v"(f.mo[E][1]), "n"(OFF), "n"(OFF + 1024))
template <int P, int E, int OFF> __device__ __forceinline__ void qk_pair(f32x4& a0, f32x4& a1, const bf16x8 (&w0)[2], const bf16x8 (&w1)[2], bf16x8 x0, bf16x8 x1,
                                                                  bf16x8& y0, bf16x8& y1, unsigned la, Soft& f) {
  if constexpr (P == 0) QK_PAIR("0", "0", QK_G0, QK_G1);
  if constexpr (P == 1) QK_PAIR("%0", "%1", QK_G2, QK_G3);
  if constexpr (P == 2) QK_PAIR("%0", "%1", QK_G4, QK_G5);
  if constexpr (P == 3) QK_PAIR("%0", "%1", QK_G6, QK_G7);
  if constexpr (P == 4) QK_PAIR("%0", "%1", QK_G8, QK_G9);
  if constexpr (P == 5) QK_PAIR("%0", "%1", "", "");
}
__device__ __forceinline__ void soft3(Soft& f) {
  int u = 0;
  asm volatile("s_nop 1\n\t" QK_G0 QK_G1 QK_G2 "s_nop 0\n\t" QK_G3 QK_G4 QK_G5 QK_G6 QK_G7 QK_G8 QK_G9 : "+s"(u), "+s"(u), "+s"(u), "+s"(u), QK_OUT
               : "n"(0), "n"(0), "n"(0), "n"(0), "n"(0), "n"(0), "n"(0), "v"(f.mo[0][0]), "v"(f.mo[0][1]));
}
__device__ __forceinline__ void mf8(f32x4& a0, f32x4& a1, const bf16x8 (&w0)[2], const bf16x8 (&w1)[2], const bf16x8 (&w2)[2], const bf16x8 (&w3)[2],
                                    const bf16x8& x0, const bf16x8& x1, const bf16x8& x2, const bf16x8& x3) {
  asm volatile(QK_M("%1", "%3", "%10", "%1") QK_M("%0", "%2", "%10", "%0") QK_M("%1", "%5", "%11", "%1") QK_M("%0", "%4", "%11", "%0")
               QK_M("%1", "%7", "%12", "%1") QK_M("%0", "%6", "%12", "%0") QK_M("%1", "%9", "%13", "%1") QK_M("%0", "%8", "%13", "%0")
               : "+v"(a0), "+v"(a1) : "v"(w0[0]), "v"(w0[1]), "v"(w1[0]), "v"(w1[1]), "v"(w2[0]), "v"(w2[1]), "v"(w3[0]), "v"(w3[1]), "v"(x0), "v"(x1), "v"(x2), "v"(x3));
}
__device__ __forceinline__ void soft1(float& s0, float& s1, float& s2, float& s3, float& t0, float& t1, float sc) {
  asm volatile("v_max3_f32 %4, %0, %1, %2\n\tv_max_f32 %5, %4, %3\n\tv_max_f32 %4, %4, %3\n\ts_nop 1\n\tv_permlane16_swap_b32 %4, %5\n\t"
               "v_max_f32 %4, %4, %5\n\tv_mov_b32 %5, %4\n\ts_nop 1\n\tv_permlane32_swap_b32 %4, %5\n\tv_max_f32 %4, %4, %5\n\tv_mul_f32 %4, %4, %6\n\t"
               "v_fma_f32 %0, %0, %6, -%4\n\tv_fma_f32 %1, %1, %6, -%4\n\tv_fma_f32 %2, %2, %6, -%4\n\tv_fma_f32 %3, %3, %6, -%4"
               : "+v"(s0), "+v"(s1), "+v"(s2), "+v"(s3), "=&v"(t0), "=&v"(t1) : "v"(sc));
}
__device__ __forceinline__ void soft2(float& s0, float& s1, float& s2, float& s3, float& t0, float& t1) {
  asm volatile("v_exp_f32 %0, %0\n\tv_exp_f32 %1, %1\n\tv_exp_f32 %2, %2\n\tv_exp_f32 %3, %3\n\t"
               "v_add_f32 %4, %0, %1\n\tv_add_f32 %5, %2, %3\n\tv_add_f32 %4, %4, %5\n\tv_mov_b32 %5, %4\n\ts_nop 1\n\t"
               "v_permlane16_swap_b32 %4, %5\n\tv_add_f32 %4, %4, %5\n\tv_mov_b32 %5, %4"
               : "+v"(s0), "+v"(s1), "+v"(s2), "+v"(s3), "=&v"(t0), "=&v"(t1));
}
constexpr int imin(int a, int b) { return a < b ? a : b; }
template <int NK, int PD = PDG> struct Ring { bf16x8 q[PD + 1]; Prof pr; };
template <int O, int K0 = 0, int K1 = 99, int NK, int PD> __device__ __forceinline__ void prime(Ring<NK, PD>& b, unsigned la) {
  static_for<K0, imin(K1, PD)>([&](auto kc) { lds_rd<O + decltype(kc)::value * 1024>(b.q[decltype(kc)::value], la); });
}
template <int O, int BAR, int VMB, int NK, int PD, class F, class H> __device__ __forceinline__ void stream(Ring<NK, PD>& b, unsigned la, F&& f, H&& hook) {
  static_assert(BAR < 0 || BAR + PD >= NK, "barrier before the last read");
  if constexpr (BAR >= 0) PT(0);
  asm volatile("s_setprio %0" :: "n"(BAR < 0 ? PR_A : PR_B));
  static_for<0, NK>([&](auto kc) {
    constexpr int k = decltype(kc)::value;
    if constexpr (k == BAR) asm volatile("s_setprio %0\n\ts_waitcnt vmcnt(%1) lgkmcnt(0)\n\ts_barrier" :: "n"(PR_T), "n"(VMB) : "memory");
    constexpr int KZ = (BAR >= 0) ? BAR : NK - PD + 2;
    if constexpr (k < KZ) __builtin_amdgcn_s_waitcnt(0xC07F | (imin(PD - 1, NK - 1 - k) << 8));
    if constexpr (BAR < 0 && k == KZ) __builtin_amdgcn_s_waitcnt(0xC07F);
    f(kc, IC<0>{}, b.q[k % (PD + 1)]);
    if constexpr (k + PD < NK) lds_rd<O + (k + PD) * 1024>(b.q[(k + PD) % (PD + 1)], la);
    f(kc, IC<1>{}, b.q[k % (PD + 1)]);
    hook(kc);
  });
  if constexpr (BAR < 0) asm volatile("s_setprio %0" :: "n"(PR_VA));
  if constexpr (BAR < 0) PT(1);
}
template <int P> __device__ __forceinline__ void prio() { asm volatile("s_setprio %0" :: "n"(P)); }
__device__ __forceinline__ void settle() { asm volatile("s_nop 7" ::: "memory"); }

#define STAMP_AT(I)
#define STAMP_OUT()

template <int R, int NP, class I, class Q> __device__ __forceinline__ unsigned load_w(char* smem, int wave, const char* ws, I&& issue, Q&& rd) {
  if (wave == 3) STAMP_AT(72);
  static_for<0, 3>([&](auto rc) { issue(rc); });
  unsigned nonce;
  asm volatile("s_load_dword %0, %1, 0x0\n\ts_waitcnt lgkmcnt(0)" : "=s"(nonce) : "s"(ws) : "memory");
  static_for<0, R>([&](auto rc) {
    constexpr int r = decltype(rc)::value;
    wait_vm<imin(2, R - 1 - r) * NP>();
    bar_bare();
    if (wave == 3 && r < 6) STAMP_AT(73 + r);
    rd(rc);
    bar_raw();
    if constexpr (r + 3 < R) issue(IC<r + 3>{});
  });
  return nonce;
}
__device__ __forceinline__ bf16x8 ones8() { return __builtin_bit_cast(bf16x8, u32x4{0x3F803F80u, 0x3F803F80u, 0x3F803F80u, 0x3F803F80u}); }
__device__ __forceinline__ bf16x8 asb(u32x4 v) { return __builtin_bit_cast(bf16x8, v); }
__device__ __forceinline__ u32x4 widen(unsigned w0, unsigned w1) {
  const auto s0 = __builtin_amdgcn_permlane16_swap(w0, w0, false, false);
  const auto s1 = __builtin_amdgcn_permlane16_swap(w1, w1, false, false);
  return u32x4{s0[0], s1[0], s0[1], s1[1]};
}
__device__ __forceinline__ u32x4 pack8(const f32x4& a, const f32x4& b) { return u32x4{cvt2(a[0], a[1]), cvt2(a[2], a[3]), cvt2(b[0], b[1]), cvt2(b[2], b[3])}; }
__device__ __forceinline__ f32x2 row_stat(float sum, float sq) {
  const float mean = sum * (1.0f / 576.0f);
  const float var = fmaxf(sq * (1.0f / 576.0f) - mean * mean, 0.0f);
  return f32x2{-rbf(mean), __builtin_amdgcn_rcpf(rbf(__builtin_amdgcn_sqrtf(rbf(rbf(var) + 1e-5f))))};
}
__device__ __forceinline__ float diag(const f32x4& a, int p) { return (p == 0) ? a[0] : (p == 1) ? a[1] : (p == 2) ? a[2] : a[3]; }

constexpr unsigned SPIN = 400000000u;
struct Ctx { char* ws; int g, wave, lane; unsigned dl; };
constexpr unsigned long long HALF = 0x0000FFFF0000FFFFull;

__device__ __forceinline__ void role_gu(char* smem, const u16* wg, const u16* wu, u16* out, const Ctx& cx, int c) {
  const int wave = cx.wave, lane = cx.lane;
  const unsigned sm0 = lds_addr(smem);
  unsigned nonce;
  const int r = lane & 15, g = lane >> 4, q = r >> 2, p = lane & 3;
  bf16x8 W[18][2];
  {
    constexpr int BUF = 48 * 1056;
    const char* src = (const char*)((wave >> 2) ? wu : wg) + c * 256;
    const unsigned vo = (4 * (g & 1) + 16 * (g >> 1)) * (FD * 2) + r * 16;
    const char* rb = smem + ((g & 1) * 4 + q) * 1056 + (g >> 1) * 512 + wave * 32 + p * 8;
    nonce = load_w<6, 6>(smem, wave, cx.ws, [&](auto rc) {
      constexpr int rr = decltype(rc)::value;
#pragma unroll
      for (int j = 0; j < 6; ++j) {
        const int idx = 6 * (wave & 3) + j, id = idx & 7;
        dma1<false>(src + (size_t)(32 * (3 * rr + (idx >> 3)) + 8 * (id >> 2) + (id & 3)) * (FD * 2), vo, sm0 + (rr % 3) * BUF + ((wave >> 2) * 24 + idx) * 1056);
      }
    }, [&](auto rc) {
      constexpr int rr = decltype(rc)::value;
#pragma unroll
      for (int kl = 0; kl < 3; ++kl)
#pragma unroll
        for (int m = 0; m < 2; ++m) { const char* a = rb + (rr % 3) * BUF + (m * 24 + kl * 8) * 1056; W[3 * rr + kl][m] = tr_frag(a, a + 256); }
    });
#pragma unroll
    for (int m = 0; m < 2; ++m) {
      f32x4 cs = {0.f, 0.f, 0.f, 0.f};
#pragma unroll
      for (int k = 0; k < 18; ++k) cs = MFMA(ones8(), W[k][m], cs);
      const float cm = cs[0] * (1.0f / 576.0f);
#pragma unroll
      for (int k = 0; k < 18; ++k) {
        u32x4 w = __builtin_bit_cast(u32x4, W[k][m]);
#pragma unroll
        for (int j = 0; j < 4; ++j) { const unsigned x = w[j]; w[j] = cvt2(blo(x) - cm, bhi(x) - cm); }
        W[k][m] = asb(w);
        asm volatile("" : "+v"(W[k][m]));
      }
    }
  }
  const unsigned tag = make_tag(nonce);
  const char* fl = cx.ws + WS_FL + cx.g * FGRP;
  const char* hb = cx.ws + WS_H + (size_t)(cx.g * 6 + c / 6) * 64 * HB + (c % 6) * 4096 + wave * 512 - HB;
  const unsigned l16 = vopq(lane * 16), hv = vopq((g >> 1) * 256 + r * 16);
  const unsigned la[2] = {vopq(sm0 + l16), vopq(sm0 + l16 + 2 * TB)};
  f32x4 ag, au;
  Ring<18> bq;
  auto mm = [&](auto kc, auto jc, const bf16x8& x) {
    constexpr int k = decltype(kc)::value;
    if constexpr (decltype(jc)::value == 0) mf<k == 0>(ag, W[k][0], x); else mf<k == 0>(au, W[k][1], x);
  };
  auto epi = [&]() {
    asm volatile("" : "+v"(ag), "+v"(au));
    return widen(cvt2(ag[0] * au[0], ag[1] * au[1]), cvt2(ag[2] * au[2], ag[3] * au[3]));
  };
  if (wave == 1) STAMP_AT(70);
  if (wave < 4) {
    auto loop = [&](auto pc) {
      constexpr int KIND = decltype(pc)::value;
      const char* xs = cx.ws + WS_X1 + (size_t)cx.g * 64 * TB;
      unsigned fw, pv, vf[5], dl = cx.dl, cnt = DD + 1;
      const char *fr = line_of(fl, cnt), *fn;
      auto fetch = [&](auto sc) {
        static_for<0, 4>([&](auto ic) { constexpr int i = decltype(ic)::value; dma4<decltype(sc)::value * TB + i * 4096, 4>(xs, vf[i], sm0); });
        dma4<decltype(sc)::value * TB + 16384, 2>(xs, vf[4], sm0);
      };
      if constexpr (KIND == 1) {
        pv = vopq((F_X1 + (lane & 7)) * FSTR);
#pragma unroll
        for (int i = 0; i < 5; ++i) vf[i] = vopq(l16 + i * 4096);
        static_for<0, DD>([&](auto ic) {
          constexpr int i = decltype(ic)::value;
          poll(fl + (i + 1) * 128, pv, tag + i + 1, cx.dl);
          if constexpr (i == DD - 1) gld4o<0>(fw, fr, pv);
          fetch(ic);
          if constexpr (i < DD - 1) adv(xs, TB);
        });
        wait_vm<18>(); tie(fw, fw, fw, fw);
      }
      bar_bare();
      int tgt = c;
      PROF_INIT();
#pragma nounroll
      for (int it = 0; it < 16; ++it) {
        static_for<0, 4>([&](auto jc) {
          constexpr int j = decltype(jc)::value;
          prime<(j & 1) * TB>(bq, la[j >> 1]);
          stream<(j & 1) * TB, -1, 0>(bq, la[j >> 1], mm, [&](auto kc) {
            constexpr int k = decltype(kc)::value;
            if constexpr (k == 13) adv(hb, HB);
            if constexpr (KIND == 1 && k == 14) adv(xs, TB);
            if constexpr (KIND == 1 && k == 16) fn = line_of(fl, cnt + 1);
            if constexpr (KIND == 1 && k == 15 && j == 4 - DD) dl = sopq((it == 15) ? 0u : dl);
          });
          if constexpr (KIND == 1) {
            check_flags<19>(fw, fr, pv, tag + cnt, dl);
            PTQ(2);
            fr = fn; ++cnt;
            gld4o<0>(fw, fr, pv);
            fetch(IC<(j + DD) & 3>{});
            PTQ(4);
          } else settle();
          gst16h<0>(hb, hv, epi(), HALF);
          PTQ(0);
          if constexpr (KIND == 1) { LWAIT(20); } else { LWAIT(1); }
          PTQ(2);
          if constexpr (KIND == 2) {
            if (4 * it + j == tgt) {
              f32x4 s1 = {0.f, 0.f, 0.f, 0.f}, s2 = s1;
#pragma unroll
              for (int k = 0; k < 18; ++k) {
                const bf16x8 x = *(const bf16x8*)(smem + j * TB + k * 1024 + lane * 16);
                s1 = MFMA(ones8(), x, s1); s2 = MFMA(x, x, s2);
              }
              const float is = row_stat(s1[0], diag(s2, p))[1];
              const float i2 = u2f(__builtin_amdgcn_ds_bpermute((r + 16 * q) * 4, f2i(is * is)));
              gst4m<-1>(cx.ws + WS_IS + (cx.g * 64 + tgt) * 128, lane * 4, f2i(i2));
              tgt += 32;
            }
            { if constexpr (j == 3) STAMP_AT(it); }
          }
          bar_bare();
          PTQ(3);
        });
      }
      PROF_OUT();
      wait_vm<0>();
      if constexpr (KIND == 1) tie(fw, fw, fw, fw);
      bar_bare();
      if constexpr (KIND == 2) { STAMP_AT(20); STAMP_OUT(); }
    };
    if (wave == 0) loop(IC<1>{});
    else if (wave == 1) { if (c >= 32) c = 99; loop(IC<2>{}); }
    else loop(IC<0>{});
  } else {
    const char* fp = fl + (F_H + c * 4 + wave - 4) * FSTR;
    const int kb = 2 * (c % 9) + (wave & 1), k6 = kb % 6;
    int tau = c / 9 + 2 * (wave & 2);
    u32x4 pp[6] = {}, xp = {}, sel0, sel1; unsigned i2 = 0, fw = 0;
#pragma unroll
    for (int e = 0; e < 2; ++e) {
      sel0[e] = sel1[2 + e] = (r == 4 * g + 2 * e ? 0x3F80u : 0u) | (r == 4 * g + 2 * e + 1 ? 0x3F800000u : 0u);
      sel0[2 + e] = sel1[e] = 0u;
    }
    tie(sel0, sel1, sel1, sel1);
    const unsigned iv = vopq(r * 4), pq = vopq((F_P + ((kb / 6) * 4 + (k6 < 4 ? k6 : 2 * k6 - 8 + ((lane >> 3) & 1))) * 6 + min(lane & 7, 5)) * FSTR);
    const char* rp = cx.ws + WS_P + (size_t)((cx.g * 18 + kb) * 64 + tau) * PB + 3072;
    const char* rx = cx.ws + WS_X1 + (size_t)(cx.g * 64 + tau) * TB + kb * 1024;
    const char* ri = cx.ws + WS_IS + (cx.g * 64 + tau) * 128;
    const int i0 = (cx.g & 1) * 64 + (1 - (cx.g >> 1)) * 32;
    const char* rf = line_of(fl, tau + 1);
    gst16h<0>(cx.ws + WS_F2 + ((cx.g & 1) * 8 + i0 + tau) * F2ROW + kb * 128, (lane >> 3) * (8 * F2ROW) + (lane & 7) * 16, u32x4{~tag, ~tag, ~tag, ~tag} ^ 0x7FFFFFFFu, 0xFFFFFFFFull);
    Cnt vt = flag_init(fp, l16, tag);
    auto ask = [&](int step) { reduce_ask(pp, xp, i2, fw, rp, rx, ri, rf, l16, iv, pq, tag + tau + 1, cx.dl, step); };
    auto finish = [&]() {
      f32x4 v0 = {0.f, 0.f, 0.f, 0.f}, v1 = v0;
#pragma unroll
      for (int j = 0; j < 6; ++j) { v0 = MFMA(asb(sel0), asb(pp[j]), v0); v1 = MFMA(asb(sel1), asb(pp[j]), v1); }
      const bool last = tau >= 32;
      const char* ob = last ? (const char*)out + (size_t)((cx.g >> 1) * 1024 + 32 * (63 - tau) + (cx.g & 1)) * (DM * 2) + kb * 64 : cx.ws + WS_X2 + (size_t)(i0 + tau) * TB + kb * 1024;
      const float sc = u2f(i2);
      auto fin2 = [&](float a, float b2, unsigned x) { return cvt2(fmaf(a, sc, blo(x)), fmaf(b2, sc, bhi(x))); };
      gst16(ob, last ? r * 2304 + g * 16 : l16, u32x4{fin2(v0[0], v0[1], xp[0]), fin2(v0[2], v0[3], xp[1]), fin2(v1[0], v1[1], xp[2]), fin2(v1[2], v1[3], xp[3])});
    };
    auto next = [&]() { adv(rp, 8 * PB); adv(rx, 8 * TB); adv(ri, 8 * 128); tau += 8; rf = line_of(fl, tau + 1); };
    bar_bare();
    prime<0>(bq, la[0]);
    wait_lg<0>();
    PROF_INIT();
#pragma nounroll
    for (int it = 0; it < 16; ++it) {
      static_for<0, 4>([&](auto jc) {
        constexpr int j = decltype(jc)::value, j1 = (j + 1) & 3;
        stream<(j & 1) * TB, BARG, 63>(bq, la[j >> 1], mm, [&](auto kc) { if constexpr (decltype(kc)::value == 12) adv(hb, HB); });
        settle();
        u32x4 h = epi();
        asm volatile("" : "+v"(h));
        PTQ(0);
        LWAIT0();
        PTQ(4);
        flag_up(fp, vt);
        gst16h<0>(hb, hv, h, HALF);
        const int step = 4 * it + j - LAGF - tau;
        ask(step);
        if (__builtin_expect(step >= 2, 0)) {
          if (step == 2) finish();
          else {
            if (tau < 32) flag_put(cx.ws + WS_F2 + ((cx.g & 1) * 8 + i0 + tau) * F2ROW + kb * 128, l16, vt.v);
            next();
          }
        }
        prio<0>();
        prime<(j1 & 1) * TB>(bq, la[j1 >> 1]);
        if constexpr (j == 3) wait_lg<0>();
      });
    }
    PROF_OUT();
    wait_vm<0>();
    bar_bare();
    flag_up(fp, vt);
    if (64 - LAGF - tau >= 3) next();
#pragma nounroll
    while (tau < 64) {
      ask(0); wait_vm<0>(); ask(1); wait_vm<0>(); ask(2);
      finish(); next();
    }
  }
}

__device__ __forceinline__ void role_dn(char* smem, const u16* wd, const Ctx& cx, int ks, int ns) {
  const int wave = cx.wave, lane = cx.lane;
  const unsigned sm0 = lds_addr(smem);
  unsigned nonce;
  const int r = lane & 15, g = lane >> 4, q = r >> 2, p = lane & 3;
  const int kq = wave >> 2, nq = wave & 3;
  constexpr int SL = 24576, EX = 4 * SL;
  bf16x8 W[12][3];
  {
    constexpr int BUF = 49152;
    const char* src = (const char*)wd + (size_t)(768 * ks) * (DM * 2) + ns * 384;
    unsigned vo[6];
#pragma unroll
    for (int j = 0; j < 6; ++j) {
      const unsigned ch = (wave * 6 + j) * 64 + lane, row = ch / 24;
      vo[j] = ((row >> 6) * 384 + (row & 63)) * (DM * 2) + (ch - row * 24) * 16;
    }
    const char* rb = smem + (kq * 64 + 8 * g + q) * 384;
    nonce = load_w<6, 6>(smem, wave, cx.ws, [&](auto rc) {
      constexpr int rr = decltype(rc)::value;
#pragma unroll
      for (int j = 0; j < 6; ++j) dma1<false>(src + rr * 64 * (DM * 2), vo[j], sm0 + (rr % 3) * BUF + (wave * 6 + j) * 1024);
    }, [&](auto rc) {
      constexpr int rr = decltype(rc)::value;
#pragma unroll
      for (int kl = 0; kl < 2; ++kl)
#pragma unroll
        for (int m = 0; m < 3; ++m) {
          const char* a = rb + (rr % 3) * BUF + kl * 32 * 384 + ((m < 2) ? nq * 64 + p * 16 + m * 8 : 256 + nq * 32 + p * 8);
          W[2 * rr + kl][m] = tr_frag(a, a + 4 * 384);
          asm volatile("" : "+v"(W[2 * rr + kl][m]));
        }
    });
  }
  const unsigned tag = make_tag(nonce);
  const char* fl = cx.ws + WS_FL + cx.g * FGRP;
  const unsigned l16 = vopq(lane * 16);
  const unsigned la[2] = {vopq(sm0 + l16 + kq * 12288), vopq(sm0 + l16 + kq * 12288 + 2 * SL)};
  const unsigned ea = vopq(sm0 + EX + nq * 3072 + l16);
  f32x4 acc[3];
  auto mm = [&](auto kc, auto jc, const bf16x8& x) {
    constexpr int k = decltype(kc)::value;
    if constexpr (decltype(jc)::value == 0) mf<k == 0>(acc[0], W[k][0], x);
    else { mf<k == 0>(acc[1], W[k][1], x); mf<k == 0>(acc[2], W[k][2], x); }
  };
  if (wave == 1) STAMP_AT(70);
  if (wave < 4) {
    Ring<12, PDD> bq;
    const char* hs = cx.ws + WS_H + (size_t)(cx.g * 6 + ks) * 64 * HB + wave * 6144;
    const unsigned l3 = vopq(l16 + 3072), mb = sm0 + wave * 6144, pv = vopq((F_H + 24 * ks + min(lane, 23)) * FSTR);
    auto fetch = [&](auto sc) { constexpr int sl = decltype(sc)::value; dma3<sl * SL>(hs, l16, l16, l16, mb); dma3<sl * SL + 3072>(hs, l3, l3, l3, mb); };
    unsigned fw, dl = cx.dl, cnt = DD + 1;
    const char *fr = line_of(fl, cnt), *fn;
    static_for<0, DD>([&](auto ic) {
      constexpr int i = decltype(ic)::value;
      poll(fl + (i + 1) * 128, pv, tag + i + 1, cx.dl);
      if constexpr (i == DD - 1) gld4o<0>(fw, fr, pv);
      fetch(ic);
      if constexpr (i < DD - 1) adv(hs, HB);
    });
    wait_vm<6>(); tie(fw, fw, fw, fw);
    bar_bare();
    PROF_INIT();
#pragma nounroll
    for (int it = 0; it < 16; ++it) {
      static_for<0, 4>([&](auto jc) {
        constexpr int j = decltype(jc)::value;
        prime<(j & 1) * SL>(bq, la[j >> 1]);
        stream<(j & 1) * SL, -1, 0>(bq, la[j >> 1], mm, [&](auto kc) {
          constexpr int k = decltype(kc)::value;
          if constexpr (k == 8) fn = line_of(fl, cnt + 1);
          if constexpr (k == 9) adv(hs, HB);
          if constexpr (k == 10 && j == 4 - DD) dl = sopq((it == 15) ? 0u : dl);
        });
        check_flags<6>(fw, fr, pv, tag + cnt, dl);
        PTQ(2);
        fr = fn; ++cnt;
        gld4o<0>(fw, fr, pv);
        fetch(IC<(j + DD) & 3>{});
        PTQ(4);
        prio<0>();
        static_for<0, 3>([&](auto mc) { constexpr int m = decltype(mc)::value; lds_wr<(j & 1) * 12288 + m * 1024>(ea, acc[m]); });
        prio<PR_A>();
        if (wave == 1) { if constexpr (j == 3) STAMP_AT(it); }
        PTQ(0);
        if constexpr (DD == 2) { LWAIT(7); PTQ(2); }
        bar_raw();
        PTQ(3);
      });
    }
    PROF_OUT();
    wait_vm<0>(); tie(fw, fw, fw, fw);
    if (wave == 1) { STAMP_AT(20); STAMP_OUT(); }
  } else {
    Ring<12, PDB> bq;
    const char* pd = cx.ws + WS_P + (size_t)(cx.g * 18 + 6 * ns + nq) * 64 * PB + ks * 1024 - PB;
    const char* fp = fl + (F_P + (ns * 4 + nq) * 6 + ks) * FSTR;
    Cnt vt = flag_init(fp, l16, tag);
    const unsigned hv = vopq((g >> 1) * 256 + r * 16 + (4 + (nq >> 1) - nq) * 64 * PB + (nq & 1) * 512);
    bar_bare();
    prime<0>(bq, la[0]);
    wait_lg<0>();
    PROF_INIT();
#pragma nounroll
    for (int it = 0; it < 16; ++it) {
      static_for<0, 4>([&](auto jc) {
        constexpr int j = decltype(jc)::value, j1 = (j + 1) & 3;
        f32x4 e[3];
        stream<(j & 1) * SL, BARD, 63>(bq, la[j >> 1], mm, [&](auto kc) {
          constexpr int k = decltype(kc)::value;
          if constexpr (k == 8) adv(pd, PB);
          if constexpr (k == 11) static_for<0, 3>([&](auto mc) { constexpr int m = decltype(mc)::value; lds_rd<(j & 1) * 12288 + m * 1024>(e[m], ea); });
        });
        asm volatile("s_nop 7\n\ts_waitcnt lgkmcnt(0)" : "+v"(acc[0]), "+v"(acc[1]), "+v"(acc[2]), "+v"(e[0]), "+v"(e[1]), "+v"(e[2]) :: "memory");
#pragma unroll
        for (int m = 0; m < 3; ++m) e[m] += acc[m];
        u32x4 y0 = pack8(e[0], e[1]), y1 = widen(cvt2(e[2][0], e[2][1]), cvt2(e[2][2], e[2][3]));
        tie(y0, y1, y1, y1);
        PTQ(0);
        LWAIT0();
        PTQ(4);
        flag_up(fp, vt);
        gst16(pd, l16, y0);
        gst16h<0>(pd, hv, y1, HALF);
        prio<0>();
        prime<(j1 & 1) * SL>(bq, la[j1 >> 1]);
        if constexpr (j == 3) wait_lg<0>();
      });
    }
    PROF_OUT();
    wait_vm<0>();
    flag_up(fp, vt);
  }
}

__device__ __forceinline__ size_t x2_top(int g) { return WS_X2 + (size_t)((g & 1) * 64 + (1 - (g >> 1)) * 32) * TB; }

template <bool A> __device__ __forceinline__ void role_wo(char* smem, const u16* wo, const Ctx& cx, int jw) {
  const int wave = cx.wave, lane = cx.lane;
  const unsigned sm0 = lds_addr(smem);
  unsigned nonce;
  const int r = lane & 15, g = lane >> 4, q = r >> 2, p = lane & 3;
  constexpr int RS = 4 * TB, EX = RS + 4 * 9216;
  bf16x8 W[18][2], WX[9];
  {
    constexpr int BUF = 40960;
    const char* src = (const char*)wo + jw * 576;
    unsigned vo[5], vx[5];
#pragma unroll
    for (int i = 0; i < 5; ++i) {
      const unsigned ch = min((wave * 5 + i) * 64 + lane, 2303), row = ch / 36, cc = (ch - row * 36) * 16;
      const unsigned kl = row >> 5, gg = (row >> 3) & 3, e = row & 7;
      vo[i] = (36 * kl + 20 * (e >> 2) + 4 * gg + (e & 3)) * (DM * 2) + cc;
      vx[i] = (72 * (4 * kl + gg) + 16 + 36 * (e >> 2) + (e & 3)) * (DM * 2) + cc;
    }
    const char* rb = smem + (8 * g + q) * 576 + p * 16;
    nonce = load_w<9, 5>(smem, wave, cx.ws, [&](auto rc) {
      constexpr int rr = decltype(rc)::value;
#pragma unroll
      for (int i = 0; i < 5; ++i) dma1<false>(src + (rr < 8 ? rr * 72 * (DM * 2) : 0), rr < 8 ? vo[i] : vx[i], sm0 + (rr % 3) * BUF + (wave * 5 + i) * 1024);
    }, [&](auto rc) {
      constexpr int rr = decltype(rc)::value;
#pragma unroll
      for (int kl = 0; kl < 2; ++kl) {
        const char* a = rb + (rr % 3) * BUF + kl * 32 * 576;
#pragma unroll
        for (int m = 0; m < 2; ++m) { W[2 * rr + kl][m] = tr_frag(a + wave * 64 + m * 8, a + wave * 64 + m * 8 + 4 * 576); asm volatile("" : "+v"(W[2 * rr + kl][m])); }
        if constexpr (A) {
          const bf16x8 wx = tr_frag(a + 512 + (wave & 1) * 8, a + 512 + (wave & 1) * 8 + 4 * 576);
          const int k = 2 * rr + kl;
          if (k < 9) WX[k % 9] = wx; else if (wave & 2) WX[k % 9] = wx;
          asm volatile("" : "+v"(WX[k % 9]));
        }
      }
    });
  }
  const unsigned tag = make_tag(nonce);
  const char* fl = cx.ws + WS_FL + cx.g * FGRP;
  const unsigned l16 = vopq(lane * 16);
  const unsigned la[2] = {vopq(sm0 + l16), vopq(sm0 + l16 + 2 * TB)};
  const unsigned ra = vopq(sm0 + RS + wave * 1024 + l16), mr = sm0 + RS + wave * 1024;
  const char* xo = cx.ws + WS_X1 + (size_t)cx.g * 64 * TB + jw * 9216 + wave * 1024 - TB;
  const char* rs = cx.ws + x2_top(cx.g) + (WS_X0 - WS_X2) + (9 * jw + wave) * 1024;
  constexpr long RSW = TB - 32l * TB - (long)(WS_X0 - WS_X2);
  const unsigned rv = l16, rv8 = vopq(l16 + 3072);
  poll(cx.ws + WS_F2 + ((cx.g & 1) * 72 + (1 - (cx.g >> 1)) * 32) * F2ROW, F_X0 * 128 + (lane & 3) * F2ROW, tag, cx.dl);
  f32x4 a0, a1, ax;
  Ring<18> bq;
  auto init = [&](const u32x4& x) {
    a0 = f32x4{blo(x[0]), bhi(x[0]), blo(x[1]), bhi(x[1])}; a1 = f32x4{blo(x[2]), bhi(x[2]), blo(x[3]), bhi(x[3])};
    asm volatile("s_nop 1" : "+v"(a0), "+v"(a1));
  };
  dma1c<0>(rs, rv, mr); if (wave == 5) dma1c<3072>(rs, rv8, mr);
  adv(rs, TB);
  dma1c<9216>(rs, rv, mr); if (wave == 5) dma1c<9216 + 3072>(rs, rv8, mr);
  adv(rs, TB);
  wait_vm<0>();
  init(*(const u32x4*)(smem + RS + wave * 1024 + lane * 16));
  if (wave == 1) STAMP_AT(70);
  if constexpr (A) {
    const unsigned ea = vopq(sm0 + EX + wave * 1024 + l16);
    bar_bare();
    auto loop = [&](auto hc) {
      constexpr int KH = decltype(hc)::value;
      auto mm = [&](auto kc, auto jc, const bf16x8& x) {
        constexpr int k = decltype(kc)::value;
        if constexpr (decltype(jc)::value == 0) mf<false>(a0, W[k][0], x);
        else { mf<false>(a1, W[k][1], x); if constexpr (k / 9 == KH) mf<k % 9 == 0>(ax, WX[k % 9], x); }
      };
      PROF_INIT();
#pragma nounroll
      for (int it = 0; it < 16; ++it) {
        static_for<0, 4>([&](auto jc) {
          constexpr int j = decltype(jc)::value;
          u32x4 x;
          prime<(j & 1) * TB>(bq, la[j >> 1]);
          stream<(j & 1) * TB, -1, 0>(bq, la[j >> 1], mm, [&](auto kc) {
            constexpr int k = decltype(kc)::value;
            if constexpr (k == 13) adv(xo, TB);
            if constexpr (k == 16) { LWAIT0(); lds_rd<((j + 1) & 3) * 9216>(x, ra); }
          });
          dma1c<((j + 2) & 3) * 9216>(rs, rv, mr);
          asm volatile("s_nop 3" : "+v"(a0), "+v"(a1), "+v"(ax) :: "memory");
          gst16(xo, l16, pack8(a0, a1));
          asm volatile("s_waitcnt lgkmcnt(0)" : "+v"(x) :: "memory");
          init(x);
          prio<0>();
          lds_wr<(j & 1) * 4096>(ea, ax);
          prio<PR_A>();
          adv(rs, (j == 1 && it == 7) ? RSW : TB);
          if (wave == 1) { if constexpr (j == 3) STAMP_AT(it); }
          PTQ(0);
          bar_raw();
          PTQ(3);
        });
      }
      PROF_OUT();
      wait_vm<0>();
      bar_bare();
      if (wave == 1) { STAMP_AT(20); STAMP_OUT(); }
    };
    if (wave < 2) loop(IC<0>{}); else loop(IC<1>{});
  } else {
    const int b = (wave == 4) ? 0 : wave - 5;
    const char* os = cx.ws + WS_O + (size_t)(cx.g * 68 + 3) * TB + b * 6144;
    const unsigned l3 = vopq(l16 + 3072), mb = sm0 + b * 6144, ea = vopq(sm0 + EX + l16), pv = vopq((F_O + min(lane, 23)) * FSTR);
    auto mm = [&](auto kc, auto jc, const bf16x8& x) {
      constexpr int k = decltype(kc)::value;
      if constexpr (decltype(jc)::value == 0) mf<false>(a0, W[k][0], x); else mf<false>(a1, W[k][1], x);
    };
    auto loop = [&](auto pc) {
      constexpr bool SH = decltype(pc)::value;
      auto fetch = [&](auto sc) { constexpr int sl = decltype(sc)::value; dma3<sl * TB>(os, l16, l16, l16, mb); dma3<sl * TB + 3072>(os, l3, l3, l3, mb); };
      auto resid = [&](auto sc) { constexpr int sl = decltype(sc)::value; dma1c<sl * 9216>(rs, rv, mr); if constexpr (SH) dma1c<sl * 9216 + 3072>(rs, rv8, mr); };
      const char* fp = fl + (F_X1 + 4 * jw + wave - 4) * FSTR;
      Cnt vt = flag_init(fp, l16, tag);
      if constexpr (!SH) { poll(fl + OLAG * 128, pv, tag + OLAG, cx.dl); fetch(IC<0>{}); poll(fl + (OLAG + 1) * 128, pv, tag + OLAG + 1, cx.dl); adv(os, TB); fetch(IC<1>{}); }
      bar_bare();
      unsigned fw, cnt = OLAG + 3, cn;
      const char *fr = line_of(fl, cnt), *fn;
      if constexpr (!SH) { poll(fl + (OLAG + 2) * 128, pv, tag + OLAG + 2, cx.dl); gld4o<0>(fw, fr, pv); wait_vm<0>(); tie(fw, fw, fw, fw); adv(os, TB); fetch(IC<2>{}); }
      resid(IC<2>{});
      prime<0>(bq, la[0]);
      wait_lg<0>();
      unsigned dl = cx.dl;
      PROF_INIT();
#pragma nounroll
      for (int it = 0; it < 16; ++it) {
        static_for<0, 4>([&](auto jc) {
          constexpr int j = decltype(jc)::value, j1 = (j + 1) & 3;
          u32x4 x, x8; f32x4 e[4];
          stream<(j & 1) * TB, BARG, VMBX(SH ? 5 : 10)>(bq, la[j >> 1], mm, [&](auto kc) {
            constexpr int k = decltype(kc)::value;
            if constexpr (!SH && k == 10 && j == 1) dl = sopq((it == 15) ? 0u : dl);
            if constexpr (!SH && k == 9) { cn = cnt + ((j == 0 && it == 7) ? 2 : 1); fn = line_of(fl, cn); }
            if constexpr (k == 11) adv(rs, (j == 1 && it == 7) ? RSW : TB);
            if constexpr (k == 12) adv(xo, TB);
            if constexpr (!SH && k == 13) adv(os, (j == 1 && it == 7) ? 2 * TB : TB);
          });
          prio<0>();
          if constexpr (SH) {
            static_for<0, 4>([&](auto mc) { constexpr int m = decltype(mc)::value; lds_rd<(j & 1) * 4096 + m * 1024>(e[m], ea); });
            lds_rd<j * 9216 + 3072>(x8, ra);
          }
          lds_rd<j1 * 9216>(x, ra);
          prio<3>();
          asm volatile("s_nop 4" : "+v"(a0), "+v"(a1) :: "memory");
          u32x4 y = pack8(a0, a1);
          asm volatile("s_waitcnt lgkmcnt(0)" : "+v"(x), "+v"(y) :: "memory");
          if constexpr (SH) {
            tie(e[0], e[1], e[2], e[3]); tie(x8, x8, x8, x8);
            x8 = pack8(e[0] + e[2] + f32x4{blo(x8[0]), bhi(x8[0]), blo(x8[1]), bhi(x8[1])}, e[1] + e[3] + f32x4{blo(x8[2]), bhi(x8[2]), blo(x8[3]), bhi(x8[3])});
            tie(x8, x8, x8, x8);
          }
          PTQ(0);
          if constexpr (SH) { LWAIT(2); } else check_flags<7>(fw, fr, pv, tag + cnt, dl);
          PTQ(4);
          flag_up(fp, vt);
          gst16(xo, l16, y);
          if constexpr (SH) gst16(xo + 3072, l16, x8);
          if constexpr (!SH) { fr = fn; cnt = cn; gld4o<0>(fw, fr, pv); fetch(IC<(j + 3) & 3>{}); }
          resid(IC<(j + 3) & 3>{});
          init(x);
          prio<0>();
          prime<(j1 & 1) * TB>(bq, la[j1 >> 1]);
          if constexpr (j == 3) wait_lg<0>();
        });
      }
      PROF_OUT();
      wait_vm<0>();
      if constexpr (!SH) tie(fw, fw, fw, fw);
      bar_bare();
      flag_up(fp, vt);
    };
    if (wave == 5) loop(std::true_type{}); else loop(std::false_type{});
  }
}

template <class F> __device__ __forceinline__ void qa_periods(F&& f) {
#pragma nounroll
  for (int it = 0; it < 16; ++it) static_for<0, 4>([&](auto jc) { f(jc, IC<0>{}, it); });
  static_for<0, 4>([&](auto jc) { f(jc, IC<1>{}, 16); });
}
__device__ __forceinline__ void role_qa(char* smem, const u16* wq, const Ctx& cx, int jq) {
  const int wave = cx.wave, lane = cx.lane;
  const unsigned sm0 = lds_addr(smem);
  unsigned nonce;
  const int r = lane & 15, g = lane >> 4, q = r >> 2, p = lane & 3;
  constexpr int XR = 4 * TB, PP = XR + 2048, ST = PP + 8192;
  bf16x8 W[18][2];
  {
    constexpr int BUF = 49152;
    const char* src = (const char*)wq + jq * 144;
    unsigned vo[6];
#pragma unroll
    for (int i = 0; i < 6; ++i) {
      const unsigned ch = min((wave * 6 + i) * 64 + lane, 2591), row = ch / 27, cc = ch - row * 27, sec = cc / 9;
      vo[i] = row * (NQKV * 2) + sec * (DM * 2 - 144) + cc * 16;
    }
    int c0, c1;
    if (wave >= 4) { c0 = 36 * (wave & 3) + (wave & 1) * 4 + p * 8; c1 = c0 + 144; }
    else if (wave < 2) { c0 = 288 + 72 * wave + p * 8; c1 = c0 + 40; }
    else { c0 = (p >> 1) * 144 + 32 + (p & 1) * 72; c1 = 288 + 32 + (p & 1) * 72; }
    const char* rb = smem + (8 * g + q) * 432;
    nonce = load_w<6, 6>(smem, wave, cx.ws, [&](auto rc) {
      constexpr int rr = decltype(rc)::value;
#pragma unroll
      for (int i = 0; i < 6; ++i) dma1<false>(src + rr * 96 * (NQKV * 2), vo[i], sm0 + (rr % 3) * BUF + (wave * 6 + i) * 1024);
    }, [&](auto rc) {
      constexpr int rr = decltype(rc)::value;
#pragma unroll
      for (int kl = 0; kl < 3; ++kl)
#pragma unroll
        for (int m = 0; m < 2; ++m) {
          const char* a = rb + (rr % 3) * BUF + kl * 32 * 432 + (m ? c1 : c0);
          W[3 * rr + kl][m] = tr_frag(a, a + 4 * 432);
          asm volatile("" : "+v"(W[3 * rr + kl][m]));
        }
    });
  }
  const f32x4 z4 = {0.f, 0.f, 0.f, 0.f};
  const unsigned l16 = vopq(lane * 16);
  const unsigned la[2] = {vopq(sm0 + l16), vopq(sm0 + l16 + 2 * TB)};
  const bool low = cx.g < 2;
  Ring<18> bq;
  if (wave < 4) {
    const unsigned tag = make_tag(nonce);
    const char* fl = cx.ws + WS_FL + cx.g * FGRP;
    const char* xs = cx.ws + x2_top(cx.g) + (WS_X0 - WS_X2) + wave * 4096;
    const char* fr = cx.ws + WS_F2 + ((cx.g & 1) * 72 + (low ? 32 : 0)) * F2ROW;
    const unsigned mb = sm0 + wave * 4096, l4 = vopq(l16 + 4096);
    unsigned pv = vopq(F_X0 * 128), dl = cx.dl, fw;
    auto fetch = [&](auto sc, auto nc) {
      dma4<decltype(sc)::value * TB, 4>(xs, l16, mb);
      if constexpr (decltype(nc)::value == 6) dma4<decltype(sc)::value * TB + 4096, 2>(xs, l4, mb);
    };
    auto start = [&](auto nc) {
      poll(fr, pv + (lane & 3) * F2ROW, tag, cx.dl);
      fw = vopq(tag); adv(fr, DD * F2ROW);
      static_for<0, DD>([&](auto ic) { fetch(ic, nc); adv(xs, TB); });
      wait_vm<decltype(nc)::value>();
    };
    auto feed = [&](auto jc, auto nc, int it) {
      constexpr int j = decltype(jc)::value, n1 = (j + DD + 1) & 3;
      const int nx = 4 * it + j + DD + 1;
      const char* xn = xs + TB;
      adv(fr, F2ROW);
      if constexpr (n1 == 0) { const bool st = low && nx == 32; dl = st ? 0u : dl; xn = st ? xs : xn; }
      if constexpr (n1 == 1) {
        const bool st = low && nx == 65; dl = st ? 0u : cx.dl; xn = st ? xs : xn;
        if (nx == 33) { xn = cx.ws + x2_top(cx.g) + wave * 4096; fr -= 33 * F2ROW; pv = vopq(min(lane, 17) * 128); }
      }
      if constexpr (n1 == 2) dl = (nx == 66) ? 0u : dl;
      gld4o<0>(fw, fr, pv);
      fetch(IC<(j + DD) & 3>{}, nc);
      xs = xn; adv(xs, 0);
    };
    if (wave == 3) {
      start(IC<6>{});
      bar_raw();
      STAMP_AT(70);
      bf16x8 one = ones8();
      asm volatile("" : "+v"(one));
      f32x4 a0, a1;
      const unsigned sa = vopq(sm0 + ST + ((g == q) ? r * 8 : 256));
      auto mm = [&](auto kc, auto jc, const bf16x8& x) {
        constexpr int k = decltype(kc)::value;
        if constexpr (decltype(jc)::value == 0) mf<k == 0>(a0, one, x); else mf<k == 0>(a1, x, x);
      };
      PROF_INIT();
      qa_periods([&](auto jc, auto ec, int it) {
        constexpr int j = decltype(jc)::value; constexpr bool END = decltype(ec)::value;
        if constexpr (!END || j < 2) {
          prime<(j & 1) * TB>(bq, la[j >> 1]);
          stream<(j & 1) * TB, -1, 0>(bq, la[j >> 1], mm, [&](auto kc) {
              constexpr int k = decltype(kc)::value;
              if constexpr (k == 0) TR(1, 1);
              if constexpr (k == 6) TR(1, 2);
              if constexpr (k == 12) TR(1, 3);
              if constexpr (k == 17) TR(1, 4);
          });
          prio<PR_QA>();
          if constexpr (!END) {
            check_flags<6>(fw, fr, pv, tag, dl);
            PTQ(2);
            feed(jc, IC<6>{}, it);
          } else { wait_vm<0>(); tie(fw, fw, fw, fw); settle(); }
          asm volatile("" : "+v"(a0), "+v"(a1));
          const f32x2 st = row_stat(a0[0], diag(a1, p));
          asm volatile("ds_write_b64 %0, %1 offset:%2" :: "v"(sa), "v"(st), "n"((j & 1) * 128) : "memory");
          prio<PR_A>();
          if constexpr (!END && DD == 2) LWAIT(7);
        }
        { if constexpr (j == 3) STAMP_AT(it); }
        PTQ(0);
        TR(1, 6);
        bar_raw();
        TR(0, 0); TR(1, 7);
        PTQ(3);
      });
      TR_OUT();
      PROF_OUT();
      STAMP_OUT();
      if (cx.g == 0 && jq == 0) {
        const unsigned w = (lane < 36 ? F_H + 4 * lane : lane < 54 ? F_P + ((lane - 36) / 6) * 24 + (lane - 36) % 6 : lane < 62 ? F_O + 3 * (lane - 54) : F_X1 + 4 * (lane - 62)) * FSTR;
        for (int g2 = 0; g2 < 4; ++g2) poll(cx.ws + WS_FL + g2 * FGRP, w, tag, cx.dl);
        gst4m<1>(cx.ws, vopq(0), vopq(nonce + 1u));
      }
    } else {
      const char* fp = fl + (F_O + 3 * jq + wave) * FSTR;
      Cnt vt = flag_init(fp, l16, tag);
      start(IC<4>{});
      const unsigned sa = vopq(sm0 + ST + g * 32), xa = vopq(sm0 + XR + l16);
      auto loop = [&](auto xc) {
        constexpr bool X = decltype(xc)::value;
        f32x4 c0 = z4, c1 = z4;
#pragma unroll
        for (int k = 0; k < 18; ++k) { c0 = X ? MFMA(W[k][0], ones8(), c0) : MFMA(ones8(), W[k][0], c0); c1 = MFMA(ones8(), W[k][1], c1); }
        if constexpr (X) *(f32x4*)(smem + XR + 1024 + lane * 16) = c0;
        const float cs[2] = {c0[0], c1[0]};
        bar_raw();
        f32x4 a[2][2] = {{z4, z4}, {z4, z4}};
        u32x4 vw[2] = {{0u, 0u, 0u, 0u}, {0u, 0u, 0u, 0u}};
        const unsigned pa = vopq(sm0 + PP + (X ? 0 : wave * 2048) + l16);
        const char* ob = cx.ws + WS_O + (size_t)cx.g * 68 * TB + (X ? 16384 + jq * 256 : (2 * jq + wave) * 1024) - TB;
        PROF_INIT();
      qa_periods([&](auto jc, auto ec, int it) {
          constexpr int j = decltype(jc)::value, e = j & 1, o = 1 - e; constexpr bool END = decltype(ec)::value;
          auto mm = [&](auto kc, auto ic, const bf16x8& x) {
            constexpr int k = decltype(kc)::value;
            if constexpr (decltype(ic)::value == 0) { if constexpr (X) mf<k == 0>(a[e][0], W[k][0], x); else mf<k == 0>(a[e][0], x, W[k][0]); }
            else mf<k == 0>(a[e][1], x, W[k][1]);
          };
          bf16x8 pf[4]; f32x4 s0, s1;
          auto reads = [&](auto ic) {
            constexpr int i = decltype(ic)::value;
            if constexpr (i < (X ? 2 : 1)) { lds_rd<o * 4096 + i * 2048>(pf[2 * i], pa); lds_rd<o * 4096 + i * 2048 + 1024>(pf[2 * i + 1], pa); }
            if constexpr (i == 2) { lds_rd<o * 128>(s0, sa); lds_rd<o * 128 + 16>(s1, sa); }
          };
          if constexpr (!END || j < 2) {
            prime<e * TB>(bq, la[j >> 1]);
            stream<e * TB, -1, 0>(bq, la[j >> 1], mm, [&](auto kc) {
              constexpr int k = decltype(kc)::value;
              if constexpr (k == 0) TR(1, 1);
              if constexpr (k == 6) TR(1, 2);
              if constexpr (k == 12) TR(1, 3);
              if constexpr (k == 17) TR(1, 4);
              if constexpr (k == 13) adv(ob, TB);
              if constexpr (k >= 14 && k < 17) reads(IC<k - 14>{});
            });
          } else { adv(ob, TB); static_for<0, 3>([&](auto ic) { reads(ic); }); }
          asm volatile("s_waitcnt lgkmcnt(0)" : "+v"(s0), "+v"(s1) :: "memory");
          f32x4 d[4];
          static_for<0, X ? 4 : 2>([&](auto hc) { constexpr int h = X ? 3 - decltype(hc)::value : decltype(hc)::value; mf<true>(d[h], asb(vw[X ? 1 : h]), pf[h]); });
          prio<PR_QA>();
          PTQ(0);
          if constexpr (!END) check_flags<4>(fw, fr, pv, tag, dl); else { wait_vm<0>(); tie(fw, fw, fw, fw); }
          PTQ(2);
          flag_up(fp, vt);
          if constexpr (X) {
            if constexpr (!END || j < 2) lds_wr<e * 1024>(xa, a[e][0]);
            tie(d[0], d[1], d[2], d[3]);
            const unsigned e2 = cvt2(d[2][0], d[2][1]), e3 = cvt2(d[3][2], d[3][3]);
            const auto s2 = __builtin_amdgcn_permlane16_swap(e2, e2, false, false);
            const auto s3 = __builtin_amdgcn_permlane16_swap(e3, e3, false, false);
            gst16h<0>(ob, l16, u32x4{cvt2(d[0][0], d[0][1]), cvt2(d[1][2], d[1][3]), s2[1], s3[1]}, 0xFFFFull);
          } else { tie(d[0], d[1], d[1], d[1]); gst16(ob, l16, pack8(d[0], d[1])); }
          TR(1, 5);
          if constexpr (!END) feed(jc, IC<4>{}, it);
#pragma unroll
          for (int m = X ? 1 : 0; m < 2; ++m) {
            const f32x4 w = a[o][m];
            vw[m][2 * o] = cvt2(fmaf(cs[m], s0[0], w[0]) * s0[1], fmaf(cs[m], s0[2], w[1]) * s0[3]);
            vw[m][2 * o + 1] = cvt2(fmaf(cs[m], s1[0], w[2]) * s1[1], fmaf(cs[m], s1[2], w[3]) * s1[3]);
            asm volatile("" : "+v"(vw[m]));
          }
          prio<PR_A>();
          PTQ(4);
          if constexpr (!END && DD == 2) { LWAIT(7); PTQ(2); }
          TR(1, 6);
          bar_raw();
          TR(0, 0); TR(1, 7);
          PTQ(3);
        });
        TR_OUT();
        PROF_OUT();
        wait_vm<0>();
        flag_up(fp, vt);
      };
      if (wave == 2) loop(std::true_type{}); else loop(std::false_type{});
    }
  } else {
    const int h = wave - 4;
    f32x4 cq = z4, ck = z4;
#pragma unroll
    for (int k = 0; k < 18; ++k) { cq = MFMA(W[k][0], ones8(), cq); ck = MFMA(W[k][1], ones8(), ck); }
    const float ninf = -__builtin_inff();
    f32x4 bo, bp;
#pragma unroll
    for (int e = 0; e < 4; ++e) { const int m = 4 * g + e; bo[e] = (m <= r && m >= r - 7) ? 0.f : ninf; bp[e] = (m >= r + 9) ? 0.f : ninf; }
    Soft f = {};
#pragma unroll
    for (int e = 0; e < 2; ++e) {
      f.mo[0][e] = vopq((4 * g + 2 * e <= r ? 0xFFFFu : 0u) | (4 * g + 2 * e + 1 <= r ? 0xFFFF0000u : 0u)); f.mo[1][e] = vopq(~f.mo[0][e]);
    }
    const unsigned xa = vopq(sm0 + XR + (h >> 1) * 256 + r * 16 + (h & 1) * 8), sa = vopq(sm0 + ST + r * 8), pa = vopq(sm0 + PP + h * 1024 + l16);
    const float g0 = (g == 0) ? 1.f : 0.f;
    bar_raw();
    f32x2 cxq, cxk;
    lds_rd8<1024>(cxq, xa); lds_rd8<1536>(cxk, xa);
    bf16x8 x[18];
    auto first = [&](auto oc, unsigned a) { static_for<0, 6>([&](auto kc) { lds_rd<decltype(oc)::value + decltype(kc)::value * 1024>(x[decltype(kc)::value], a); }); };
    first(IC<0>{}, la[0]);
    asm volatile("s_waitcnt lgkmcnt(0)" : "+v"(cxq), "+v"(cxk) :: "memory");
    f32x4 a0 = z4, a1 = z4, so = z4;
    u32x4 qf = {0u, 0u, 0u, 0u}, kf = qf;
    float sc = 0.f, scn = 0.f;
    asm volatile("" : "+v"(qf), "+v"(kf), "+v"(bo), "+v"(bp));
    PROF_INIT();
    qa_periods([&](auto jc, auto ec, int it) {
      constexpr int j = decltype(jc)::value, e = j & 1, j1 = (j + 1) & 3; constexpr bool END = decltype(ec)::value;
      auto put = [&]() { lds_wr<e * 4096>(pa, u32x4{f.own[0], f.own[1], f.oth[0], f.oth[1]}); };
      if constexpr (END && j >= 2) {
        if constexpr (j == 2) { soft3(f); put(); }
        bar_raw();
      } else {
        PTQ(0);
        prio<PR_B>();
        static_for<0, 6>([&](auto pc) {
          constexpr int k = 2 * decltype(pc)::value;
          __builtin_amdgcn_s_waitcnt(0xC07F | (4 << 8));
          if constexpr (k == PUTK) put();
          qk_pair<k / 2, e, e * TB + (k + 6) * 1024>(a0, a1, W[k], W[k + 1], x[k], x[k + 1], x[k + 6], x[k + 7], la[j >> 1], f);
          if constexpr (k == 0) TR(1, 7);
          if constexpr (k == 4) TR(1, 8);
        });
        TR(1, 9);
        if constexpr (PUTK == 12) put();
        static_for<12, 14>([&](auto kc) {
          constexpr int k = decltype(kc)::value;
          __builtin_amdgcn_s_waitcnt(0xC07F | ((PUTK == 12 ? 5 : 4) << 8));
          mf<false>(a0, W[k][0], x[k]); mf<false>(a1, W[k][1], x[k]);
        });
        TR(1, 10);
        PTQ(1);
        asm volatile("s_setprio 3\n\ts_waitcnt lgkmcnt(0)\n\ts_barrier" ::: "memory");
        PTQ(3);
        TR(0, 0); TR(1, 11);
        f32x2 st, xq, xk;
        lds_rd8<e * 128>(st, sa); lds_rd8<e * 1024 + 512>(xk, xa); lds_rd8<e * 1024>(xq, xa);
        mf8(a0, a1, W[14], W[15], W[16], W[17], x[14], x[15], x[16], x[17]);
        asm volatile("s_waitcnt lgkmcnt(0)" : "+v"(st), "+v"(xq), "+v"(xk) :: "memory");
        TR(0, 1);
        const float ix = st[1] * g0;
        kf[2] = cvt2(fmaf(cxk[0], st[0], xk[0]) * ix, fmaf(cxk[1], st[0], xk[1]) * ix);
        sc = scn; scn = st[1] * (0.23570226039551584f * 1.4426950408889634f);
        asm volatile("" : "+v"(kf), "+v"(scn), "+v"(a0));
        TR(0, 2);
        asm volatile("" : "+v"(a1));
        kf[0] = cvt2(fmaf(ck[0], st[0], a1[0]) * st[1], fmaf(ck[1], st[0], a1[1]) * st[1]);
        kf[1] = cvt2(fmaf(ck[2], st[0], a1[2]) * st[1], fmaf(ck[3], st[0], a1[3]) * st[1]);
        f32x4 sp;
        if constexpr ((!END && j == 0) || (END && j == 1)) {
          const float off = (low && (END || it == 8)) ? ninf : 0.f;
          sp = MFMA(asb(kf), asb(qf), bp + off);
        } else sp = MFMA(asb(kf), asb(qf), bp);
        __builtin_amdgcn_sched_barrier(0);
        TR(0, 3);
        qf[0] = cvt2(fmaf(cq[0], st[0], a0[0]), fmaf(cq[1], st[0], a0[1]));
        qf[1] = cvt2(fmaf(cq[2], st[0], a0[2]), fmaf(cq[3], st[0], a0[3]));
        qf[2] = cvt2(fmaf(cxq[0], st[0], xq[0]), fmaf(cxq[1], st[0], xq[1]));
        asm volatile("" : "+v"(qf), "+v"(sp));
        max4(f.s, so, sp);
        mfc(so, asb(kf), asb(qf), bo);
        soft1(f.s[0], f.s[1], f.s[2], f.s[3], f.t0, f.t1, sc);
        TR(0, 4);
        if constexpr (!(END && j == 1)) first(IC<(j1 & 1) * TB>{}, la[j1 >> 1]);
        TR(0, 5);
        soft2(f.s[0], f.s[1], f.s[2], f.s[3], f.t0, f.t1);
        TR(0, 6);
        if constexpr (j == 3) wait_lg<0>();
      }
    });
    TR_OUT();
    PROF_OUT();
  }
}

__global__ __launch_bounds__(512, 2) void k_main(const u16* x0, const u16* wqkv, const u16* wo,
                                                 const u16* wg, const u16* wu, const u16* wd,
                                                 char* ws, u16* out) {
  __shared__ __attribute__((aligned(1024))) char smem[SMEM_BYTES];
  Ctx cx;
  cx.ws = ws;
  cx.wave = __builtin_amdgcn_readfirstlane(threadIdx.x >> 6);
  cx.lane = threadIdx.x & 63;
  cx.dl = ((unsigned)__builtin_amdgcn_s_memtime() + SPIN) | 1u;
  const unsigned t0 = (unsigned)__builtin_amdgcn_s_memrealtime();
  const int bid = blockIdx.x, xh = bid & 1, i = bid >> 3;
#define ROT 1
  cx.g = ((bid >> 1) + ROT * (i < 4 ? 2 : (i == 4 || i >= 23) ? 1 : 0)) & 3;
  {
    const unsigned dm = lds_addr(smem) + 162816;
    const int lane = cx.lane, wave = cx.wave;
    if (i >= 5) {
      const int ln = min(((i - 5) * 8 + wave) * 64 + lane, 11519), sg = ln / 5;
      const char* bq = (const char*)wqkv; const char* bw = (const char*)wo - 1728 * 1152;
      touch((sg < 1728 ? bq : bw) + (size_t)sg * 1152 + (xh * 4 + ln - sg * 5) * 128, dm);
    }
    if (wave < 2) touch(ws + min((size_t)(wave * 64 + lane) << 20, WS_END - 4), dm);
    else if (wave == 2) touch((const char*)out + min(lane << 20, 2359292), dm);
    else if (wave == 4) {
      unsigned long long pc; unsigned len;
      asm volatile("s_getpc_b64 %0\n.Lkbeg:\n\ts_mov_b32 %1, .Lkend-.Lkbeg" : "=s"(pc), "=s"(len));
      touch((const char*)pc + min((unsigned)((i * 64 + lane) * 128), len), dm);
    }
    if (i >= 24) {
      const int n = ((wave >> 2) * 8 + i - 24) * 2 + xh, idx = (1 - (cx.g >> 1)) * 32 + n, w4 = wave & 3;
      const char* src = (const char*)x0 + (size_t)((cx.g >> 1) * 1024 + 32 * (31 - n) + (cx.g & 1)) * (DM * 2);
      const char* dst = ws + WS_X0 + (size_t)((cx.g & 1) * 64 + idx) * TB;
      u32x4 v[5];
#pragma unroll
      for (int k = 0; k < 5; ++k) gld16(v[k], src + ((k < 4) ? 4 * w4 + k : 16 + (w4 & 1)) * 64, (lane & 15) * 2304 + (lane >> 4) * 16);
      unsigned nonce;
      asm volatile("s_load_dword %0, %1, 0x0\n\ts_waitcnt vmcnt(0) lgkmcnt(0)" : "=s"(nonce) : "s"(ws) : "memory");
      tie(v[0], v[1], v[2], v[3]); tie(v[4], v[4], v[4], v[4]);
#pragma unroll
      for (int k = 0; k < 5; ++k) if (k < 4 || w4 < 2) gst16(dst + ((k < 4) ? 4 * w4 + k : 16 + w4) * 1024, lane * 16, v[k]);
      wait_vm<0>();
      bar_bare();
      if (w4 == 0) gst4m<-1>(ws + WS_F2 + ((cx.g & 1) * 72 + idx) * F2ROW + F_X0 * 128, lane * 4, vopq(make_tag(nonce)));
    }
  }
  if (i >= 5) { const unsigned dly = (i < 23) ? DLY_G : DLY_D; while ((unsigned)__builtin_amdgcn_s_memrealtime() - t0 < dly) __builtin_amdgcn_s_sleep(4); }
  if (i < 4) role_qa(smem, wqkv, cx, xh * 4 + i);
  else if (i == 4) { if (cx.wave < 4) role_wo<true>(smem, wo, cx, xh); else role_wo<false>(smem, wo, cx, xh); }
  else if (i < 23) role_gu(smem, wg, wu, out, cx, xh * 18 + i - 5);
  else role_dn(smem, wd, cx, xh * 3 + (i - 23) / 3, (i - 23) % 3);
  asm volatile(".Lkend:");
}
#if defined(__HIP_DEVICE_COMPILE__)
#pragma clang attribute pop
#endif

extern "C" void kernel_launch(void* const* d_in, const int* in_sizes, int n_in,
                              void* d_out, int out_size, void* d_ws, size_t ws_size,
                              hipStream_t stream) {
  k_main<<<256, 512, 0, stream>>>((const u16*)d_in[0], (const u16*)d_in[1], (const u16*)d_in[2], (const u16*)d_in[4],
                                  (const u16*)d_in[5], (const u16*)d_in[6], (char*)d_ws, (u16*)d_out);
}
```

```cpp
#include <hip/hip_runtime.h>
#include <stdint.h>
#include <type_traits>

#if defined(__HIP_DEVICE_COMPILE__)
#pragma clang attribute push(__attribute__((target("no-packed-fp32-ops"))), apply_to = function)
#endif
typedef __attribute__((ext_vector_type(8))) short bf16x8;
typedef __attribute__((ext_vector_type(4))) short bf16x4;
typedef __attribute__((ext_vector_type(4))) float f32x4;
typedef __attribute__((ext_vector_type(2))) float f32x2;
typedef __attribute__((ext_vector_type(4))) unsigned u32x4;
typedef __attribute__((ext_vector_type(2))) unsigned u32x2;
typedef __bf16 bf2v __attribute__((ext_vector_type(2)));
typedef unsigned short u16;

#define MFMA(b, a, c) __builtin_amdgcn_mfma_f32_16x16x32_bf16(b, a, c, 0, 0, 0)
constexpr int DM = 576, FD = 4608, NQKV = 3 * DM;
constexpr int TB = 18432;
constexpr int HB = 24576;
constexpr int PB = 6144;
constexpr int FSTR = 72 * 128, FGRP = 80 * FSTR, F2ROW = 19 * 128;
constexpr size_t WS_FL = 4096;
constexpr size_t WS_IS = WS_FL + 4 * FGRP;
constexpr size_t WS_F2 = WS_IS + 4 * 64 * 128;
constexpr size_t WS_O = WS_F2 + 2 * 72 * F2ROW;
constexpr size_t WS_X1 = WS_O + (size_t)4 * 68 * TB;
constexpr size_t WS_X2 = WS_X1 + (size_t)256 * TB;
constexpr size_t WS_X0 = WS_X2 + (size_t)128 * TB;
constexpr size_t WS_P = WS_X0 + (size_t)128 * TB;
constexpr size_t WS_H = WS_P + (size_t)72 * 64 * PB;
constexpr size_t WS_END = WS_H + (size_t)(24 * 64 + 4) * HB;
static_assert(WS_O % 1024 == 0 && WS_END <= ((size_t)256 << 20), "workspace");
constexpr int F_H = 0, F_P = 36, F_X1 = 54, F_O = 56, F_X0 = 18;
constexpr int TLAG = 2, PLAG = 3, OLAG = 4;
constexpr int SMEM_BYTES = 163840;
#define GRP 2
#define GRD 2
#define GRB 12
#define GRW 1
#define GRQ 1
#define PDB 12
#define PDD 5
constexpr int PDG = 6;
#define BARU 14
#define BARD 10
#define FKG 14
#define FKD 9
constexpr int BARG = 14;
constexpr int PR_A = 2, PR_B = 1;
#define DLY_G 200
#define DLY_D 700
#define SLP "s_sleep 2\n\t"
#define EXP 3
#define FK -1
#define FKW -1
#define HLAG 2
#define EK 10
#define LAGF 9

__device__ __forceinline__ float u2f(unsigned u) { return __builtin_bit_cast(float, u); }
__device__ __forceinline__ int f2i(float f) { return __builtin_bit_cast(int, f); }
__device__ __forceinline__ float rbf(float f) { __bf16 b = (__bf16)f; return u2f((unsigned)__builtin_bit_cast(u16, b) << 16); }
__device__ __forceinline__ unsigned cvt2(float a, float b) { return __builtin_bit_cast(unsigned, __builtin_convertvector(f32x2{a, b}, bf2v)); }
__device__ __forceinline__ float blo(unsigned w) { return u2f(w << 16); }
__device__ __forceinline__ float bhi(unsigned w) { return u2f(w & 0xffff0000u); }
typedef __attribute__((address_space(3))) bf16x4* lds_b4p;
__device__ __forceinline__ bf16x4 tr_read(const char* p) {
  return __builtin_amdgcn_ds_read_tr16_b64_v4i16((lds_b4p)(__attribute__((address_space(3))) void*)(p));
}
__device__ __forceinline__ bf16x8 tr_frag(const char* lo, const char* hi) {
  return __builtin_shufflevector(tr_read(lo), tr_read(hi), 0, 1, 2, 3, 4, 5, 6, 7);
}
__device__ __forceinline__ unsigned lds_addr(const void* p) { return (unsigned)(size_t)(__attribute__((address_space(3))) const char*)p; }
template <int I, int N, class F> __device__ __forceinline__ void static_for(F&& f) {
  if constexpr (I < N) { f(std::integral_constant<int, I>{}); static_for<I + 1, N>(f); }
}
template <int N> using IC = std::integral_constant<int, N>;
constexpr int imin(int a, int b) { return a < b ? a : b; }
template <int N> __device__ __forceinline__ void wait_vm() { asm volatile("s_waitcnt vmcnt(%0)" ::"n"(N) : "memory"); }
template <int N> __device__ __forceinline__ void wait_lg() { asm volatile("s_waitcnt lgkmcnt(%0)" ::"n"(N) : "memory"); }
template <int N> __device__ __forceinline__ void wait_frag() { __builtin_amdgcn_s_waitcnt(0xC07F | (N << 8)); }
__device__ __forceinline__ void bar_raw() { asm volatile("s_waitcnt lgkmcnt(0)\n\ts_barrier" ::: "memory"); }
__device__ __forceinline__ void bar_bare() { asm volatile("s_barrier" ::: "memory"); }
template <int P> __device__ __forceinline__ void prio() { asm volatile("s_setprio %0" :: "n"(P)); }
__device__ __forceinline__ void settle() { asm volatile("s_nop 7" ::: "memory"); }
__device__ __forceinline__ int sopq(int v) { asm volatile("" : "+s"(v)); return v; }
__device__ __forceinline__ unsigned vopq(unsigned v) { asm volatile("" : "+v"(v)); return v; }
template <class A> __device__ __forceinline__ void tie(A& a) { asm volatile("" : "+v"(a)); }
template <class A, class... R> __device__ __forceinline__ void tie(A& a, R&... r) { tie(a); tie(r...); }
__device__ __forceinline__ void adv(const char*& p, long d) { p += d; asm volatile("" : "+s"(p)); }

__device__ __forceinline__ void dma_w(const void* s, unsigned v, unsigned m) {
  asm volatile("s_mov_b32 m0, %2\n\ts_nop 0\n\tglobal_load_lds_dwordx4 %1, %0" :: "s"(s), "v"(v), "s"(m) : "memory");
}
__device__ __forceinline__ void dma_wh(const void* s, unsigned v, unsigned m) {
  asm volatile("s_mov_b32 m0, %2\n\ts_mov_b32 exec_hi, 0\n\tglobal_load_lds_dwordx4 %1, %0\n\ts_mov_b32 exec_hi, -1" :: "s"(s), "v"(v), "s"(m) : "memory");
}
#define DMA_AT(O) "\n\tglobal_load_lds_dwordx4 %1, %0 offset:" #O " sc1"
#define DMA_N(BODY) asm volatile("s_add_u32 m0, %2, %3\n\ts_nop 0" BODY :: "s"(s), "v"(v), "s"(m), "n"(MO) : "memory", "scc")
template <int MO, int N> __device__ __forceinline__ void dma(const void* s, unsigned v, unsigned m) {
  if constexpr (N == 0) DMA_N("\n\ts_mov_b32 exec_hi, 0" DMA_AT(0) "\n\ts_mov_b32 exec_hi, -1");
  if constexpr (N == 1) DMA_N(DMA_AT(0));
  if constexpr (N == 2) DMA_N(DMA_AT(0) DMA_AT(1024));
  if constexpr (N == 3) DMA_N(DMA_AT(0) DMA_AT(1024) DMA_AT(2048));
  if constexpr (N == 4) DMA_N(DMA_AT(0) DMA_AT(1024) DMA_AT(2048) DMA_AT(3072));
}
__device__ __forceinline__ void touch(const void* p, unsigned lds_dummy) {
  asm volatile("s_mov_b32 m0, %1\n\ts_nop 0\n\tglobal_load_lds_dword %0, off" :: "v"(p), "s"(lds_dummy) : "memory");
}
__device__ __forceinline__ void dma_w2(const void* s, unsigned v, unsigned m) {
  asm volatile("s_mov_b32 m0, %2\n\ts_nop 0\n\tglobal_load_lds_dwordx4 %1, %0 nt" :: "s"(s), "v"(v), "s"(m) : "memory");
}
__device__ __forceinline__ void gld16(u32x4& d, const void* s, unsigned v) { asm volatile("global_load_dwordx4 %0, %1, %2 sc1" : "=v"(d) : "v"(v), "s"(s) : "memory"); }
template <int OFF> __device__ __forceinline__ void gld4(unsigned& d, const void* s, unsigned v) { asm volatile("global_load_dword %0, %1, %2 offset:%3 sc1" : "=v"(d) : "v"(v), "s"(s), "n"(OFF) : "memory"); }
#define LWAIT(N) wait_vm<N>()
#define VMBX(N) N
__device__ __forceinline__ void gst16(const void* s, unsigned v, u32x4 d) {
  asm volatile("global_store_dwordx4 %0, %1, %2 sc1\n\ts_nop 1" :: "v"(v), "v"(d), "s"(s) : "memory");
}
__device__ __forceinline__ void gst16m(const void* s, unsigned v, u32x4 d, unsigned long long m) {
  asm volatile("s_mov_b64 exec, %3\n\tglobal_store_dwordx4 %0, %1, %2 sc1\n\ts_mov_b64 exec, -1\n\ts_nop 0" :: "v"(v), "v"(d), "s"(s), "s"(m) : "memory");
}
template <int LO, int OFF = 0> __device__ __forceinline__ void gst4m(const void* s, unsigned v, unsigned d) {
  asm volatile("s_mov_b32 exec_lo, %3\n\ts_mov_b32 exec_hi, 0\n\tglobal_store_dword %0, %1, %2 offset:%4 sc1\n\ts_mov_b64 exec, -1" :: "v"(v), "v"(d), "s"(s), "n"(LO), "n"(OFF) : "memory");
}
#define COLD(L, BACK, BODY) BACK ":\n\t.subsection 1\n" L ":\n\t" BODY "s_branch " BACK "\n\t.subsection 0"
template <int OFF> __device__ __forceinline__ void flag_by(unsigned long long who, const void* s, unsigned l16, unsigned tag) {
  asm volatile("s_mov_b64 exec, %3\n\tglobal_store_dword %0, %1, %2 offset:%4 sc1\n\ts_mov_b64 exec, -1" :: "v"(l16), "v"(tag), "s"(s), "s"(who), "n"(OFF) : "memory");
}
#define TIME_UP(DL, L) SLP "s_memtime vcc\n\ts_waitcnt lgkmcnt(0)\n\ts_sub_u32 vcc_lo, vcc_lo, " DL "\n\ts_cmp_ge_i32 vcc_lo, 0\n\ts_cbranch_scc1 " L "\n\t"
template <int N, int OFF = 0> __device__ __forceinline__ void check_flags(unsigned& v, const void* s, unsigned voff, unsigned tag, unsigned deadline) {
  asm volatile("s_waitcnt vmcnt(%5)\n\tv_cmp_ne_u32 vcc, %4, %0\n\ts_cbranch_vccnz .Lsl%=\n"
               COLD(".Lsl%=", ".Lok%=", "s_cmp_eq_u32 %1, 0\n\ts_cbranch_scc1 .Lok%=\n"
                    ".Lsp%=:\n\tglobal_load_dword %0, %2, %3 offset:%6 sc1\n\ts_waitcnt vmcnt(0)\n\tv_cmp_ne_u32 vcc, %4, %0\n\ts_cbranch_vccz .Lok%=\n\t"
                    TIME_UP("%1", ".Lok%=") "s_branch .Lsp%=\n\t")
               : "+v"(v) : "s"(deadline), "v"(voff), "s"(s), "s"(tag), "n"(N), "n"(OFF) : "memory", "vcc", "scc");
}
__device__ __forceinline__ void poll(const void* s, unsigned voff, unsigned tag, unsigned deadline) {
  unsigned v; gld4<0>(v, s, voff); check_flags<0>(v, s, voff, tag, deadline);
}
__device__ __forceinline__ unsigned make_tag(unsigned nonce) { return nonce * 2654435761u + 0x7F4A7C15u; }
struct Red { u32x4 pp[6], xp; unsigned i2, fw; const char *rp, *rx, *ri, *rf, *f2, *ob; };
#define RED_M(D, S, P, C) "v_mfma_f32_16x16x32_bf16 " D ", " S ", " P ", " C "\n\t"
__device__ __forceinline__ void reduce_ask(Red& u, unsigned l16, unsigned iv, unsigned pv, unsigned vt, unsigned tag, unsigned deadline, int step) {
  asm volatile("s_cmp_lt_u32 %20, 4\n\ts_cbranch_scc1 .Ls%=\n"
               COLD(".Ls%=", ".Lz%=", "s_cmp_eq_u32 %20, 1\n\ts_cbranch_scc1 .Lc%=\n\ts_cmp_eq_u32 %20, 2\n\ts_cbranch_scc1 .Lz%=\n\ts_cmp_eq_u32 %20, 3\n\ts_cbranch_scc1 .Lp%=\n\t"
               "global_load_dword %8, %16, %12 sc1\n\ts_branch .Lz%=\n"
               ".Lp%=:\n\ts_mov_b64 exec, 1\n\tglobal_store_dword %14, %17, %13 sc1\n\ts_mov_b64 exec, -1\n\ts_branch .Lz%=\n"
               ".Lc%=:\n\t"
               "v_cmp_ne_u32 vcc, %18, %8\n\ts_cbranch_vccz .Lr%=\n\t" TIME_UP("%19", ".Lr%=")
               "global_load_dword %8, %16, %12 sc1\n\ts_waitcnt vmcnt(0)\n\ts_branch .Lc%=\n"
               ".Lr%=:\n\tglobal_load_dwordx4 %0, %14, %9 offset:-3072 sc1\n\tglobal_load_dwordx4 %1, %14, %9 offset:-2048 sc1\n\t"
               "global_load_dwordx4 %2, %14, %9 offset:-1024 sc1\n\tglobal_load_dwordx4 %3, %14, %9 sc1\n\tglobal_load_dwordx4 %4, %14, %9 offset:1024 sc1\n\t"
               "global_load_dwordx4 %5, %14, %9 offset:2048 sc1\n\tglobal_load_dwordx4 %6, %14, %10 sc1\n\tglobal_load_dword %7, %15, %11 sc1\n\t")
               : "+v"(u.pp[0]), "+v"(u.pp[1]), "+v"(u.pp[2]), "+v"(u.pp[3]), "+v"(u.pp[4]), "+v"(u.pp[5]), "+v"(u.xp), "+v"(u.i2), "+v"(u.fw)
               : "s"(u.rp), "s"(u.rx), "s"(u.ri), "s"(u.rf), "s"(u.f2), "v"(l16), "v"(iv), "v"(pv), "v"(vt), "s"(tag), "s"(deadline), "s"(step) : "memory", "vcc", "scc");
}
__device__ __forceinline__ void reduce_sum(const Red& u, const u32x4& s0, const u32x4& s1, f32x4& v0, f32x4& v1, float (&x)[8]) {
  asm volatile(RED_M("%0", "%10", "%12", "0") RED_M("%1", "%11", "%12", "0") "v_lshlrev_b32 %2, 16, %18\n\tv_and_b32 %3, 0xffff0000, %18\n\t"
               RED_M("%0", "%10", "%13", "%0") RED_M("%1", "%11", "%13", "%1") "v_lshlrev_b32 %4, 16, %19\n\tv_and_b32 %5, 0xffff0000, %19\n\t"
               RED_M("%0", "%10", "%14", "%0") RED_M("%1", "%11", "%14", "%1") "v_lshlrev_b32 %6, 16, %20\n\tv_and_b32 %7, 0xffff0000, %20\n\t"
               RED_M("%0", "%10", "%15", "%0") RED_M("%1", "%11", "%15", "%1") "v_lshlrev_b32 %8, 16, %21\n\tv_and_b32 %9, 0xffff0000, %21\n\t"
               RED_M("%0", "%10", "%16", "%0") RED_M("%1", "%11", "%16", "%1") RED_M("%0", "%10", "%17", "%0") RED_M("%1", "%11", "%17", "%1")
               : "=&v"(v0), "=&v"(v1), "=&v"(x[0]), "=&v"(x[1]), "=&v"(x[2]), "=&v"(x[3]), "=&v"(x[4]), "=&v"(x[5]), "=&v"(x[6]), "=&v"(x[7])
               : "v"(s0), "v"(s1), "v"(u.pp[0]), "v"(u.pp[1]), "v"(u.pp[2]), "v"(u.pp[3]), "v"(u.pp[4]), "v"(u.pp[5]), "v"(u.xp[0]), "v"(u.xp[1]), "v"(u.xp[2]), "v"(u.xp[3]));
}
__device__ __forceinline__ void wait_store(int step) {
  asm volatile("s_cmp_lt_u32 %0, 4\n\ts_cbranch_scc1 .La%=\n\ts_waitcnt vmcnt(0)\n"
               COLD(".La%=", ".Lb%=", "s_waitcnt vmcnt(8)\n\ts_cmp_eq_u32 %0, 1\n\ts_cbranch_scc1 .Lb%=\n\ts_waitcnt vmcnt(1)\n\t") :: "s"(step) : "memory", "scc");
}

#define STAMP_AT(I)
#define STAMP_IT()
#define STAMP_OUT()
#define TR_DECL()
#define TR(J, I)
#define TR_OUT()

template <bool Z> __device__ __forceinline__ void mf(f32x4& c, const bf16x8& a, const bf16x8& b) {
  if (Z) asm volatile("v_mfma_f32_16x16x32_bf16 %0, %1, %2, 0" : "=&v"(c) : "v"(a), "v"(b));
  else asm volatile("v_mfma_f32_16x16x32_bf16 %0, %1, %2, %0" : "+v"(c) : "v"(a), "v"(b));
}
__device__ __forceinline__ void mfc(f32x4& d, const bf16x8& a, const bf16x8& b, const f32x4& c) {
  asm volatile("v_mfma_f32_16x16x32_bf16 %0, %1, %2, %3" : "=&v"(d) : "v"(a), "v"(b), "v"(c));
}
template <int OFF, class T> __device__ __forceinline__ void lds_rd(T& d, unsigned a) { asm volatile("ds_read_b128 %0, %1 offset:%2" : "=v"(d) : "v"(a), "n"(OFF)); }
template <int OFF, class T> __device__ __forceinline__ void lds_rd8(T& d, unsigned a) { asm volatile("ds_read_b64 %0, %1 offset:%2" : "=v"(d) : "v"(a), "n"(OFF)); }
template <int OFF, class T> __device__ __forceinline__ void lds_wr(unsigned a, const T& d) { asm volatile("ds_write_b128 %0, %1 offset:%2" :: "v"(a), "v"(d), "n"(OFF) : "memory"); }
template <int OFF, class T> __device__ __forceinline__ void lds_wr8(unsigned a, const T& d) { asm volatile("ds_write_b64 %0, %1 offset:%2" :: "v"(a), "v"(d), "n"(OFF) : "memory"); }
template <int NK, int PD = PDG, int G = GRP> struct Ring { bf16x8 q[NK]; };
template <int O, int NK, int PD, int G> __device__ __forceinline__ void prime(Ring<NK, PD, G>& b, unsigned la) {
  static_for<0, PD>([&](auto kc) { lds_rd<O + decltype(kc)::value * 1024>(b.q[decltype(kc)::value], la); });
}
template <int O, int BAR, int VMB, int NK, int PD, int G, class F, class H> __device__ __forceinline__ void stream(Ring<NK, PD, G>& b, unsigned la, F&& f, H&& hook) {
  constexpr int NT = 36 / NK;
  static_assert((BAR < 0 || BAR + PD >= NK) && NK % G == 0, "barrier before the last read");
  if constexpr (BAR >= 0) prio<PR_B>();
  static_for<0, 36>([&](auto sc) {
    constexpr int s = decltype(sc)::value, c = s / NT, grp = s / (G * NT), in = s % (G * NT);
    constexpr int k = grp * G + in % G, t = (G > 1 && (grp & 1)) ? NT - 1 - in / G : in / G;
    constexpr int KZ = (BAR >= 0) ? BAR : NK - PD + 2;
    if constexpr (s == BAR * NT) asm volatile("s_setprio 3\n\ts_waitcnt vmcnt(%0) lgkmcnt(0)\n\ts_barrier" :: "n"(VMB) : "memory");
    if constexpr (BAR < 0 && s == KZ * NT) wait_frag<0>();
    if constexpr (c < KZ && in < G) wait_frag<imin(NK - 1, PD - 1 + (s + NT - 1) / NT) - k>();
    f(IC<k>{}, IC<t>{}, b.q[k]);
    if constexpr (s % NT == 0 && c + PD < NK) lds_rd<O + (c + PD) * 1024>(b.q[c + PD], la);
    if constexpr (in == G * NT - 1) static_for<grp * G, grp * G + G>([&](auto kc) { f(kc, IC<NT>{}, b.q[decltype(kc)::value]); });
    if constexpr (s % NT == NT - 1) hook(IC<c>{});
  });
}
template <class F> __device__ __forceinline__ void periods(F&& f) {
#pragma nounroll
  for (int it = 0; it < 16; ++it) static_for<0, 4>([&](auto jc) { f(jc, it); });
}

__device__ __forceinline__ void max4(float (&s)[4], const f32x4& a, const f32x4& b) {
  asm volatile("v_max_f32 %0, %4, %8\n\tv_max_f32 %1, %5, %9\n\tv_max_f32 %2, %6, %10\n\tv_max_f32 %3, %7, %11" : "=&v"(s[0]), "=&v"(s[1]), "=&v"(s[2]), "=&v"(s[3])
               : "v"(a[0]), "v"(a[1]), "v"(a[2]), "v"(a[3]), "v"(b[0]), "v"(b[1]), "v"(b[2]), "v"(b[3]));
}
struct Soft { float s[4], t0, t1; unsigned mo[2]; };
__device__ __forceinline__ void soft1(Soft& f, float sc) {
  asm volatile("v_max3_f32 %4, %0, %1, %2\n\tv_max_f32 %5, %4, %3\n\tv_max_f32 %4, %4, %3\n\ts_nop 1\n\tv_permlane16_swap_b32 %4, %5\n\t"
               "v_max_f32 %4, %4, %5\n\tv_mov_b32 %5, %4\n\ts_nop 1\n\tv_permlane32_swap_b32 %4, %5\n\tv_max_f32 %4, %4, %5\n\tv_mul_f32 %4, %4, %6\n\t"
               "v_fma_f32 %0, %0, %6, -%4\n\tv_fma_f32 %1, %1, %6, -%4\n\tv_fma_f32 %2, %2, %6, -%4\n\tv_fma_f32 %3, %3, %6, -%4"
               : "+v"(f.s[0]), "+v"(f.s[1]), "+v"(f.s[2]), "+v"(f.s[3]), "=&v"(f.t0), "=&v"(f.t1) : "v"(sc));
}
__device__ __forceinline__ void soft2(Soft& f) {
  asm volatile("v_exp_f32 %0, %0\n\tv_exp_f32 %1, %1\n\tv_exp_f32 %2, %2\n\tv_exp_f32 %3, %3\n\t"
               "v_add_f32 %4, %0, %1\n\tv_add_f32 %5, %2, %3\n\tv_add_f32 %4, %4, %5\n\tv_mov_b32 %5, %4\n\ts_nop 1\n\t"
               "v_permlane16_swap_b32 %4, %5\n\tv_add_f32 %4, %4, %5\n\tv_mov_b32 %5, %4"
               : "+v"(f.s[0]), "+v"(f.s[1]), "+v"(f.s[2]), "+v"(f.s[3]), "=&v"(f.t0), "=&v"(f.t1));
}
#define QK_M(D, W, X, C) "v_mfma_f32_16x16x32_bf16 " D ", " W ", " X ", " C "\n\t"
#define QK_G0 "v_permlane32_swap_b32 %8, %9\n\t"
#define QK_G1 "v_add_f32 %8, %8, %9\n\t"
#define QK_G2 "v_rcp_f32 %8, %8\n\t"
#define QK_G3 "v_mul_f32 %4, %4, %8\n\t"
#define QK_G4 "v_mul_f32 %5, %5, %8\n\tv_mul_f32 %6, %6, %8\n\t"
#define QK_G5 "v_mul_f32 %7, %7, %8\n\t"
#define QK_G6 "v_cvt_pk_bf16_f32 %8, %4, %5\n\tv_cvt_pk_bf16_f32 %9, %6, %7\n\t"
#define QK_G7 "v_and_b32 %4, %8, %17\n\t"
#define QK_G8 "v_and_b32 %5, %9, %18\n\tv_xor_b32 %6, %8, %4\n\t"
#define QK_G9 "v_xor_b32 %7, %9, %5\n\t"
#define QK_OUT(A, B, C, D) "+v"(f.s[A]), "+v"(f.s[B]), "+v"(f.s[C]), "+v"(f.s[D]), "+v"(f.t0), "+v"(f.t1)
#define QK_PAIR(C0, C1, GA, GB, S) asm volatile(QK_M("%0", "%10", "%14", C0) "ds_read_b128 %2, %16 offset:%19\n\t" QK_M("%1", "%11", "%14", C1) GA \
    QK_M("%0", "%12", "%15", "%0") "ds_read_b128 %3, %16 offset:%20\n\t" QK_M("%1", "%13", "%15", "%1") GB \
    : "+v"(a0), "+v"(a1), "=&v"(y0), "=&v"(y1), QK_OUT(S, S + 1, 2 - S, 3 - S) \
    : "v"(w0[0]), "v"(w0[1]), "v"(w1[0]), "v"(w1[1]), "v"(x0), "v"(x1), "v"(la), "v"(f.mo[0]), "v"(f.mo[1]), "n"(OFF), "n"(OFF + 1024))
template <int P, int E, int OFF> __device__ __forceinline__ void qk_pair(f32x4& a0, f32x4& a1, const bf16x8 (&w0)[2], const bf16x8 (&w1)[2], bf16x8 x0, bf16x8 x1,
                                                                         bf16x8& y0, bf16x8& y1, unsigned la, Soft& f) {
  if constexpr (P == 0) QK_PAIR("0", "0", QK_G0, QK_G1, 0);
  if constexpr (P == 1) QK_PAIR("%0", "%1", QK_G2, QK_G3, 0);
  if constexpr (P == 2) QK_PAIR("%0", "%1", QK_G4, QK_G5, 0);
  if constexpr (P == 3) QK_PAIR("%0", "%1", QK_G6, "", 0);
  if constexpr (P == 4) QK_PAIR("%0", "%1", QK_G7 "v_and_b32 %5, %9, %18\n\t", "v_xor_b32 %6, %8, %4\n\t" QK_G9, 2 * E);
}
__device__ __forceinline__ void soft3(Soft& f) {
  int u = 0;
  asm volatile("s_nop 1\n\t" QK_G0 QK_G1 QK_G2 "s_nop 0\n\t" QK_G3 QK_G4 QK_G5 QK_G6 QK_G7 QK_G8 QK_G9 : "+s"(u), "+s"(u), "+s"(u), "+s"(u), QK_OUT(0, 1, 2, 3)
               : "n"(0), "n"(0), "n"(0), "n"(0), "n"(0), "n"(0), "n"(0), "v"(f.mo[0]), "v"(f.mo[1]));
}
__device__ __forceinline__ void mf8(f32x4& a0, f32x4& a1, const bf16x8 (&w0)[2], const bf16x8 (&w1)[2], const bf16x8 (&w2)[2], const bf16x8 (&w3)[2],
                                    const bf16x8& x0, const bf16x8& x1, const bf16x8& x2, const bf16x8& x3) {
  asm volatile(QK_M("%1", "%3", "%10", "%1") QK_M("%0", "%2", "%10", "%0") QK_M("%1", "%5", "%11", "%1") QK_M("%0", "%4", "%11", "%0")
               QK_M("%1", "%7", "%12", "%1") QK_M("%0", "%6", "%12", "%0") QK_M("%1", "%9", "%13", "%1") QK_M("%0", "%8", "%13", "%0")
               : "+v"(a0), "+v"(a1) : "v"(w0[0]), "v"(w0[1]), "v"(w1[0]), "v"(w1[1]), "v"(w2[0]), "v"(w2[1]), "v"(w3[0]), "v"(w3[1]), "v"(x0), "v"(x1), "v"(x2), "v"(x3));
}

constexpr unsigned SPIN = 400000000u;
struct Ctx { char* ws; int g, wave, lane; unsigned dl; const char *fl, *fp, *code; unsigned long long w0; };
constexpr unsigned long long HALF = 0x0000FFFF0000FFFFull;

__device__ __forceinline__ unsigned get_tag(const char* ws) {
  unsigned nonce;
  asm volatile("s_load_dword %0, %1, 0x0\n\ts_waitcnt lgkmcnt(0)" : "=s"(nonce) : "s"(ws) : "memory");
  return make_tag(nonce);
}
template <int R, int NP, int NX = 0, class I, class Q> __device__ __forceinline__ void load_w(char* smem, const Ctx& cx, unsigned& tag, I&& issue, Q&& rd) {
  const int wave = cx.wave;
  if (wave == 3) STAMP_AT(72);
  static_for<0, 3>([&](auto rc) { issue(rc); });
  tag = get_tag(cx.ws);
  static_for<0, R>([&](auto rc) {
    constexpr int r = decltype(rc)::value;
    wait_vm<imin(2, R - 1 - r) * NP + ((r >= 1 && r <= 3) ? NX : 0)>();
    bar_bare();
    if (wave == 3 && r < 6) STAMP_AT(73 + r);
    rd(rc);
    bar_raw();
    if constexpr (r + 3 < R) issue(IC<r + 3>{});
    if constexpr (r + 3 == R) { if (wave == 4) touch(cx.code, lds_addr(smem) + 162816); }
  });
}
__device__ __forceinline__ bf16x8 asb(u32x4 v) { return __builtin_bit_cast(bf16x8, v); }
__device__ __forceinline__ bf16x8 ones8() { return asb(u32x4{0x3F803F80u, 0x3F803F80u, 0x3F803F80u, 0x3F803F80u}); }
__device__ __forceinline__ u32x4 widen(unsigned w0, unsigned w1) {
  const auto s0 = __builtin_amdgcn_permlane16_swap(w0, w0, false, false);
  const auto s1 = __builtin_amdgcn_permlane16_swap(w1, w1, false, false);
  return u32x4{s0[0], s1[0], s0[1], s1[1]};
}
__device__ __forceinline__ u32x4 pack8(const f32x4& a, const f32x4& b) { return u32x4{cvt2(a[0], a[1]), cvt2(a[2], a[3]), cvt2(b[0], b[1]), cvt2(b[2], b[3])}; }
__device__ __forceinline__ f32x2 row_stat(float sum, float sq) {
  const float mean = sum * (1.0f / 576.0f);
  const float var = fmaxf(sq * (1.0f / 576.0f) - mean * mean, 0.0f);
  return f32x2{-rbf(mean), __builtin_amdgcn_rcpf(rbf(__builtin_amdgcn_sqrtf(rbf(rbf(var) + 1e-5f))))};
}
__device__ __forceinline__ float diag(const f32x4& a, int p) { return (p == 0) ? a[0] : (p == 1) ? a[1] : (p == 2) ? a[2] : a[3]; }
__device__ __forceinline__ int top(int g) { return (1 - (g >> 1)) * 32; }

__device__ __forceinline__ void role_gu(char* smem, const u16* wg, const u16* wu, u16* out, const Ctx& cx, int c) {
  const int wave = cx.wave, lane = cx.lane;
  const unsigned sm0 = lds_addr(smem);
  unsigned tag;
  const int r = lane & 15, g = lane >> 4, q = r >> 2, p = lane & 3;
  TR_DECL();
  bf16x8 W[18][2];
  u32x4 sel0, sel1;
#pragma unroll
  for (int e = 0; e < 2; ++e) {
    sel0[e] = sel1[2 + e] = (r == 4 * g + 2 * e ? 0x3F80u : 0u) | (r == 4 * g + 2 * e + 1 ? 0x3F800000u : 0u);
    sel0[2 + e] = sel1[e] = 0u;
  }
  tie(sel0, sel1);
  {
    f32x4 cs[2] = {{0.f, 0.f, 0.f, 0.f}, {0.f, 0.f, 0.f, 0.f}};
    constexpr int BUF = 48 * 1056;
    const char* src = (const char*)((wave >> 2) ? wu : wg) + c * 256;
    const unsigned vo = (4 * (g & 1) + 16 * (g >> 1)) * (FD * 2) + r * 16;
    const char* rb = smem + ((g & 1) * 4 + q) * 1056 + (g >> 1) * 512 + wave * 32 + p * 8;
    load_w<6, 6>(smem, cx, tag, [&](auto rc) {
      constexpr int rr = decltype(rc)::value;
#pragma unroll
      for (int j = 0; j < 6; ++j) {
        const int idx = 6 * (wave & 3) + j, id = idx & 7;
        dma_w2(src + (size_t)(32 * (3 * rr + (idx >> 3)) + 8 * (id >> 2) + (id & 3)) * (FD * 2), vo, sm0 + (rr % 3) * BUF + ((wave >> 2) * 24 + idx) * 1056);
      }
    }, [&](auto rc) {
      constexpr int rr = decltype(rc)::value;
#pragma unroll
      for (int kl = 0; kl < 3; ++kl)
#pragma unroll
        for (int m = 0; m < 2; ++m) { const char* a = rb + (rr % 3) * BUF + (m * 24 + kl * 8) * 1056; W[3 * rr + kl][m] = tr_frag(a, a + 256); cs[m] = MFMA(ones8(), W[3 * rr + kl][m], cs[m]); }
    });
#pragma unroll
    for (int m = 0; m < 2; ++m) {
      const float cm = cs[m][0] * (-1.0f / 576.0f);
      const f32x4 c4 = {cm, cm, cm, cm};
#pragma unroll
      for (int k = 0; k < 18; ++k) { W[k][m] = asb(pack8(MFMA(asb(sel0), W[k][m], c4), MFMA(asb(sel1), W[k][m], c4))); tie(W[k][m]); }
    }
  }
  const char* hb = cx.ws + WS_H + (size_t)(cx.g * 6 + c / 6) * 64 * HB + (c % 6) * 4096 + wave * 512 - HB;
  const unsigned l16 = vopq(lane * 16), hv = vopq((g >> 1) * 256 + r * 16);
  const unsigned la[2] = {vopq(sm0 + l16), vopq(sm0 + l16 + 2 * TB)};
  f32x4 ag, au;
  Ring<18> bq;
  auto mm = [&](auto kc, auto jc, const bf16x8& x) {
    constexpr int k = decltype(kc)::value;
    if constexpr (decltype(jc)::value == 0) mf<k == 0>(ag, W[k][0], x);
    if constexpr (decltype(jc)::value == 1) mf<k == 0>(au, W[k][1], x);
  };
  auto epi = [&]() {
    tie(ag, au);
    return widen(cvt2(ag[0] * au[0], ag[1] * au[1]), cvt2(ag[2] * au[2], ag[3] * au[3]));
  };
  if (wave == 3) STAMP_AT(70);
  if (wave < 4) {
    const char* xs = cx.ws + WS_X1 + (size_t)cx.g * 64 * TB + wave * 4608;
    const char *fr = cx.fl + TLAG * 128, *fp = cx.fp;
    const unsigned pv = vopq((F_X1 + (lane & 1)) * FSTR), l4 = vopq(l16 + 4096), mb = sm0 + wave * 4608, vt = vopq(tag);
    auto fetch = [&](auto sc) { dma<decltype(sc)::value * TB, 4>(xs, l16, mb); dma<decltype(sc)::value * TB + 4096, 0>(xs, l4, mb); };
    unsigned dl = cx.dl, fw;
    int tgt = (wave == 1 && c < 32) ? c : 99;
    poll(fr, pv, tag, dl); fetch(IC<0>{});
    poll(fr + 128, pv, tag, dl);
    adv(fr, 256); gld4<0>(fw, fr, pv);
    adv(xs, TB); fetch(IC<1>{});
    wait_vm<5>();
    prio<PR_A>();
    bar_bare();
    if (wave == 3) STAMP_AT(55);
    periods([&](auto jc, int it) {
      constexpr int j = decltype(jc)::value;
      prime<(j & 1) * TB>(bq, la[j >> 1]);
      flag_by<j * 128>(cx.w0, fp, l16, vt);
      stream<(j & 1) * TB, -1, 0>(bq, la[j >> 1], mm, [&](auto kc) {
        constexpr int k = decltype(kc)::value;
        if constexpr (k == 0) TR(1, 1);
        if constexpr (k == 17) TR(1, 2);
        if constexpr (k == 11) adv(xs, TB);
        if constexpr (k == 12 && j == 2) dl = sopq((it == 15) ? 0u : dl);
        if constexpr (k == 13) adv(hb, HB);
        if constexpr (k == 16 && j == 3) adv(fp, 512);
      });
      prio<3>();
      check_flags<6, j * 128>(fw, fr, pv, tag, dl);
      TR(1, 3);
      gld4<(j + 1) * 128>(fw, fr, pv);
      fetch(IC<(j + 2) & 3>{});
      TR(1, 4);
      gst16m(hb, hv, epi(), HALF);
      if constexpr (j == 3) adv(fr, 512);
      TR(1, 5);
      LWAIT(7);
      TR(1, 6);
      if (__builtin_expect(4 * it + j == tgt, 0)) {
        f32x4 s1 = {0.f, 0.f, 0.f, 0.f}, s2 = s1;
#pragma unroll
        for (int k = 0; k < 18; ++k) {
          const bf16x8 x = *(const bf16x8*)(smem + j * TB + k * 1024 + lane * 16);
          s1 = MFMA(ones8(), x, s1); s2 = MFMA(x, x, s2);
        }
        const float is = row_stat(s1[0], diag(s2, p))[1];
        const float i2 = u2f(__builtin_amdgcn_ds_bpermute((r + 16 * q) * 4, f2i(is * is)));
        gst4m<-1>(cx.ws + WS_IS + (cx.g * 64 + tgt) * 128, lane * 4, f2i(i2));
        tgt += 32;
      }
      STAMP_IT();
      prio<PR_A>();
      bar_bare();
      TR(0, 0); TR(1, 7);
    });
    TR_OUT();
    flag_by<0>(cx.w0, fp, l16, vt);
    wait_vm<0>(); tie(fw);
    bar_bare();
    flag_by<128>(cx.w0, fp, l16, vt); flag_by<256>(cx.w0, fp, l16, vt);
    STAMP_OUT();
  } else {
    const int kb = 2 * (c % 9) + (wave & 1);
    int tau = c / 9 + 2 * (wave & 2);
    Red u = {};
    const unsigned vt = vopq(tag), iv = vopq(r * 4), pq = vopq((F_P + (kb / 6) * 6 + min(lane, 5)) * FSTR), vrow = vopq(r * 2304 + g * 16);
    u.rp = cx.ws + WS_P + (size_t)((cx.g * 18 + kb) * 64 + tau) * PB + 3072;
    u.rx = cx.ws + WS_X1 + (size_t)(cx.g * 64 + tau) * TB + kb * 1024;
    u.ri = cx.ws + WS_IS + (cx.g * 64 + tau) * 128;
    u.rf = cx.fl + (tau + PLAG) * 128;
    u.f2 = cx.ws + WS_F2 + ((cx.g & 1) * 72 + top(cx.g) + tau - 8) * F2ROW + kb * 128;
    u.ob = cx.ws + WS_X2 + (size_t)((cx.g & 1) * 64 + top(cx.g) + tau) * TB + kb * 1024;
    const char* ob2 = (const char*)out + (size_t)((cx.g >> 1) * 1024 + 32 * (31 - tau) + (cx.g & 1)) * (DM * 2) + kb * 64;
    gst16m(u.f2 + 8 * F2ROW, (lane >> 3) * (8 * F2ROW) + (lane & 7) * 16, u32x4{~tag, ~tag, ~tag, ~tag}, 0xFFFFFFFFull);
    auto ask = [&](int step) { reduce_ask(u, l16, iv, pq, vt, tag, cx.dl, step); };
    auto finish = [&](int tau_) {
      f32x4 v0, v1; float x[8];
      reduce_sum(u, sel0, sel1, v0, v1, x);
      adv(u.rp, 0); adv(u.rx, 0); adv(u.ri, 0); adv(u.rf, 0); adv(u.f2, 0); adv(u.ob, 0);
      const int tau = sopq(tau_);
      const char* o = u.ob;
      const unsigned ov = vopq((tau >= 32) ? vrow : l16);
      adv(u.rp, 8 * PB); adv(u.rx, 8 * TB); adv(u.ri, 8 * 128); adv(u.rf, 8 * 128);
      adv(u.f2, (tau < 32) ? 8 * F2ROW : 0);
      u.ob = ((tau >> 3) == 3) ? ob2 : u.ob + ((tau < 32) ? 8l * TB : -8l * 32 * DM * 2); adv(u.ob, 0);
      tie(v0, v1);
      const float sc = u2f(u.i2);
      gst16(o, ov, u32x4{cvt2(fmaf(v0[0], sc, x[0]), fmaf(v0[1], sc, x[1])), cvt2(fmaf(v0[2], sc, x[2]), fmaf(v0[3], sc, x[3])),
                         cvt2(fmaf(v1[0], sc, x[4]), fmaf(v1[1], sc, x[5])), cvt2(fmaf(v1[2], sc, x[6]), fmaf(v1[3], sc, x[7]))});
    };
    int step = -1;
    bar_bare();
    prime<0>(bq, la[0]);
    wait_lg<0>();
    periods([&](auto jc, int it) {
      constexpr int j = decltype(jc)::value, j1 = (j + 1) & 3;
      stream<(j & 1) * TB, BARU, 63>(bq, la[j >> 1], mm, [&](auto kc) {
        constexpr int k = decltype(kc)::value;
        if constexpr (k == 0) TR(1, 6);
        if constexpr (k == BARU - 2) TR(1, 7);
        if constexpr (k == BARU) { TR(0, 0); TR(1, 8); }
        if constexpr (k == 10) adv(hb, HB);
        if constexpr (HLAG == 2 && k == BARU - 1) wait_store(step);
      });
      settle();
      u32x4 h = epi();
      tie(h);
      TR(0, 1);
      LWAIT(0);
      TR(0, 2);
      gst16m(hb, hv, h, HALF);
      step = 4 * it + j - LAGF - tau;
      ask(step);
      TR(0, 3);
      if (__builtin_expect(step == 2, 0)) finish(tau);
      tau += (step == 3) ? 8 : 0;
      TR(0, 4);
      prio<0>();
      prime<(j1 & 1) * TB>(bq, la[j1 >> 1]);
      TR(0, 5);
      if constexpr (j == 3) wait_lg<0>();
    });
    TR_OUT();
    wait_vm<0>();
    bar_bare();
    if (step == 2) tau += 8;
#pragma nounroll
    while (tau < 64) {
      ask(0); wait_vm<0>(); ask(1); wait_vm<0>(); ask(2);
      finish(tau); tau += 8;
    }
  }
}

__device__ __forceinline__ void role_dn(char* smem, const u16* wd, const Ctx& cx, int ks, int ns) {
  const int wave = cx.wave, lane = cx.lane;
  const unsigned sm0 = lds_addr(smem);
  unsigned tag;
  const int r = lane & 15, g = lane >> 4, q = r >> 2, p = lane & 3;
  const int kq = wave >> 2, nq = wave & 3;
  constexpr int EX = 4 * HB;
  TR_DECL();
  bf16x8 W[12][3];
  {
    constexpr int BUF = 49152;
    const char* src = (const char*)wd + (size_t)(768 * ks) * (DM * 2) + ns * 384;
    unsigned vo[6];
#pragma unroll
    for (int j = 0; j < 6; ++j) {
      const unsigned ch = (wave * 6 + j) * 64 + lane, row = ch / 24;
      vo[j] = ((row >> 6) * 384 + (row & 63)) * (DM * 2) + (ch - row * 24) * 16;
    }
    const char* rb = smem + (kq * 64 + 8 * g + q) * 384;
    load_w<6, 6>(smem, cx, tag, [&](auto rc) {
      constexpr int rr = decltype(rc)::value;
#pragma unroll
      for (int j = 0; j < 6; ++j) dma_w2(src + rr * 64 * (DM * 2), vo[j], sm0 + (rr % 3) * BUF + (wave * 6 + j) * 1024);
    }, [&](auto rc) {
      constexpr int rr = decltype(rc)::value;
#pragma unroll
      for (int kl = 0; kl < 2; ++kl)
#pragma unroll
        for (int m = 0; m < 3; ++m) {
          const char* a = rb + (rr % 3) * BUF + kl * 32 * 384 + ((m < 2) ? nq * 64 + p * 16 + m * 8 : 256 + nq * 32 + p * 8);
          W[2 * rr + kl][m] = tr_frag(a, a + 4 * 384);
          tie(W[2 * rr + kl][m]);
        }
    });
  }
  const unsigned l16 = vopq(lane * 16);
  const unsigned la[2] = {vopq(sm0 + l16 + kq * 12288), vopq(sm0 + l16 + kq * 12288 + 2 * HB)};
  const unsigned ea = vopq(sm0 + EX + nq * 3072 + l16);
  f32x4 acc[3];
  auto mm = [&](auto kc, auto jc, const bf16x8& x) {
    constexpr int k = decltype(kc)::value;
    if constexpr (decltype(jc)::value < 3) mf<k == 0>(acc[decltype(jc)::value % 3], W[k][decltype(jc)::value % 3], x);
  };
  if (wave == 3) STAMP_AT(70);
  if (wave < 4) {
    Ring<12, PDD, GRD> bq;
    const char* hs = cx.ws + WS_H + (size_t)(cx.g * 6 + ks) * 64 * HB + nq * 6144;
    const char *fr = cx.fl + HLAG * 128, *fp = cx.fp;
    const unsigned l3 = vopq(l16 + 3072), mb = sm0 + nq * 6144, pv = vopq((F_H + 6 * ks + min(lane, 5)) * FSTR), vt = vopq(tag);
    auto fetch = [&](auto sc) { dma<decltype(sc)::value * HB, 3>(hs, l16, mb); dma<decltype(sc)::value * HB + 3072, 3>(hs, l3, mb); };
    const unsigned lm[3] = {l16, vopq(l16 + 2048), vopq(l16 + 4096)};
    unsigned dl = cx.dl, fw;
    poll(fr, pv, tag, dl); fetch(IC<0>{});
    poll(fr + 128, pv, tag, dl);
    adv(fr, 256); gld4<0>(fw, fr, pv);
    adv(hs, HB); fetch(IC<1>{});
    prio<PR_A>();
    bar_bare();
    if (wave == 3) STAMP_AT(55);
    periods([&](auto jc, int it) {
      constexpr int j = decltype(jc)::value;
      prime<(j & 1) * HB>(bq, la[j >> 1]);
      flag_by<j * 128>(cx.w0, fp, l16, vt);
      stream<(j & 1) * HB, -1, 0>(bq, la[j >> 1], mm, [&](auto kc) {
        constexpr int k = decltype(kc)::value;
        if constexpr (k == 0) TR(1, 1);
        if constexpr (k == 11) TR(1, 2);
        if constexpr (k == 6) adv(hs, HB);
        if constexpr (k == 7 && j == 2) dl = sopq((it == 15) ? 0u : dl);
        if constexpr (k == 10 && j == 3) adv(fp, 512);
      });
      prio<3>();
      check_flags<6, j * 128>(fw, fr, pv, tag, dl);
      TR(1, 6);
      gld4<(j + 1) * 128>(fw, fr, pv);
      prio<EXP>();
      static_for<0, 3>([&](auto mc) {
        constexpr int m = decltype(mc)::value;
        dma<((j + 2) & 3) * HB + m * 2048, 2>(hs, lm[m], mb);
        lds_wr<(j & 1) * 12288 + m * 1024>(ea, acc[m]);
      });
      STAMP_IT();
      prio<PR_A>();
      TR(1, 3);
      if constexpr (j == 3) adv(fr, 512);
      LWAIT(7);
      TR(1, 7);
      bar_raw();
      TR(0, 0); TR(1, 4);
    });
    TR_OUT();
    flag_by<0>(cx.w0, fp, l16, vt);
    bar_bare();
    flag_by<128>(cx.w0, fp, l16, vt); flag_by<256>(cx.w0, fp, l16, vt);
    wait_vm<0>(); tie(fw);
    STAMP_OUT();
  } else {
    Ring<12, PDB, GRB> bq;
    const char* pd = cx.ws + WS_P + (size_t)(cx.g * 18 + 6 * ns + nq) * 64 * PB + ks * 1024 - PB;
    const unsigned hv = vopq((g >> 1) * 256 + r * 16 + (4 + (nq >> 1) - nq) * 64 * PB + (nq & 1) * 512);
    bar_bare();
    prime<0>(bq, la[0]);
    wait_lg<0>();
    periods([&](auto jc, int it) {
      constexpr int j = decltype(jc)::value, j1 = (j + 1) & 3;
      f32x4 e[3];
      stream<(j & 1) * HB, BARD, VMBX(2)>(bq, la[j >> 1], mm, [&](auto kc) {
        constexpr int k = decltype(kc)::value;
        if constexpr (k == 0) TR(1, 6);
        if constexpr (k == BARD - 1) TR(1, 7);
        if constexpr (k == BARD) { TR(0, 0); TR(1, 8); }
        if constexpr (k == 6) adv(pd, PB);
        if constexpr (k == EK) static_for<0, 3>([&](auto mc) { constexpr int m = decltype(mc)::value; lds_rd<(j & 1) * 12288 + m * 1024>(e[m], ea); });
      });
      TR(0, 1);
      settle();
      wait_lg<0>();
      TR(0, 2);
      tie(acc[0], acc[1], acc[2], e[0], e[1], e[2]);
      gst16(pd, l16, pack8(e[0] + acc[0], e[1] + acc[1]));
      TR(0, 3);
      e[2] += acc[2];
      gst16m(pd, hv, widen(cvt2(e[2][0], e[2][1]), cvt2(e[2][2], e[2][3])), HALF);
      TR(0, 4);
      prio<0>();
      prime<(j1 & 1) * HB>(bq, la[j1 >> 1]);
      TR(0, 5);
      if constexpr (j == 3) wait_lg<0>();
    });
    TR_OUT();
    wait_vm<0>();
    bar_bare();
  }
}

template <bool A> __device__ __forceinline__ void role_wo(char* smem, const u16* wo, const Ctx& cx, int jw) {
  const int wave = cx.wave, lane = cx.lane;
  const unsigned sm0 = lds_addr(smem);
  unsigned tag;
  const int r = lane & 15, g = lane >> 4, q = r >> 2, p = lane & 3;
  constexpr int RS = 4 * TB, EX = RS + 4 * 9216;
  TR_DECL();
  bf16x8 W[18][2], WX[9];
  {
    constexpr int BUF = 40960;
    const char* src = (const char*)wo + jw * 576;
    unsigned vo[5], vx[5];
#pragma unroll
    for (int i = 0; i < 5; ++i) {
      const unsigned ch = min((wave * 5 + i) * 64 + lane, 2303), row = ch / 36, cc = (ch - row * 36) * 16;
      const unsigned kl = row >> 5, gg = (row >> 3) & 3, e = row & 7;
      vo[i] = (36 * kl + 20 * (e >> 2) + 4 * gg + (e & 3)) * (DM * 2) + cc;
      vx[i] = (72 * (4 * kl + gg) + 16 + 36 * (e >> 2) + (e & 3)) * (DM * 2) + cc;
    }
    const char* rb = smem + (8 * g + q) * 576 + p * 16;
    load_w<9, 5>(smem, cx, tag, [&](auto rc) {
      constexpr int rr = decltype(rc)::value;
#pragma unroll
      for (int i = 0; i < 5; ++i) dma_w(src + (rr < 8 ? rr * 72 * (DM * 2) : 0), rr < 8 ? vo[i] : vx[i], sm0 + (rr % 3) * BUF + (wave * 5 + i) * 1024);
    }, [&](auto rc) {
      constexpr int rr = decltype(rc)::value;
#pragma unroll
      for (int kl = 0; kl < 2; ++kl) {
        const char* a = rb + (rr % 3) * BUF + kl * 32 * 576;
#pragma unroll
        for (int m = 0; m < 2; ++m) { W[2 * rr + kl][m] = tr_frag(a + wave * 64 + m * 8, a + wave * 64 + m * 8 + 4 * 576); tie(W[2 * rr + kl][m]); }
        if constexpr (A) {
          const bf16x8 wx = tr_frag(a + 512 + (wave & 1) * 8, a + 512 + (wave & 1) * 8 + 4 * 576);
          const int k = 2 * rr + kl;
          if (k < 9) WX[k % 9] = wx; else if (wave & 2) WX[k % 9] = wx;
          tie(WX[k % 9]);
        }
      }
    });
  }
  const unsigned l16 = vopq(lane * 16), l3 = vopq(l16 + 3072);
  const unsigned la[2] = {vopq(sm0 + l16), vopq(sm0 + l16 + 2 * TB)};
  const unsigned ra = vopq(sm0 + RS + wave * 1024 + l16), mr = sm0 + RS + wave * 1024;
  const char* xo = cx.ws + WS_X1 + (size_t)cx.g * 64 * TB + jw * 9216 + wave * 1024 - TB;
  const char* rs = cx.ws + WS_X0 + (size_t)((cx.g & 1) * 64 + top(cx.g)) * TB + (9 * jw + wave) * 1024;
  constexpr long RSW = TB - 32l * TB - (long)(WS_X0 - WS_X2);
  poll(cx.ws + WS_F2 + ((cx.g & 1) * 72 + top(cx.g)) * F2ROW, F_X0 * 128 + (lane & 1) * F2ROW, tag, cx.dl);
  f32x4 a0, a1, ax;
  Ring<18, PDG, GRW> bq;
  auto init = [&](const u32x4& x) {
    a0 = f32x4{blo(x[0]), bhi(x[0]), blo(x[1]), bhi(x[1])}; a1 = f32x4{blo(x[2]), bhi(x[2]), blo(x[3]), bhi(x[3])};
    tie(a0, a1);
  };
  dma<0, 1>(rs, l16, mr); if (wave == 5) dma<3072, 1>(rs, l3, mr);
  adv(rs, TB);
  dma<9216, 1>(rs, l16, mr); if (wave == 5) dma<9216 + 3072, 1>(rs, l3, mr);
  wait_vm<0>();
  init(*(const u32x4*)(smem + RS + wave * 1024 + lane * 16));
  if (wave == 3) STAMP_AT(70);
  if constexpr (A) {
    const unsigned ea = vopq(sm0 + EX + wave * 1024 + l16), vt = vopq(tag);
    prio<PR_A>();
    bar_bare();
    if (wave == 3) STAMP_AT(55);
    auto loop = [&](auto hc) {
      constexpr int KH = decltype(hc)::value;
      auto mm = [&](auto kc, auto jc, const bf16x8& x) {
        constexpr int k = decltype(kc)::value;
        if constexpr (decltype(jc)::value == 0) mf<false>(a0, W[k][0], x);
        if constexpr (decltype(jc)::value == 1) mf<false>(a1, W[k][1], x);
        if constexpr (decltype(jc)::value == 2 && k / 9 == KH) mf<k % 9 == 0>(ax, WX[k % 9], x);
      };
      const char* fp = cx.fp;
      periods([&](auto jc, int it) {
        constexpr int j = decltype(jc)::value;
        u32x4 x;
        prime<(j & 1) * TB>(bq, la[j >> 1]);
        if constexpr (KH == 0) flag_by<j * 128>(cx.w0, fp, l16, vt);
        stream<(j & 1) * TB, -1, 0>(bq, la[j >> 1], mm, [&](auto kc) {
          constexpr int k = decltype(kc)::value;
          if constexpr (k == 0) TR(1, 1);
          if constexpr (k == 17) TR(1, 2);
          if constexpr (k == 12) adv(rs, (j == 2 && it == 7) ? RSW : TB);
          if constexpr (k == 13) adv(xo, TB);
          if constexpr (k == 14 && j == 3) adv(fp, 512);
          if constexpr (k == 16) { LWAIT(0); lds_rd<((j + 1) & 3) * 9216>(x, ra); }
        });
        prio<3>();
        dma<((j + 2) & 3) * 9216, 1>(rs, l16, mr);
        asm volatile("s_nop 3" : "+v"(a0), "+v"(a1), "+v"(ax) :: "memory");
        gst16(xo, l16, pack8(a0, a1));
        asm volatile("s_waitcnt lgkmcnt(0)" : "+v"(x) :: "memory");
        init(x);
        prio<0>();
        lds_wr<(j & 1) * 4096>(ea, ax);
        STAMP_IT();
        prio<PR_A>();
        TR(1, 3);
        bar_raw();
        TR(0, 0); TR(1, 4);
      });
      if constexpr (KH == 0) flag_by<0>(cx.w0, fp, l16, vt);
      wait_vm<0>();
      bar_bare();
      if constexpr (KH == 0) flag_by<128>(cx.w0, fp, l16, vt);
      STAMP_OUT();
      TR_OUT();
    };
    if (wave < 2) loop(IC<0>{}); else loop(IC<1>{});
  } else {
    const int b = (wave == 4) ? 0 : wave - 5;
    const char* os = cx.ws + WS_O + (size_t)(cx.g * 68 + 3) * TB + b * 6144;
    const unsigned mb = sm0 + b * 6144, ea = vopq(sm0 + EX + l16), pv = vopq((F_O + min(lane, 23)) * FSTR);
    auto mm = [&](auto kc, auto jc, const bf16x8& x) {
      constexpr int k = decltype(kc)::value;
      if constexpr (decltype(jc)::value == 0) mf<false>(a0, W[k][0], x);
      if constexpr (decltype(jc)::value == 1) mf<false>(a1, W[k][1], x);
    };
    auto loop = [&](auto pc) {
      constexpr bool SH = decltype(pc)::value;
      auto fetch = [&](auto sc) { dma<decltype(sc)::value * TB, 3>(os, l16, mb); dma<decltype(sc)::value * TB + 3072, 3>(os, l3, mb); };
      const char* fr = cx.fl + OLAG * 128;
      unsigned dl = cx.dl;
      if constexpr (!SH) { poll(fr, pv, tag, dl); fetch(IC<0>{}); wait_vm<0>(); }
      bar_bare();
      if constexpr (!SH) { poll(fr + 128, pv, tag, dl); adv(os, TB); fetch(IC<1>{}); }
      adv(fr, 256);
      unsigned fw;
      if constexpr (!SH && FKW < 0) gld4<0>(fw, fr, pv);
      prime<0>(bq, la[0]);
      wait_lg<0>();
      periods([&](auto jc, int it) {
        constexpr int j = decltype(jc)::value, j1 = (j + 1) & 3;
        u32x4 x, x8; f32x4 e[4];
        TR(1, 5);
        stream<(j & 1) * TB, BARG, VMBX(SH ? 0 : 1)>(bq, la[j >> 1], mm, [&](auto kc) {
          constexpr int k = decltype(kc)::value;
          if constexpr (k == 0) TR(1, 6);
          if constexpr (k == 13) TR(1, 7);
          if constexpr (k == 14) { TR(0, 0); TR(1, 8); }
          if constexpr (!SH && k == FKW) gld4<j * 128>(fw, fr, pv);
          if constexpr (!SH && k == 10 && j == 2) dl = sopq((it == 15) ? 0u : dl);
          if constexpr (k == 11) adv(rs, (j == 2 && it == 7) ? RSW : TB);
          if constexpr (k == 12) adv(xo, TB);
          if constexpr (!SH && k == 13) adv(os, (j == 2 && it == 7) ? 2 * TB : TB);
        });
        TR(0, 1);
        if constexpr (!SH) { check_flags<0, j * 128>(fw, fr, pv, tag, dl); fetch(IC<(j + 2) & 3>{}); }
        dma<((j + 2) & 3) * 9216, 1>(rs, l16, mr);
        if constexpr (SH) dma<((j + 2) & 3) * 9216 + 3072, 1>(rs, l3, mr);
        TR(0, 2);
        prio<0>();
        if constexpr (SH) {
          static_for<0, 4>([&](auto mc) { constexpr int m = decltype(mc)::value; lds_rd<(j & 1) * 4096 + m * 1024>(e[m], ea); });
          lds_rd<j * 9216 + 3072>(x8, ra);
        }
        lds_rd<j1 * 9216>(x, ra);
        prio<3>();
        tie(a0, a1);
        gst16(xo, l16, pack8(a0, a1));
        TR(0, 3);
        asm volatile("s_waitcnt lgkmcnt(0)" : "+v"(x) :: "memory");
        if constexpr (SH) {
          tie(e[0], e[1], e[2], e[3], x8);
          gst16(xo + 3072, l16, pack8(e[0] + e[2] + f32x4{blo(x8[0]), bhi(x8[0]), blo(x8[1]), bhi(x8[1])}, e[1] + e[3] + f32x4{blo(x8[2]), bhi(x8[2]), blo(x8[3]), bhi(x8[3])}));
        }
        init(x);
        TR(0, 4);
        prio<0>();
        prime<(j1 & 1) * TB>(bq, la[j1 >> 1]);
        if constexpr (j == 1) adv(fr, (it == 7) ? 128 : 0);
        if constexpr (!SH && FKW < 0) gld4<(j + 1) * 128>(fw, fr, pv);
        if constexpr (j == 3) { adv(fr, 512); wait_lg<0>(); }
      });
      wait_vm<0>();
      if constexpr (!SH) tie(fw);
      bar_bare();
      TR_OUT();
    };
    if (wave == 5) loop(std::true_type{}); else loop(std::false_type{});
  }
}

template <class F> __device__ __forceinline__ void qa_periods(F&& f) {
#pragma nounroll
  for (int it = 0; it < 16; ++it) static_for<0, 4>([&](auto jc) { f(jc, IC<0>{}, it); });
  static_for<0, 4>([&](auto jc) { f(jc, IC<1>{}, 16); });
}
__device__ __forceinline__ void role_qa(char* smem, const u16* wq, const u16* x0, const Ctx& cx, int jq) {
  const int wave = cx.wave, lane = cx.lane;
  const unsigned sm0 = lds_addr(smem);
  unsigned tag;
  const int r = lane & 15, g = lane >> 4, q = r >> 2, p = lane & 3;
  constexpr int XR = 4 * TB, PP = XR + 2048, ST = PP + 8192;
  const f32x4 z4 = {0.f, 0.f, 0.f, 0.f};
  const unsigned l16 = vopq(lane * 16);
  const bool low = cx.g < 2;
  TR_DECL();
  const char* xs = cx.ws + WS_X0 + (size_t)((cx.g & 1) * 64 + top(cx.g)) * TB + (wave & 3) * 4096;
  const char* fr = cx.ws + WS_F2 + ((cx.g & 1) * 72 + top(cx.g)) * F2ROW;
  const unsigned mb = sm0 + (wave & 3) * 4096, l4 = vopq(l16 + 4096);
  auto fetch = [&](auto sc, auto nc) {
    dma<decltype(sc)::value * TB, 4>(xs, l16, mb);
    if constexpr (decltype(nc)::value == 6) dma<decltype(sc)::value * TB + 4096, 2>(xs, l4, mb);
  };
  bf16x8 W[18][2];
  {
    constexpr int BUF = 41472, B0 = 2 * TB;
    const char* src = (const char*)wq + jq * 144;
    unsigned vo[6];
#pragma unroll
    for (int i = 0; i < 6; ++i) {
      const unsigned ch = min((i < 5 ? wave * 5 + i : 40) * 64 + lane, 2591), row = ch / 27, cc = ch - row * 27, sec = cc / 9;
      vo[i] = row * (NQKV * 2) + sec * (DM * 2 - 144) + cc * 16;
    }
    int c0, c1;
    if (wave >= 4) { c0 = 36 * (wave & 3) + (wave & 1) * 4 + p * 8; c1 = c0 + 144; }
    else if (wave < 2) { c0 = 288 + 72 * wave + p * 8; c1 = c0 + 40; }
    else { c0 = (p >> 1) * 144 + 32 + (p & 1) * 72; c1 = 288 + 32 + (p & 1) * 72; }
    const char* rb = smem + B0 + (8 * g + q) * 432;
    load_w<6, 6, 5>(smem, cx, tag, [&](auto rc) {
      constexpr int rr = decltype(rc)::value;
#pragma unroll
      for (int i = 0; i < 5; ++i) dma_w(src + rr * 96 * (NQKV * 2), vo[i], sm0 + B0 + (rr % 3) * BUF + (wave * 5 + i) * 1024);
      dma_wh(src + rr * 96 * (NQKV * 2), vo[5], sm0 + B0 + (rr % 3) * BUF + 40960);
      if constexpr (rr == 3)
    {
      const char* xr = (const char*)x0 + (size_t)((cx.g >> 1) * 1024 + 32 * (31 - (wave >> 2)) + (cx.g & 1)) * (DM * 2);
#pragma unroll
      for (int i = 0; i < 5; ++i) { const int kb = min((wave & 3) * 5 + i, 17); dma_w(xr + kb * 64, r * 2304 + g * 16, sm0 + (wave >> 2) * TB + kb * 1024); }
    }
    }, [&](auto rc) {
      constexpr int rr = decltype(rc)::value;
#pragma unroll
      for (int kl = 0; kl < 3; ++kl)
#pragma unroll
        for (int m = 0; m < 2; ++m) {
          const char* a = rb + (rr % 3) * BUF + kl * 32 * 432 + (m ? c1 : c0);
          W[3 * rr + kl][m] = tr_frag(a, a + 4 * 432);
          tie(W[3 * rr + kl][m]);
        }
    });
  }
  const unsigned la[2] = {vopq(sm0 + l16), vopq(sm0 + l16 + 2 * TB)};
  if (wave < 4) {
    Ring<18, PDG, GRQ> bq;
    unsigned pv = vopq(F_X0 * 128), dl = cx.dl, fw = vopq(~tag);
    adv(fr, 2 * F2ROW); adv(xs, 2 * TB);
    auto feed = [&](auto jc, auto nc, int it) {
      constexpr int j = decltype(jc)::value;
      const char* xn = xs + TB;
      adv(fr, F2ROW);
      if constexpr (j == 1) { const bool st = low && it == 7; dl = st ? 0u : dl; xn = st ? xs : xn; }
      if constexpr (j == 2) {
        const bool st = low && it == 15; dl = st ? 0u : cx.dl; xn = st ? xs : xn;
        if (it == 7) { xn = cx.ws + WS_X2 + (size_t)((cx.g & 1) * 64 + top(cx.g)) * TB + wave * 4096; fr -= 33 * F2ROW; pv = vopq(min(lane, 17) * 128); }
      }
      gld4<0>(fw, fr, pv);
      fetch(IC<(j + 2) & 3>{}, nc);
      xs = xn; adv(xs, 0);
    };
    if (wave == 3) {
      STAMP_AT(70);
      bf16x8 one = ones8();
      tie(one);
      f32x4 a0, a1;
      const unsigned sa = vopq(sm0 + ST + ((g == q) ? r * 8 : 256));
      auto mm = [&](auto kc, auto jc, const bf16x8& x) {
        constexpr int k = decltype(kc)::value;
        if constexpr (decltype(jc)::value == 0) mf<k == 0>(a0, one, x);
        if constexpr (decltype(jc)::value == 1) mf<k == 0>(a1, x, x);
      };
      prio<PR_A>();
      bar_raw();
      STAMP_AT(55);
      qa_periods([&](auto jc, auto ec, int it) {
        constexpr int j = decltype(jc)::value; constexpr bool END = decltype(ec)::value;
        if constexpr (!END || j < 2) {
          prime<(j & 1) * TB>(bq, la[j >> 1]);
          stream<(j & 1) * TB, -1, 0>(bq, la[j >> 1], mm, [&](auto kc) {
            constexpr int k = decltype(kc)::value;
            if constexpr (k == 0) TR(1, 1);
            if constexpr (k == 17) TR(1, 4);
          });
          prio<0>();
          if constexpr (!END) { check_flags<6>(fw, fr, pv, tag, dl); feed(jc, IC<6>{}, it); }
          else { wait_vm<0>(); tie(fw); settle(); }
          tie(a0, a1);
          lds_wr8<(j & 1) * 128>(sa, row_stat(a0[0], diag(a1, p)));
          prio<PR_A>();
          if constexpr (!END) LWAIT(7);
        }
        STAMP_IT();
        TR(1, 6);
        bar_raw();
        TR(0, 0); TR(1, 7);
      });
      TR_OUT();
      STAMP_OUT();
      if (cx.g == 0 && jq == 0) {
        for (int g2 = 0; g2 < 4; ++g2) poll(cx.ws + WS_FL + g2 * FGRP, lane * FSTR, tag, cx.dl);
        gst4m<1>(cx.ws, l16, vopq((tag - 0x7F4A7C15u) * 244002641u + 1u));
      }
    } else {
      const unsigned sa = vopq(sm0 + ST + g * 32), xa = vopq(sm0 + XR + l16), vt = vopq(tag);
      auto loop = [&](auto xc) {
        constexpr bool X = decltype(xc)::value;
        f32x4 c0 = z4, c1 = z4;
#pragma unroll
        for (int k = 0; k < 18; ++k) { c0 = X ? MFMA(W[k][0], ones8(), c0) : MFMA(ones8(), W[k][0], c0); c1 = MFMA(ones8(), W[k][1], c1); }
        if constexpr (X) *(f32x4*)(smem + XR + 1024 + lane * 16) = c0;
        const float cv[2] = {c0[0], c1[0]};
        f32x4 a[2][2] = {{z4, z4}, {z4, z4}};
        u32x4 vw[2] = {{0u, 0u, 0u, 0u}, {0u, 0u, 0u, 0u}};
        const unsigned pa = vopq(sm0 + PP + (X ? 0 : wave * 2048) + l16);
        const char* ob = cx.ws + WS_O + (size_t)cx.g * 68 * TB + (X ? 16384 + jq * 256 : (2 * jq + wave) * 1024) - TB;
        const char* fp = cx.fp + wave * 8 * FSTR;
        prio<PR_A>();
        bar_raw();
        qa_periods([&](auto jc, auto ec, int it) {
          constexpr int j = decltype(jc)::value, e = j & 1, o = 1 - e; constexpr bool END = decltype(ec)::value;
          auto mm = [&](auto kc, auto ic, const bf16x8& x) {
            constexpr int k = decltype(kc)::value;
            if constexpr (decltype(ic)::value == 0) { if constexpr (X) mf<k == 0>(a[e][0], W[k][0], x); else mf<k == 0>(a[e][0], x, W[k][0]); }
            if constexpr (decltype(ic)::value == 1) mf<k == 0>(a[e][1], x, W[k][1]);
          };
          bf16x8 pf[4]; f32x4 d[4], s0, s1;
          auto reads = [&](auto ic) {
            constexpr int i = decltype(ic)::value;
            if constexpr (i < (X ? 2 : 1)) { lds_rd<o * 4096 + i * 2048>(pf[2 * i], pa); lds_rd<o * 4096 + i * 2048 + 1024>(pf[2 * i + 1], pa); }
            if constexpr (i == 2) { lds_rd<o * 128>(s0, sa); lds_rd<o * 128 + 16>(s1, sa); }
          };
          if constexpr (!END || j < 2) {
            prime<e * TB>(bq, la[j >> 1]);
            stream<e * TB, -1, 0>(bq, la[j >> 1], mm, [&](auto kc) {
              constexpr int k = decltype(kc)::value;
              if constexpr (k == 0) TR(1, 1);
              if constexpr (k == 6) TR(1, 2);
              if constexpr (k == 12) TR(1, 3);
              if constexpr (k == 17) TR(1, 4);
              if constexpr (k == 13) adv(ob, TB);
              if constexpr (k >= 14 && k < 17) reads(IC<k - 14>{});
            });
          } else { adv(ob, TB); static_for<0, 3>([&](auto ic) { reads(ic); }); }
          asm volatile("s_waitcnt lgkmcnt(0)" : "+v"(s0), "+v"(s1) :: "memory");
          static_for<0, X ? 4 : 2>([&](auto hc) { constexpr int h = X ? 3 - decltype(hc)::value : decltype(hc)::value; mf<true>(d[h], asb(vw[X ? 1 : h]), pf[h]); });
          prio<0>();
          if constexpr (!END) check_flags<4>(fw, fr, pv, tag, dl); else { wait_vm<0>(); tie(fw); settle(); }
          gst4m<1, j * 128>(fp, l16, vt);
          if constexpr (X) {
            if constexpr (!END || j < 2) lds_wr<e * 1024>(xa, a[e][0]);
            tie(d[0], d[1], d[2], d[3]);
            unsigned e2 = cvt2(d[2][0], d[2][1]), e3 = cvt2(d[3][2], d[3][3]), e0 = cvt2(d[0][0], d[0][1]), e1 = cvt2(d[1][2], d[1][3]), n2, n3;
            asm volatile("s_nop 0\n\tv_permlane16_swap_b32 %0, %2\n\tv_permlane16_swap_b32 %1, %3" : "+v"(e2), "+v"(e3), "=&v"(n2), "=&v"(n3) : "v"(e0), "v"(e1));
            gst16m(ob, l16, u32x4{e0, e1, n2, n3}, 0xFFFFull);
          } else { tie(d[0], d[1]); gst16(ob, l16, pack8(d[0], d[1])); }
          TR(1, 5);
          if constexpr (!END) feed(jc, IC<4>{}, it);
#pragma unroll
          for (int m = X ? 1 : 0; m < 2; ++m) {
            const f32x4 w = a[o][m];
            vw[m][2 * o] = cvt2(fmaf(cv[m], s0[0], w[0]) * s0[1], fmaf(cv[m], s0[2], w[1]) * s0[3]);
            vw[m][2 * o + 1] = cvt2(fmaf(cv[m], s1[0], w[2]) * s1[1], fmaf(cv[m], s1[2], w[3]) * s1[3]);
            tie(vw[m]);
          }
          prio<PR_A>();
          if constexpr (j == 3) adv(fp, 512);
          if constexpr (!END) LWAIT(7);
          TR(1, 6);
          bar_raw();
          TR(0, 0); TR(1, 7);
        });
        wait_vm<0>();
        gst4m<1>(fp, l16, vt);
        TR_OUT();
      };
      if (wave == 2) loop(std::true_type{}); else loop(std::false_type{});
    }
  } else {
    const int h = wave - 4;
    f32x4 cq = z4, ck = z4;
#pragma unroll
    for (int k = 0; k < 18; ++k) { cq = MFMA(W[k][0], ones8(), cq); ck = MFMA(W[k][1], ones8(), ck); }
    tie(cq, ck);
    const float ninf = -__builtin_inff();
    f32x4 bo, bp;
#pragma unroll
    for (int e = 0; e < 4; ++e) { const int m = 4 * g + e; bo[e] = (m <= r && m >= r - 7) ? 0.f : ninf; bp[e] = (m >= r + 9) ? 0.f : ninf; }
    Soft f = {};
#pragma unroll
    for (int e = 0; e < 2; ++e) f.mo[e] = vopq((4 * g + 2 * e <= r ? 0xFFFFu : 0u) | (4 * g + 2 * e + 1 <= r ? 0xFFFF0000u : 0u));
    const unsigned xa = vopq(sm0 + XR + (h >> 1) * 256 + r * 16 + (h & 1) * 8), sa = vopq(sm0 + ST + r * 8), pa = vopq(sm0 + PP + h * 1024 + l16);
    const float g0 = (g == 0) ? 1.f : 0.f;
    bar_raw();
    f32x2 cxq, cxk;
    lds_rd8<1024>(cxq, xa); lds_rd8<1536>(cxk, xa);
    bf16x8 x[18];
    auto first = [&](auto oc, unsigned a) { static_for<0, 8>([&](auto kc) { lds_rd<decltype(oc)::value + decltype(kc)::value * 1024>(x[decltype(kc)::value], a); }); };
    first(IC<0>{}, la[0]);
    wait_lg<0>();
    tie(cxq, cxk);
    f32x4 a0 = z4, a1 = z4, so = z4;
    u32x4 qf = {0u, 0u, 0u, 0u}, kf = qf;
    float sc = 0.f, scn = 0.f;
    tie(qf, kf, bo, bp);
    qa_periods([&](auto jc, auto ec, int it) {
      constexpr int j = decltype(jc)::value, e = j & 1, j1 = (j + 1) & 3; constexpr bool END = decltype(ec)::value;
      auto put = [&]() { lds_wr<e * 4096>(pa, f32x4{f.s[0], f.s[1], f.s[2], f.s[3]}); };
      if constexpr (END && j >= 2) {
        if constexpr (j == 2) { soft3(f); put(); }
        bar_raw();
      } else {
        prio<PR_B>();
        static_for<0, 5>([&](auto pc) {
          constexpr int k = 2 * decltype(pc)::value;
          wait_frag<6>();
          qk_pair<k / 2, e, e * TB + (k + 8) * 1024>(a0, a1, W[k], W[k + 1], x[k], x[k + 1], x[k + 8], x[k + 9], la[j >> 1], f);
          if constexpr (k == 0) TR(1, 7);
          if constexpr (k == 4) TR(1, 8);
        });
        TR(1, 9);
        wait_frag<4>();
        mf<false>(a0, W[10][0], x[10]);
        put();
        mf<false>(a1, W[10][1], x[10]);
        static_for<11, 14>([&](auto kc) {
          constexpr int k = decltype(kc)::value;
          wait_frag<5>();
          mf<false>(a0, W[k][0], x[k]); mf<false>(a1, W[k][1], x[k]);
        });
        TR(1, 10);
        asm volatile("s_setprio 3\n\ts_waitcnt lgkmcnt(0)\n\ts_barrier" ::: "memory");
        TR(0, 0); TR(1, 11);
        f32x2 st, xq, xk;
        lds_rd8<e * 128>(st, sa); lds_rd8<e * 1024 + 512>(xk, xa); lds_rd8<e * 1024>(xq, xa);
        mf8(a0, a1, W[14], W[15], W[16], W[17], x[14], x[15], x[16], x[17]);
        asm volatile("s_waitcnt lgkmcnt(0)" : "+v"(st), "+v"(xq), "+v"(xk) :: "memory");
        TR(0, 1);
        const float ix = st[1] * g0;
        kf[2] = cvt2(fmaf(cxk[0], st[0], xk[0]) * ix, fmaf(cxk[1], st[0], xk[1]) * ix);
        sc = scn; scn = st[1] * (0.23570226039551584f * 1.4426950408889634f);
        asm volatile("" : "+v"(kf), "+v"(scn), "+v"(a0), "+v"(a1));
        kf[0] = cvt2(fmaf(ck[0], st[0], a1[0]) * st[1], fmaf(ck[1], st[0], a1[1]) * st[1]);
        kf[1] = cvt2(fmaf(ck[2], st[0], a1[2]) * st[1], fmaf(ck[3], st[0], a1[3]) * st[1]);
        f32x4 sp;
        if constexpr ((!END && j == 0) || (END && j == 1)) {
          const float off = (low && (END || it == 8)) ? ninf : 0.f;
          sp = MFMA(asb(kf), asb(qf), bp + off);
        } else sp = MFMA(asb(kf), asb(qf), bp);
        __builtin_amdgcn_sched_barrier(0);
        qf[0] = cvt2(fmaf(cq[0], st[0], a0[0]), fmaf(cq[1], st[0], a0[1]));
        qf[1] = cvt2(fmaf(cq[2], st[0], a0[2]), fmaf(cq[3], st[0], a0[3]));
        qf[2] = cvt2(fmaf(cxq[0], st[0], xq[0]), fmaf(cxq[1], st[0], xq[1]));
        asm volatile("" : "+v"(qf), "+v"(sp));
        TR(0, 3);
        max4(f.s, so, sp);
        if constexpr (!(END && j == 1)) mfc(so, asb(kf), asb(qf), bo);
        soft1(f, sc);
        if constexpr (END) tie(bo);
        TR(0, 4);
        if constexpr (!(END && j == 1)) first(IC<(j1 & 1) * TB>{}, la[j1 >> 1]);
        TR(0, 5);
        soft2(f);
        TR(0, 6);
        if constexpr (j == 3) wait_lg<0>();
      }
    });
    TR_OUT();
  }
}

__global__ __launch_bounds__(512, 2) void k_main(const u16* x0, const u16* wqkv, const u16* wo,
                                                 const u16* wg, const u16* wu, const u16* wd,
                                                 char* ws, u16* out) {
  __shared__ __attribute__((aligned(1024))) char smem[SMEM_BYTES];
  Ctx cx;
  cx.ws = ws;
  cx.wave = __builtin_amdgcn_readfirstlane(threadIdx.x >> 6);
  cx.lane = threadIdx.x & 63;
  asm volatile("s_cmp_eq_u32 %1, 0\n\ts_cselect_b64 %0, 1, 0" : "=s"(cx.w0) : "s"(cx.wave) : "scc");
  cx.dl = ((unsigned)__builtin_amdgcn_s_memtime() + SPIN) | 1u;
  const unsigned t0 = (unsigned)__builtin_amdgcn_s_memrealtime();
  const int bid = blockIdx.x, xh = bid & 1, x4 = bid & 3, qd = (bid >> 2) & 1, i = bid >> 3, lane = cx.lane, wave = cx.wave;
  const int d = i - 23, tri = d / 3;
  const int c = x4 * 9 + ((i - 5) >> 1), ks = (tri < 2) ? x4 : 4 + (x4 >> 1), ns = d - 3 * tri;
  cx.g = (i < 4) ? 2 * (1 - qd) + 1 - (x4 >> 1) : (i == 4) ? 2 * (1 - qd) + (x4 >> 1) : (i < 23) ? 2 * qd + ((i - 5) & 1) : 2 * (1 - qd) + (tri < 2 ? tri : xh);
  cx.fl = ws + WS_FL + cx.g * FGRP;
  cx.fp = cx.fl + (i < 4 ? F_O + xh * 4 + i : i == 4 ? F_X1 + xh : i < 23 ? F_H + c : F_P + ns * 6 + ks) * FSTR;
  {
    unsigned long long pc; unsigned len;
    asm volatile("s_getpc_b64 %0\n.Lkbeg:\n\ts_mov_b32 %1, .Lkend-.Lkbeg" : "=s"(pc), "=s"(len));
    cx.code = (const char*)pc + min((unsigned)((((i + 11) & 15) * 64 + lane) * 128), len);
  }
  {
    const unsigned dm = lds_addr(smem) + 162816;
    if (i >= 5) {
      const int ln = min(((i - 5) * 8 + wave) * 64 + lane, 11519), sg = ln / 5;
      const char* bq = (const char*)wqkv; const char* bw = (const char*)wo - 1728 * 1152;
      touch((sg < 1728 ? bq : bw) + (size_t)sg * 1152 + (xh * 4 + ln - sg * 5) * 128, dm);
    }
    if (wave < 2) touch(ws + min((size_t)(wave * 64 + lane) << 20, WS_END - 4), dm);
    else if (wave == 2) touch((const char*)out + min(lane << 20, 2359292), dm);
    else if (wave == 3 && i >= 5 && i < 20) {
      const int ln = min(((i - 5) * 2 + xh) * 64 + lane, 26 * 72 - 1);
      touch(ws + WS_FL + (size_t)((bid >> 1) & 3) * FGRP + F_X1 * FSTR + ln * 128, dm);
    } else if (wave == 5 && i >= 5 && i < 10) {
      const int ga = 2 * (1 - qd) + 1 - (x4 >> 1), ln = min((i - 5) * 64 + lane, 287), rw = ln / 9;
      touch((const char*)x0 + (size_t)((ga >> 1) * 1024 + 960 + 2 * rw + (ga & 1)) * (DM * 2) + (ln - rw * 9) * 128, dm);
    } else if (wave == 4) {
      touch(cx.code, dm);
    }
  }
  if (i >= 24) {
    u32x4 v[5];
    const int g0 = (bid >> 1) & 3, n0 = ((wave >> 2) * 8 + i - 24) * 2 + xh, w4 = wave & 3;
    const char* src = (const char*)x0 + (size_t)((g0 >> 1) * 1024 + 32 * (31 - n0) + (g0 & 1)) * (DM * 2);
    const char* dst = ws + WS_X0 + (size_t)((g0 & 1) * 64 + top(g0) + n0) * TB;
#pragma unroll
    for (int k = 0; k < 5; ++k) gld16(v[k], src + ((k < 4) ? 4 * w4 + k : 16 + (w4 & 1)) * 64, (lane & 15) * 2304 + (lane >> 4) * 16);
    const unsigned tag = get_tag(ws);
    wait_vm<0>();
    if (wave == 3) STAMP_AT(68);
    tie(v[0], v[1], v[2], v[3], v[4]);
#pragma unroll
    for (int k = 0; k < 5; ++k) if (k < 4 || w4 < 2) gst16(dst + ((k < 4) ? 4 * w4 + k : 16 + w4) * 1024, lane * 16, v[k]);
    wait_vm<0>();
    bar_bare();
    if (wave == 3) STAMP_AT(69);
    if (w4 == 0) gst4m<-1>(ws + WS_F2 + ((g0 & 1) * 72 + top(g0) + n0) * F2ROW + F_X0 * 128, lane * 4, vopq(tag));
  }
  if (i >= 5) {
    const unsigned z = ~get_tag(ws);
    gst16(cx.fp + wave * 1024, lane * 16, u32x4{z, z, z, z});
    if (wave == 0) gst16(cx.fp + 8192, lane * 16, u32x4{z, z, z, z});
  }
  if (i >= 5) { const unsigned dly = (i < 23) ? DLY_G : DLY_D; while ((unsigned)__builtin_amdgcn_s_memrealtime() - t0 < dly) __builtin_amdgcn_s_sleep(4); }
  if (i < 4) role_qa(smem, wqkv, x0, cx, xh * 4 + i);
  else if (i == 4) { if (wave < 4) role_wo<true>(smem, wo, cx, xh); else role_wo<false>(smem, wo, cx, xh); }
  else if (i < 23) role_gu(smem, wg, wu, out, cx, c);
  else role_dn(smem, wd, cx, ks, ns);
  asm volatile(".subsection 2\n.Lkend:\n\t.fill 64, 4, 0xBF800000\n\t.subsection 0");
}
#if defined(__HIP_DEVICE_COMPILE__)
#pragma clang attribute pop
#endif

extern "C" void kernel_launch(void* const* d_in, const int* in_sizes, int n_in,
                              void* d_out, int out_size, void* d_ws, size_t ws_size,
                              hipStream_t stream) {
  k_main<<<256, 512, 0, stream>>>((const u16*)d_in[0], (const u16*)d_in[1], (const u16*)d_in[2], (const u16*)d_in[4],
                                  (const u16*)d_in[5], (const u16*)d_in[6], (char*)d_ws, (u16*)d_out);
}
```

```cpp
#include <hip/hip_runtime.h>
#include <stdint.h>
#include <type_traits>

#if defined(__HIP_DEVICE_COMPILE__)
#pragma clang attribute push(__attribute__((target("no-packed-fp32-ops"))), apply_to = function)
#endif
typedef __attribute__((ext_vector_type(8))) short bf16x8;
typedef __attribute__((ext_vector_type(4))) short bf16x4;
typedef __attribute__((ext_vector_type(4))) float f32x4;
typedef __attribute__((ext_vector_type(2))) float f32x2;
typedef __attribute__((ext_vector_type(4))) unsigned u32x4;
typedef __attribute__((ext_vector_type(2))) unsigned u32x2;
typedef __bf16 bf2v __attribute__((ext_vector_type(2)));
typedef unsigned short u16;

#define MFMA(b, a, c) __builtin_amdgcn_mfma_f32_16x16x32_bf16(b, a, c, 0, 0, 0)
constexpr int DM = 576, FD = 4608, NQKV = 3 * DM;
constexpr int TB = 18432;
constexpr int HB = 24576;
constexpr int PB = 6144;
constexpr int FSTR = 72 * 128, FGRP = 80 * FSTR, F2ROW = 19 * 128;
constexpr size_t WS_FL = 4096;
constexpr size_t WS_IS = WS_FL + 4 * FGRP;
constexpr size_t WS_F2 = WS_IS + 4 * 64 * 128;
constexpr size_t WS_O = WS_F2 + 2 * 72 * F2ROW;
constexpr size_t WS_X1 = WS_O + (size_t)4 * 68 * TB;
constexpr size_t WS_X2 = WS_X1 + (size_t)256 * TB;
constexpr size_t WS_X0 = WS_X2 + (size_t)128 * TB;
constexpr size_t WS_P = WS_X0 + (size_t)128 * TB;
constexpr size_t WS_H = WS_P + (size_t)72 * 64 * PB;
constexpr size_t WS_END = WS_H + (size_t)(24 * 64 + 4) * HB;
static_assert(WS_O % 1024 == 0 && WS_END <= ((size_t)256 << 20), "workspace");
constexpr int F_H = 0, F_P = 36, F_X1 = 54, F_O = 56, F_X0 = 18;
constexpr int TLAG = 2, PLAG = 3, OLAG = 4;
constexpr int SMEM_BYTES = 163840;
#define GRP 2
#define GRD 2
#define GRB 12
#define GRW 1
#define GRQ 1
#define PDB 12
#define PDD 5
constexpr int PDG = 6;
#define BARU 14
#define BARD 10
#define FKG 14
#define FKD 9
constexpr int BARG = 14;
constexpr int PR_A = 2, PR_B = 1;
#define DLY_G 200
#define DLY_D 700
#define SLP "s_sleep 2\n\t"
#define EXP 3
#define FK -1
#define FKW -1
#define HLAG 2
#define EK 10
#define F0R 5
#define FRG 1
#define FRD 1
#define RK0 3
#define BT 7
#define LAGF 8

__device__ __forceinline__ float u2f(unsigned u) { return __builtin_bit_cast(float, u); }
__device__ __forceinline__ int f2i(float f) { return __builtin_bit_cast(int, f); }
__device__ __forceinline__ float rbf(float f) { __bf16 b = (__bf16)f; return u2f((unsigned)__builtin_bit_cast(u16, b) << 16); }
__device__ __forceinline__ unsigned cvt2(float a, float b) { return __builtin_bit_cast(unsigned, __builtin_convertvector(f32x2{a, b}, bf2v)); }
__device__ __forceinline__ float blo(unsigned w) { return u2f(w << 16); }
__device__ __forceinline__ float bhi(unsigned w) { return u2f(w & 0xffff0000u); }
typedef __attribute__((address_space(3))) bf16x4* lds_b4p;
__device__ __forceinline__ bf16x4 tr_read(const char* p) {
  return __builtin_amdgcn_ds_read_tr16_b64_v4i16((lds_b4p)(__attribute__((address_space(3))) void*)(p));
}
__device__ __forceinline__ bf16x8 tr_frag(const char* lo, const char* hi) {
  return __builtin_shufflevector(tr_read(lo), tr_read(hi), 0, 1, 2, 3, 4, 5, 6, 7);
}
__device__ __forceinline__ unsigned lds_addr(const void* p) { return (unsigned)(size_t)(__attribute__((address_space(3))) const char*)p; }
template <int I, int N, class F> __device__ __forceinline__ void static_for(F&& f) {
  if constexpr (I < N) { f(std::integral_constant<int, I>{}); static_for<I + 1, N>(f); }
}
template <int N> using IC = std::integral_constant<int, N>;
constexpr int imin(int a, int b) { return a < b ? a : b; }
template <int N> __device__ __forceinline__ void wait_vm() { asm volatile("s_waitcnt vmcnt(%0)" ::"n"(N) : "memory"); }
template <int N> __device__ __forceinline__ void wait_lg() { asm volatile("s_waitcnt lgkmcnt(%0)" ::"n"(N) : "memory"); }
template <int N> __device__ __forceinline__ void wait_frag() { __builtin_amdgcn_s_waitcnt(0xC07F | (N << 8)); }
__device__ __forceinline__ void bar_raw() { asm volatile("s_waitcnt lgkmcnt(0)\n\ts_barrier" ::: "memory"); }
__device__ __forceinline__ void bar_bare() { asm volatile("s_barrier" ::: "memory"); }
template <int P> __device__ __forceinline__ void prio() { asm volatile("s_setprio %0" :: "n"(P)); }
__device__ __forceinline__ void settle() { asm volatile("s_nop 7" ::: "memory"); }
__device__ __forceinline__ int sopq(int v) { asm volatile("" : "+s"(v)); return v; }
__device__ __forceinline__ unsigned vopq(unsigned v) { asm volatile("" : "+v"(v)); return v; }
template <class A> __device__ __forceinline__ void tie(A& a) { asm volatile("" : "+v"(a)); }
template <class A, class... R> __device__ __forceinline__ void tie(A& a, R&... r) { tie(a); tie(r...); }
__device__ __forceinline__ void adv(const char*& p, long d) { p += d; asm volatile("" : "+s"(p)); }

__device__ __forceinline__ void dma_w(const void* s, unsigned v, unsigned m) {
  asm volatile("s_mov_b32 m0, %2\n\ts_nop 0\n\tglobal_load_lds_dwordx4 %1, %0" :: "s"(s), "v"(v), "s"(m) : "memory");
}
__device__ __forceinline__ void dma_wh(const void* s, unsigned v, unsigned m) {
  asm volatile("s_mov_b32 m0, %2\n\ts_mov_b32 exec_hi, 0\n\tglobal_load_lds_dwordx4 %1, %0\n\ts_mov_b32 exec_hi, -1" :: "s"(s), "v"(v), "s"(m) : "memory");
}
#define DMA_AT(O) "\n\tglobal_load_lds_dwordx4 %1, %0 offset:" #O " sc1"
#define DMA_N(BODY) asm volatile("s_add_u32 m0, %2, %3\n\ts_nop 0" BODY :: "s"(s), "v"(v), "s"(m), "n"(MO) : "memory", "scc")
template <int MO, int N> __device__ __forceinline__ void dma(const void* s, unsigned v, unsigned m) {
  if constexpr (N == 0) DMA_N("\n\ts_mov_b32 exec_hi, 0" DMA_AT(0) "\n\ts_mov_b32 exec_hi, -1");
  if constexpr (N == 1) DMA_N(DMA_AT(0));
  if constexpr (N == 2) DMA_N(DMA_AT(0) DMA_AT(1024));
  if constexpr (N == 3) DMA_N(DMA_AT(0) DMA_AT(1024) DMA_AT(2048));
  if constexpr (N == 4) DMA_N(DMA_AT(0) DMA_AT(1024) DMA_AT(2048) DMA_AT(3072));
}
__device__ __forceinline__ void touch(const void* p, unsigned lds_dummy) {
  asm volatile("s_mov_b32 m0, %1\n\ts_nop 0\n\tglobal_load_lds_dword %0, off" :: "v"(p), "s"(lds_dummy) : "memory");
}
__device__ __forceinline__ void dma_w2(const void* s, unsigned v, unsigned m) {
  asm volatile("s_mov_b32 m0, %2\n\ts_nop 0\n\tglobal_load_lds_dwordx4 %1, %0 nt" :: "s"(s), "v"(v), "s"(m) : "memory");
}
__device__ __forceinline__ void gld16(u32x4& d, const void* s, unsigned v) { asm volatile("global_load_dwordx4 %0, %1, %2 sc1" : "=v"(d) : "v"(v), "s"(s) : "memory"); }
template <int OFF> __device__ __forceinline__ void gld4(unsigned& d, const void* s, unsigned v) { asm volatile("global_load_dword %0, %1, %2 offset:%3 sc1" : "=v"(d) : "v"(v), "s"(s), "n"(OFF) : "memory"); }
#define LWAIT(N) wait_vm<N>()
#define VMBX(N) N
__device__ __forceinline__ void gst16(const void* s, unsigned v, u32x4 d) {
  asm volatile("global_store_dwordx4 %0, %1, %2 sc1\n\ts_nop 1" :: "v"(v), "v"(d), "s"(s) : "memory");
}
__device__ __forceinline__ void gst16m(const void* s, unsigned v, u32x4 d, unsigned long long m) {
  asm volatile("s_mov_b64 exec, %3\n\tglobal_store_dwordx4 %0, %1, %2 sc1\n\ts_mov_b64 exec, -1\n\ts_nop 0" :: "v"(v), "v"(d), "s"(s), "s"(m) : "memory");
}
template <int LO, int OFF = 0> __device__ __forceinline__ void gst4m(const void* s, unsigned v, unsigned d) {
  asm volatile("s_mov_b32 exec_lo, %3\n\ts_mov_b32 exec_hi, 0\n\tglobal_store_dword %0, %1, %2 offset:%4 sc1\n\ts_mov_b64 exec, -1" :: "v"(v), "v"(d), "s"(s), "n"(LO), "n"(OFF) : "memory");
}
#define COLD(L, BACK, BODY) BACK ":\n\t.subsection 1\n" L ":\n\t" BODY "s_branch " BACK "\n\t.subsection 0"
template <int OFF> __device__ __forceinline__ void flag_by(unsigned long long who, const void* s, unsigned l16, unsigned tag) {
  asm volatile("s_mov_b64 exec, %3\n\tglobal_store_dword %0, %1, %2 offset:%4 sc1\n\ts_mov_b64 exec, -1" :: "v"(l16), "v"(tag), "s"(s), "s"(who), "n"(OFF) : "memory");
}
#define TIME_UP(DL, L) SLP "s_memtime vcc\n\ts_waitcnt lgkmcnt(0)\n\ts_sub_u32 vcc_lo, vcc_lo, " DL "\n\ts_cmp_ge_i32 vcc_lo, 0\n\ts_cbranch_scc1 " L "\n\t"
template <int N, int OFF = 0> __device__ __forceinline__ void check_flags(unsigned& v, const void* s, unsigned voff, unsigned tag, unsigned deadline) {
  asm volatile("s_waitcnt vmcnt(%5)\n\tv_cmp_ne_u32 vcc, %4, %0\n\ts_cbranch_vccnz .Lsl%=\n"
               COLD(".Lsl%=", ".Lok%=", "s_cmp_eq_u32 %1, 0\n\ts_cbranch_scc1 .Lok%=\n"
                    ".Lsp%=:\n\tglobal_load_dword %0, %2, %3 offset:%6 sc1\n\ts_waitcnt vmcnt(0)\n\tv_cmp_ne_u32 vcc, %4, %0\n\ts_cbranch_vccz .Lok%=\n\t"
                    TIME_UP("%1", ".Lok%=") "s_branch .Lsp%=\n\t")
               : "+v"(v) : "s"(deadline), "v"(voff), "s"(s), "s"(tag), "n"(N), "n"(OFF) : "memory", "vcc", "scc");
}
__device__ __forceinline__ void poll(const void* s, unsigned voff, unsigned tag, unsigned deadline) {
  unsigned v; gld4<0>(v, s, voff); check_flags<0>(v, s, voff, tag, deadline);
}
__device__ __forceinline__ unsigned make_tag(unsigned nonce) { return nonce * 2654435761u + 0x7F4A7C15u; }
struct Red { u32x4 pp[6], xp; unsigned i2, fw; const char *rp, *rx, *ri, *rf, *f2, *ob; };
#define RED_M(D, S, P, C) "v_mfma_f32_16x16x32_bf16 " D ", " S ", " P ", " C "\n\t"
__device__ __forceinline__ void reduce_ask(Red& u, unsigned l16, unsigned iv, unsigned pv, unsigned vt, unsigned tag, unsigned deadline, int step) {
  asm volatile("s_cmp_lt_u32 %20, 4\n\ts_cbranch_scc1 .Ls%=\n"
               COLD(".Ls%=", ".Lz%=", "s_cmp_eq_u32 %20, 1\n\ts_cbranch_scc1 .Lc%=\n\ts_cmp_eq_u32 %20, 2\n\ts_cbranch_scc1 .Lz%=\n\ts_cmp_eq_u32 %20, 3\n\ts_cbranch_scc1 .Lp%=\n\t"
               "global_load_dword %8, %16, %12 sc1\n\ts_branch .Lz%=\n"
               ".Lp%=:\n\ts_mov_b64 exec, 1\n\tglobal_store_dword %14, %17, %13 sc1\n\ts_mov_b64 exec, -1\n\ts_branch .Lz%=\n"
               ".Lc%=:\n\t"
               "v_cmp_ne_u32 vcc, %18, %8\n\ts_cbranch_vccz .Lr%=\n\t" TIME_UP("%19", ".Lr%=")
               "global_load_dword %8, %16, %12 sc1\n\ts_waitcnt vmcnt(0)\n\ts_branch .Lc%=\n"
               ".Lr%=:\n\tglobal_load_dwordx4 %0, %14, %9 offset:-3072 sc1\n\tglobal_load_dwordx4 %1, %14, %9 offset:-2048 sc1\n\t"
               "global_load_dwordx4 %2, %14, %9 offset:-1024 sc1\n\tglobal_load_dwordx4 %3, %14, %9 sc1\n\tglobal_load_dwordx4 %4, %14, %9 offset:1024 sc1\n\t"
               "global_load_dwordx4 %5, %14, %9 offset:2048 sc1\n\tglobal_load_dwordx4 %6, %14, %10 sc1\n\tglobal_load_dword %7, %15, %11 sc1\n\t")
               : "+v"(u.pp[0]), "+v"(u.pp[1]), "+v"(u.pp[2]), "+v"(u.pp[3]), "+v"(u.pp[4]), "+v"(u.pp[5]), "+v"(u.xp), "+v"(u.i2), "+v"(u.fw)
               : "s"(u.rp), "s"(u.rx), "s"(u.ri), "s"(u.rf), "s"(u.f2), "v"(l16), "v"(iv), "v"(pv), "v"(vt), "s"(tag), "s"(deadline), "s"(step) : "memory", "vcc", "scc");
}
__device__ __forceinline__ void reduce_sum(const Red& u, const u32x4& s0, const u32x4& s1, f32x4& v0, f32x4& v1, float (&x)[8]) {
  asm volatile(RED_M("%0", "%10", "%12", "0") RED_M("%1", "%11", "%12", "0") "v_lshlrev_b32 %2, 16, %18\n\tv_and_b32 %3, 0xffff0000, %18\n\t"
               RED_M("%0", "%10", "%13", "%0") RED_M("%1", "%11", "%13", "%1") "v_lshlrev_b32 %4, 16, %19\n\tv_and_b32 %5, 0xffff0000, %19\n\t"
               RED_M("%0", "%10", "%14", "%0") RED_M("%1", "%11", "%14", "%1") "v_lshlrev_b32 %6, 16, %20\n\tv_and_b32 %7, 0xffff0000, %20\n\t"
               RED_M("%0", "%10", "%15", "%0") RED_M("%1", "%11", "%15", "%1") "v_lshlrev_b32 %8, 16, %21\n\tv_and_b32 %9, 0xffff0000, %21\n\t"
               RED_M("%0", "%10", "%16", "%0") RED_M("%1", "%11", "%16", "%1") RED_M("%0", "%10", "%17", "%0") RED_M("%1", "%11", "%17", "%1")
               : "=&v"(v0), "=&v"(v1), "=&v"(x[0]), "=&v"(x[1]), "=&v"(x[2]), "=&v"(x[3]), "=&v"(x[4]), "=&v"(x[5]), "=&v"(x[6]), "=&v"(x[7])
               : "v"(s0), "v"(s1), "v"(u.pp[0]), "v"(u.pp[1]), "v"(u.pp[2]), "v"(u.pp[3]), "v"(u.pp[4]), "v"(u.pp[5]), "v"(u.xp[0]), "v"(u.xp[1]), "v"(u.xp[2]), "v"(u.xp[3]));
}
__device__ __forceinline__ void wait_store(int step) {
  asm volatile("s_cmp_lt_u32 %0, 4\n\ts_cbranch_scc1 .La%=\n\ts_waitcnt vmcnt(0)\n"
               COLD(".La%=", ".Lb%=", "s_waitcnt vmcnt(8)\n\ts_cmp_eq_u32 %0, 1\n\ts_cbranch_scc1 .Lb%=\n\ts_waitcnt vmcnt(1)\n\t") :: "s"(step) : "memory", "scc");
}

#define FIRST_STAMP if (wave == 3 && it < 2) STAMP_AT(56 + 4 * it + j);
#define STAMP_AT(I)
#define STAMP_IT()
#define STAMP_OUT()
#define TR_DECL()
#define TR(J, I)
#define TR_OUT()

template <bool Z> __device__ __forceinline__ void mf(f32x4& c, const bf16x8& a, const bf16x8& b) {
  if (Z) asm volatile("v_mfma_f32_16x16x32_bf16 %0, %1, %2, 0" : "=&v"(c) : "v"(a), "v"(b));
  else asm volatile("v_mfma_f32_16x16x32_bf16 %0, %1, %2, %0" : "+v"(c) : "v"(a), "v"(b));
}
__device__ __forceinline__ void mfc(f32x4& d, const bf16x8& a, const bf16x8& b, const f32x4& c) {
  asm volatile("v_mfma_f32_16x16x32_bf16 %0, %1, %2, %3" : "=&v"(d) : "v"(a), "v"(b), "v"(c));
}
template <int OFF, class T> __device__ __forceinline__ void lds_rd(T& d, unsigned a) { asm volatile("ds_read_b128 %0, %1 offset:%2" : "=v"(d) : "v"(a), "n"(OFF)); }
template <int OFF, class T> __device__ __forceinline__ void lds_rd8(T& d, unsigned a) { asm volatile("ds_read_b64 %0, %1 offset:%2" : "=v"(d) : "v"(a), "n"(OFF)); }
template <int OFF, class T> __device__ __forceinline__ void lds_wr(unsigned a, const T& d) { asm volatile("ds_write_b128 %0, %1 offset:%2" :: "v"(a), "v"(d), "n"(OFF) : "memory"); }
template <int OFF, class T> __device__ __forceinline__ void lds_wr8(unsigned a, const T& d) { asm volatile("ds_write_b64 %0, %1 offset:%2" :: "v"(a), "v"(d), "n"(OFF) : "memory"); }
template <int NK, int PD = PDG, int G = GRP> struct Ring { bf16x8 q[NK]; };
template <int O, int NK, int PD, int G> __device__ __forceinline__ void prime(Ring<NK, PD, G>& b, unsigned la) {
  static_for<0, PD>([&](auto kc) { lds_rd<O + decltype(kc)::value * 1024>(b.q[decltype(kc)::value], la); });
}
template <int O, int BAR, int VMB, int NK, int PD, int G, class F, class H> __device__ __forceinline__ void stream(Ring<NK, PD, G>& b, unsigned la, F&& f, H&& hook) {
  constexpr int NT = 36 / NK;
  static_assert((BAR < 0 || BAR + PD >= NK) && NK % G == 0, "barrier before the last read");
  if constexpr (BAR >= 0) prio<PR_B>();
  static_for<0, 36>([&](auto sc) {
    constexpr int s = decltype(sc)::value, c = s / NT, grp = s / (G * NT), in = s % (G * NT);
    constexpr int k = grp * G + in % G, t = (G > 1 && (grp & 1)) ? NT - 1 - in / G : in / G;
    constexpr int KZ = (BAR >= 0) ? BAR : NK - PD + 2;
    if constexpr (s == BAR * NT - ((BT >> (G == 1 ? 0 : NK == 18 ? 1 : 2)) & 1)) asm volatile("s_setprio 3\n\ts_waitcnt vmcnt(%0) lgkmcnt(0)" :: "n"(VMB) : "memory");
    if constexpr (s == BAR * NT) { asm volatile("" ::: "memory"); __builtin_amdgcn_s_barrier(); asm volatile("" ::: "memory"); }
    if constexpr (BAR < 0 && s == KZ * NT) wait_frag<0>();
    if constexpr (c < KZ && in < G) wait_frag<imin(NK - 1, PD - 1 + (s + NT - 1) / NT) - k>();
    f(IC<k>{}, IC<t>{}, b.q[k]);
    if constexpr (s % NT == 0 && c + PD < NK) lds_rd<O + (c + PD) * 1024>(b.q[c + PD], la);
    if constexpr (in == G * NT - 1) static_for<grp * G, grp * G + G>([&](auto kc) { f(kc, IC<NT>{}, b.q[decltype(kc)::value]); });
    if constexpr (s % NT == NT - 1) hook(IC<c>{});
  });
}
template <class F> __device__ __forceinline__ void periods(F&& f) {
#pragma nounroll
  for (int it = 0; it < 16; ++it) static_for<0, 4>([&](auto jc) { f(jc, it); });
}

__device__ __forceinline__ void max4(float (&s)[4], const f32x4& a, const f32x4& b) {
  asm volatile("v_max_f32 %0, %4, %8\n\tv_max_f32 %1, %5, %9\n\tv_max_f32 %2, %6, %10\n\tv_max_f32 %3, %7, %11" : "=&v"(s[0]), "=&v"(s[1]), "=&v"(s[2]), "=&v"(s[3])
               : "v"(a[0]), "v"(a[1]), "v"(a[2]), "v"(a[3]), "v"(b[0]), "v"(b[1]), "v"(b[2]), "v"(b[3]));
}
struct Soft { float s[4], t0, t1; unsigned mo[2]; };
__device__ __forceinline__ void soft1(Soft& f, float sc) {
  asm volatile("v_max3_f32 %4, %0, %1, %2\n\tv_max_f32 %5, %4, %3\n\tv_max_f32 %4, %4, %3\n\ts_nop 1\n\tv_permlane16_swap_b32 %4, %5\n\t"
               "v_max_f32 %4, %4, %5\n\tv_mov_b32 %5, %4\n\ts_nop 1\n\tv_permlane32_swap_b32 %4, %5\n\tv_max_f32 %4, %4, %5\n\tv_mul_f32 %4, %4, %6\n\t"
               "v_fma_f32 %0, %0, %6, -%4\n\tv_fma_f32 %1, %1, %6, -%4\n\tv_fma_f32 %2, %2, %6, -%4\n\tv_fma_f32 %3, %3, %6, -%4"
               : "+v"(f.s[0]), "+v"(f.s[1]), "+v"(f.s[2]), "+v"(f.s[3]), "=&v"(f.t0), "=&v"(f.t1) : "v"(sc));
}
#define SOFT2(R0, R1, R2, R3, R4, R5, R6, R7, PAD) \
  "v_exp_f32 %0, %0\n\t" R0 "v_exp_f32 %1, %1\n\t" R1 "v_exp_f32 %2, %2\n\t" R2 "v_exp_f32 %3, %3\n\t" R3 \
  "v_add_f32 %4, %0, %1\n\tv_add_f32 %5, %2, %3\n\t" R4 "v_add_f32 %4, %4, %5\n\tv_mov_b32 %5, %4\n\t" R5 PAD \
  "v_permlane16_swap_b32 %4, %5\n\t" R6 "v_add_f32 %4, %4, %5\n\tv_mov_b32 %5, %4\n\t" R7 PAD \
  "v_permlane32_swap_b32 %4, %5\n\tv_add_f32 %4, %4, %5\n\tv_rcp_f32 %4, %4"
#define SOFT_RD(N, K) "ds_read_b128 %" #N ", %14 offset:%15+" #K "\n\t"
template <int OFF> __device__ __forceinline__ void soft2(Soft& f, bf16x8 (&x)[18], unsigned la) {
  if constexpr (OFF < 0)
    asm volatile(SOFT2("", "", "", "", "", "", "", "", "s_nop 1\n\t") : "+v"(f.s[0]), "+v"(f.s[1]), "+v"(f.s[2]), "+v"(f.s[3]), "=&v"(f.t0), "=&v"(f.t1));
  else
    asm volatile(SOFT2(SOFT_RD(6, 0), SOFT_RD(7, 1024), SOFT_RD(8, 2048), SOFT_RD(9, 3072), SOFT_RD(10, 4096), SOFT_RD(11, 5120), SOFT_RD(12, 6144), SOFT_RD(13, 7168), "s_nop 0\n\t")
                 : "+v"(f.s[0]), "+v"(f.s[1]), "+v"(f.s[2]), "+v"(f.s[3]), "=&v"(f.t0), "=&v"(f.t1),
                   "=&v"(x[0]), "=&v"(x[1]), "=&v"(x[2]), "=&v"(x[3]), "=&v"(x[4]), "=&v"(x[5]), "=&v"(x[6]), "=&v"(x[7]) : "v"(la), "n"(OFF < 0 ? 0 : OFF));
}
#define QK_M(D, W, X, C) "v_mfma_f32_16x16x32_bf16 " D ", " W ", " X ", " C "\n\t"
#define QK_G3 "v_mul_f32 %4, %4, %8\n\t"
#define QK_G4 "v_mul_f32 %5, %5, %8\n\t"
#define QK_G5 "v_mul_f32 %6, %6, %8\n\t"
#define QK_G6 "v_mul_f32 %7, %7, %8\n\t"
#define QK_G7 "v_cvt_pk_bf16_f32 %8, %4, %5\n\t"
#define QK_G8 "v_cvt_pk_bf16_f32 %9, %6, %7\n\t"
#define QK_G9 "v_and_b32 %4, %8, %17\n\t"
#define QK_GA "v_and_b32 %5, %9, %18\n\t"
#define QK_GB "v_xor_b32 %6, %8, %4\n\t"
#define QK_GC "v_xor_b32 %7, %9, %5\n\t"
#define QK_OUT(A, B, C, D) "+v"(f.s[A]), "+v"(f.s[B]), "+v"(f.s[C]), "+v"(f.s[D]), "+v"(f.t0), "+v"(f.t1)
#define QK_PAIR(C0, C1, GA, GB, S) asm volatile(QK_M("%0", "%10", "%14", C0) "ds_read_b128 %2, %16 offset:%19\n\t" QK_M("%1", "%11", "%14", C1) GA \
    QK_M("%0", "%12", "%15", "%0") "ds_read_b128 %3, %16 offset:%20\n\t" QK_M("%1", "%13", "%15", "%1") GB \
    : "+v"(a0), "+v"(a1), "=&v"(y0), "=&v"(y1), QK_OUT(S, S + 1, 2 - S, 3 - S) \
    : "v"(w0[0]), "v"(w0[1]), "v"(w1[0]), "v"(w1[1]), "v"(x0), "v"(x1), "v"(la), "v"(f.mo[0]), "v"(f.mo[1]), "n"(OFF), "n"(OFF + 1024))
template <int P, int E, int OFF> __device__ __forceinline__ void qk_pair(f32x4& a0, f32x4& a1, const bf16x8 (&w0)[2], const bf16x8 (&w1)[2], bf16x8 x0, bf16x8 x1,
                                                                         bf16x8& y0, bf16x8& y1, unsigned la, Soft& f) {
  if constexpr (P == 0) QK_PAIR("0", "0", QK_G3, QK_G4, 0);
  if constexpr (P == 1) QK_PAIR("%0", "%1", QK_G5, QK_G6, 0);
  if constexpr (P == 2) QK_PAIR("%0", "%1", QK_G7, QK_G8, 0);
  if constexpr (P == 3) QK_PAIR("%0", "%1", QK_G9, QK_GA, 2 * E);
  if constexpr (P == 4) QK_PAIR("%0", "%1", QK_GB, QK_GC, 2 * E);
}
__device__ __forceinline__ void soft3(Soft& f) {
  int u = 0;
  asm volatile("s_nop 0\n\t" QK_G3 QK_G4 QK_G5 QK_G6 QK_G7 QK_G8 QK_G9 QK_GA QK_GB QK_GC : "+s"(u), "+s"(u), "+s"(u), "+s"(u), QK_OUT(0, 1, 2, 3)
               : "n"(0), "n"(0), "n"(0), "n"(0), "n"(0), "n"(0), "n"(0), "v"(f.mo[0]), "v"(f.mo[1]));
}
template <int E> __device__ __forceinline__ void mf8(f32x4& a0, f32x4& a1, const bf16x8 (&w0)[2], const bf16x8 (&w1)[2], const bf16x8 (&w2)[2], const bf16x8 (&w3)[2],
                                                     const bf16x8& x0, const bf16x8& x1, const bf16x8& x2, const bf16x8& x3, f32x2& st, f32x2& xk, f32x2& xq, unsigned sa, unsigned xa) {
  asm volatile(QK_M("%0", "%5", "%13", "%0") "ds_read_b64 %2, %17 offset:%19\n\tds_read_b64 %3, %18 offset:%20\n\tds_read_b64 %4, %18 offset:%21\n\t"
               QK_M("%1", "%6", "%13", "%1") QK_M("%1", "%8", "%14", "%1") QK_M("%0", "%7", "%14", "%0")
               QK_M("%1", "%10", "%15", "%1") QK_M("%0", "%9", "%15", "%0") QK_M("%1", "%12", "%16", "%1") QK_M("%0", "%11", "%16", "%0")
               : "+v"(a0), "+v"(a1), "=&v"(st), "=&v"(xk), "=&v"(xq)
               : "v"(w0[0]), "v"(w0[1]), "v"(w1[0]), "v"(w1[1]), "v"(w2[0]), "v"(w2[1]), "v"(w3[0]), "v"(w3[1]), "v"(x0), "v"(x1), "v"(x2), "v"(x3),
                 "v"(sa), "v"(xa), "n"(E * 128), "n"(E * 1024 + 512), "n"(E * 1024));
}

constexpr unsigned SPIN = 400000000u;
struct Ctx { char* ws; int g, wave, lane; unsigned dl; const char *fl, *fp, *code; unsigned long long w0; };
constexpr unsigned long long HALF = 0x0000FFFF0000FFFFull;

__device__ __forceinline__ unsigned get_tag(const char* ws) {
  unsigned nonce;
  asm volatile("s_load_dword %0, %1, 0x0\n\ts_waitcnt lgkmcnt(0)" : "=s"(nonce) : "s"(ws) : "memory");
  return make_tag(nonce);
}
template <int R, int NP, int NX = 0, class I, class Q, class P> __device__ __forceinline__ void load_w(char* smem, const Ctx& cx, unsigned& tag, I&& issue, Q&& rd, P&& then) {
  const int wave = cx.wave;
  if (wave == 3) STAMP_AT(72);
  static_for<0, 3>([&](auto rc) { issue(rc); });
  tag = get_tag(cx.ws);
  static_for<0, R>([&](auto rc) {
    constexpr int r = decltype(rc)::value;
    wait_vm<imin(2, R - 1 - r) * NP + (r == R - 2 ? NX : 0)>();
    bar_bare();
    if (wave == 3 && r < 6) STAMP_AT(73 + r);
    rd(rc);
    if constexpr (r + 3 < R || r + 1 == R) bar_raw();
    if constexpr (r + 3 < R) issue(IC<r + 3>{});
    if constexpr (r + 3 == R && NX == 0) { if (wave == ((blockIdx.x >> 4) & 7)) touch(cx.code, lds_addr(smem) + 162816); }
    then(rc);
  });
}
__device__ __forceinline__ bf16x8 asb(u32x4 v) { return __builtin_bit_cast(bf16x8, v); }
__device__ __forceinline__ bf16x8 ones8() { return asb(u32x4{0x3F803F80u, 0x3F803F80u, 0x3F803F80u, 0x3F803F80u}); }
__device__ __forceinline__ u32x4 widen(unsigned w0, unsigned w1) {
  const auto s0 = __builtin_amdgcn_permlane16_swap(w0, w0, false, false);
  const auto s1 = __builtin_amdgcn_permlane16_swap(w1, w1, false, false);
  return u32x4{s0[0], s1[0], s0[1], s1[1]};
}
__device__ __forceinline__ u32x4 pack8(const f32x4& a, const f32x4& b) { return u32x4{cvt2(a[0], a[1]), cvt2(a[2], a[3]), cvt2(b[0], b[1]), cvt2(b[2], b[3])}; }
__device__ __forceinline__ f32x2 row_stat(float sum, float sq) {
  const float mean = sum * (1.0f / 576.0f);
  const float var = fmaxf(sq * (1.0f / 576.0f) - mean * mean, 0.0f);
  return f32x2{-rbf(mean), __builtin_amdgcn_rcpf(rbf(__builtin_amdgcn_sqrtf(rbf(rbf(var) + 1e-5f))))};
}
__device__ __forceinline__ float diag(const f32x4& a, int p) { return (p == 0) ? a[0] : (p == 1) ? a[1] : (p == 2) ? a[2] : a[3]; }
__device__ __forceinline__ int top(int g) { return (1 - (g >> 1)) * 32; }

__device__ __forceinline__ void role_gu(char* smem, const u16* wg, const u16* wu, u16* out, const Ctx& cx, int c) {
  const int wave = cx.wave, lane = cx.lane;
  const unsigned sm0 = lds_addr(smem);
  unsigned tag;
  const int r = lane & 15, g = lane >> 4, q = r >> 2, p = lane & 3;
  constexpr int SUMS = 4 * TB;
  TR_DECL();
  bf16x8 W[18][2];
  u32x4 sel0, sel1;
#pragma unroll
  for (int e = 0; e < 2; ++e) {
    sel0[e] = sel1[2 + e] = (r == 4 * g + 2 * e ? 0x3F80u : 0u) | (r == 4 * g + 2 * e + 1 ? 0x3F800000u : 0u);
    sel0[2 + e] = sel1[e] = 0u;
  }
  tie(sel0, sel1);
  {
    f32x4 cs[2] = {{0.f, 0.f, 0.f, 0.f}, {0.f, 0.f, 0.f, 0.f}};
    constexpr int BUF = 48 * 1056;
    const char* src = (const char*)((wave >> 2) ? wu : wg) + c * 256;
    const unsigned vo = (4 * (g & 1) + 16 * (g >> 1)) * (FD * 2) + r * 16;
    const char* rb = smem + ((g & 1) * 4 + q) * 1056 + (g >> 1) * 512 + wave * 32 + p * 8;
    load_w<6, 6>(smem, cx, tag, [&](auto rc) {
      constexpr int rr = decltype(rc)::value;
#pragma unroll
      for (int j = 0; j < 6; ++j) {
        const int idx = 6 * (wave & 3) + j, id = idx & 7;
        dma_w2(src + (size_t)(32 * (3 * rr + (idx >> 3)) + 8 * (id >> 2) + (id & 3)) * (FD * 2), vo, sm0 + (rr % 3) * BUF + ((wave >> 2) * 24 + idx) * 1056);
      }
    }, [&](auto rc) {
      constexpr int rr = decltype(rc)::value;
#pragma unroll
      for (int kl = 0; kl < 3; ++kl)
#pragma unroll
        for (int m = 0; m < 2; ++m) { const char* a = rb + (rr % 3) * BUF + (m * 24 + kl * 8) * 1056; W[3 * rr + kl][m] = tr_frag(a, a + 256); }
      tie(W[3 * rr][0], W[3 * rr][1], W[3 * rr + 1][0], W[3 * rr + 1][1], W[3 * rr + 2][0], W[3 * rr + 2][1]);
    }, [&](auto rc) {
#pragma unroll
      for (int k = 0; k < 6; ++k) cs[k & 1] = MFMA(ones8(), W[3 * decltype(rc)::value + (k >> 1)][k & 1], cs[k & 1]);
      tie(cs[0], cs[1]);
    });
#pragma unroll
    for (int m = 0; m < 2; ++m) {
      const float cm = cs[m][0] * (-1.0f / 576.0f);
      const f32x4 c4 = {cm, cm, cm, cm};
#pragma unroll
      for (int k = 0; k < 18; ++k) { W[k][m] = asb(pack8(MFMA(asb(sel0), W[k][m], c4), MFMA(asb(sel1), W[k][m], c4))); tie(W[k][m]); }
    }
  }
  const char* hb = cx.ws + WS_H + (size_t)(cx.g * 6 + c / 6) * 64 * HB + (c % 6) * 4096 + wave * 512 - HB;
  const unsigned l16 = vopq(lane * 16), hv = vopq((g >> 1) * 256 + r * 16);
  const unsigned la[2] = {vopq(sm0 + l16), vopq(sm0 + l16 + 2 * TB)};
  f32x4 ag, au;
  Ring<18> bq;
  auto mm = [&](auto kc, auto jc, const bf16x8& x) {
    constexpr int k = decltype(kc)::value;
    if constexpr (decltype(jc)::value == 0) mf<k == 0>(ag, W[k][0], x);
    if constexpr (decltype(jc)::value == 1) mf<k == 0>(au, W[k][1], x);
  };
  auto epi = [&]() {
    tie(ag, au);
    return widen(cvt2(ag[0] * au[0], ag[1] * au[1]), cvt2(ag[2] * au[2], ag[3] * au[3]));
  };
  if (wave == 3) STAMP_AT(70);
  if (wave < 4) {
    const char* xs = cx.ws + WS_X1 + (size_t)cx.g * 64 * TB + wave * 4608;
    const char* fr = cx.fl + TLAG * 128;
    const unsigned pv = vopq((F_X1 + (lane & 1)) * FSTR), l4 = vopq(l16 + 4096), mb = sm0 + wave * 4608;
    auto fetch = [&](auto sc) { dma<decltype(sc)::value * TB, 4>(xs, l16, mb); dma<decltype(sc)::value * TB + 4096, 0>(xs, l4, mb); };
    unsigned dl = cx.dl, fw;
    int tgt = (wave == 1 && c < 32) ? c + 2 : 99;
    auto scale = [&]() {
      f32x2 t = *(const f32x2*)(smem + SUMS + lane * 8);
#pragma unroll
      for (int e = 0; e < 2; ++e) {
        const auto a = __builtin_amdgcn_permlane16_swap(f2i(t[e]), f2i(t[e]), false, false);
        const float h = u2f(a[0]) + u2f(a[1]);
        const auto b = __builtin_amdgcn_permlane32_swap(f2i(h), f2i(h), false, false);
        t[e] = u2f(b[0]) + u2f(b[1]);
      }
      const float is = row_stat(t[0], t[1])[1];
      gst4m<-1>(cx.ws + WS_IS + (cx.g * 64 + tgt - 2) * 128, lane * 4, f2i(is * is));
      tgt += 32;
    };
    poll(fr, pv, tag, dl); fetch(IC<0>{});
    poll(fr + 128, pv, tag, dl);
    adv(fr, 256); fw = vopq(~tag);
    adv(xs, TB); fetch(IC<1>{});
    prio<PR_A>();
    bar_bare();
    if (wave == 3) STAMP_AT(55);
    periods([&](auto jc, int it) {
      constexpr int j = decltype(jc)::value;
      prime<(j & 1) * TB>(bq, la[j >> 1]);
      if constexpr (FRG) gld4<j * 128>(fw, fr, pv);
      stream<(j & 1) * TB, -1, 0>(bq, la[j >> 1], mm, [&](auto kc) {
        constexpr int k = decltype(kc)::value;
        if constexpr (k == 0) TR(1, 1);
        if constexpr (k == 17) TR(1, 2);
        if constexpr (k == 11) adv(xs, TB);
        if constexpr (k == 12 && j == 2) dl = sopq((it == 15) ? 0u : dl);
        if constexpr (k == 13) adv(hb, HB);
      });
      prio<3>();
      check_flags<FRG ? 0 : 6, j * 128>(fw, fr, pv, tag, dl);
      TR(1, 3);
      if constexpr (!FRG) gld4<(j + 1) * 128>(fw, fr, pv);
      fetch(IC<(j + 2) & 3>{});
      TR(1, 4);
      gst16m(hb, hv, epi(), HALF);
      if constexpr (j == 3) adv(fr, 512);
      TR(1, 5);
      LWAIT(7);
      TR(1, 6);
      if (__builtin_expect(4 * it + j == tgt, 0)) scale();
      STAMP_IT();
      prio<PR_A>();
      bar_bare();
      TR(0, 0); TR(1, 7);
    });
    TR_OUT();
    if (c == 31) bar_bare();
    if (tgt < 66) scale();
    wait_vm<0>(); tie(fw);
    bar_bare();
    STAMP_OUT();
  } else {
    const int kb = 2 * (c % 9) + (wave & 1);
    int tau = 2 * (c / 9) + ((wave >> 1) & 1);
    Red u = {};
    const unsigned vt = vopq(tag), iv = vopq(r * 4), pq = vopq((F_P + (kb / 6) * 6 + min(lane, 5)) * FSTR), vrow = vopq(r * 2304 + g * 16);
    u.rp = cx.ws + WS_P + (size_t)((cx.g * 18 + kb) * 64 + tau) * PB + 3072;
    u.rx = cx.ws + WS_X1 + (size_t)(cx.g * 64 + tau) * TB + kb * 1024;
    u.ri = cx.ws + WS_IS + (cx.g * 64 + tau) * 128;
    u.rf = cx.fl + (tau + PLAG) * 128;
    u.f2 = cx.ws + WS_F2 + ((cx.g & 1) * 72 + top(cx.g) + tau - 8) * F2ROW + kb * 128;
    u.ob = cx.ws + WS_X2 + (size_t)((cx.g & 1) * 64 + top(cx.g) + tau) * TB + kb * 1024;
    const char* ob2 = (const char*)out + (size_t)((cx.g >> 1) * 1024 + 32 * (31 - tau) + (cx.g & 1)) * (DM * 2) + kb * 64;
    gst16m(u.f2 + 8 * F2ROW, (lane >> 3) * (8 * F2ROW) + (lane & 7) * 16, u32x4{~tag, ~tag, ~tag, ~tag}, 0xFFFFFFFFull);
    auto ask = [&](int step) { reduce_ask(u, l16, iv, pq, vt, tag, cx.dl, step); };
    auto finish = [&](int tau_) {
      f32x4 v0, v1; float x[8];
      reduce_sum(u, sel0, sel1, v0, v1, x);
      adv(u.rp, 0); adv(u.rx, 0); adv(u.ri, 0); adv(u.rf, 0); adv(u.f2, 0); adv(u.ob, 0);
      const int tau = sopq(tau_);
      const char* o = u.ob;
      const unsigned ov = vopq((tau >= 32) ? vrow : l16);
      adv(u.rp, 8 * PB); adv(u.rx, 8 * TB); adv(u.ri, 8 * 128); adv(u.rf, 8 * 128);
      adv(u.f2, (tau < 32) ? 8 * F2ROW : 0);
      u.ob = ((tau >> 3) == 3) ? ob2 : u.ob + ((tau < 32) ? 8l * TB : -8l * 32 * DM * 2); adv(u.ob, 0);
      tie(v0, v1);
      const float sc = u2f(u.i2);
      gst16(o, ov, u32x4{cvt2(fmaf(v0[0], sc, x[0]), fmaf(v0[1], sc, x[1])), cvt2(fmaf(v0[2], sc, x[2]), fmaf(v0[3], sc, x[3])),
                         cvt2(fmaf(v1[0], sc, x[4]), fmaf(v1[1], sc, x[5])), cvt2(fmaf(v1[2], sc, x[6]), fmaf(v1[3], sc, x[7]))});
    };
    int step = -1, tgt = (c < 32) ? c : 99;
    const char* fp = cx.fp - 512;
    bar_bare();
    prime<0>(bq, la[0]);
    wait_lg<0>();
    periods([&](auto jc, int it) {
      constexpr int j = decltype(jc)::value, j1 = (j + 1) & 3;
      stream<(j & 1) * TB, BARU, 63>(bq, la[j >> 1], mm, [&](auto kc) {
        constexpr int k = decltype(kc)::value;
        if constexpr (k == 0) TR(1, 6);
        if constexpr (k == BARU - 2) TR(1, 7);
        if constexpr (k == BARU) { TR(0, 0); TR(1, 8); }
        if constexpr (k == 9 && j == 0) adv(fp, 512);
        if constexpr (k == 10) adv(hb, HB);
        if constexpr (HLAG == 2 && k == BARU - 1 - ((BT >> 1) & 1)) wait_store(step);
      });
      LWAIT(0);
      flag_by<(j + 1) * 128>(cx.w0, fp, l16, vt);
      asm volatile("s_nop 3");
      u32x4 h = epi();
      TR(0, 2);
      gst16m(hb, hv, h, HALF);
      step = 4 * it + j - LAGF - (tau | 1);
      ask(step);
      TR(0, 3);
      if (__builtin_expect(step == 2, 0)) finish(tau);
      tau += (step == 3) ? 8 : 0;
      if (__builtin_expect(4 * it + j == tgt, 0)) {
        f32x4 s1 = {0.f, 0.f, 0.f, 0.f}, s2 = s1;
        bf16x8 x[5];
#pragma unroll
        for (int k = 0; k < 5; ++k) x[k] = *(const bf16x8*)(smem + j * TB + (k < 4 ? 4 * (wave - 4) + k : 16 + (wave & 1)) * 1024 + lane * 16);
        if (wave >= 6) x[4] = bf16x8{0, 0, 0, 0, 0, 0, 0, 0};
#pragma unroll
        for (int k = 0; k < 5; ++k) { s1 = MFMA(ones8(), x[k], s1); s2 = MFMA(x[k], x[k], s2); }
        if (g == q) *(f32x2*)(smem + SUMS + (wave - 4) * 128 + r * 8) = f32x2{s1[0], diag(s2, p)};
        tgt += 32;
      }
      TR(0, 4);
      prio<0>();
      prime<(j1 & 1) * TB>(bq, la[j1 >> 1]);
      TR(0, 5);
      if constexpr (j == 3) wait_lg<0>();
    });
    TR_OUT();
    if (c == 31) bar_bare();
    wait_vm<0>();
    bar_bare();
    flag_by<640>(cx.w0, fp, l16, vt);
    if (step == 2) tau += 8;
#pragma nounroll
    while (tau < 64) {
      ask(0); wait_vm<0>(); ask(1); wait_vm<0>(); ask(2);
      finish(tau); tau += 8;
    }
  }
}

__device__ __forceinline__ void role_dn(char* smem, const u16* wd, const Ctx& cx, int ks, int ns) {
  const int wave = cx.wave, lane = cx.lane;
  const unsigned sm0 = lds_addr(smem);
  unsigned tag;
  const int r = lane & 15, g = lane >> 4, q = r >> 2, p = lane & 3;
  const int kq = wave >> 2, nq = wave & 3;
  constexpr int EX = 4 * HB;
  TR_DECL();
  bf16x8 W[12][3];
  {
    constexpr int BUF = 49152;
    const char* src = (const char*)wd + (size_t)(768 * ks) * (DM * 2) + ns * 384;
    unsigned vo[6];
#pragma unroll
    for (int j = 0; j < 6; ++j) {
      const unsigned ch = (wave * 6 + j) * 64 + lane, row = ch / 24;
      vo[j] = ((row >> 6) * 384 + (row & 63)) * (DM * 2) + (ch - row * 24) * 16;
    }
    const char* rb = smem + (kq * 64 + 8 * g + q) * 384;
    load_w<6, 6>(smem, cx, tag, [&](auto rc) {
      constexpr int rr = decltype(rc)::value;
#pragma unroll
      for (int j = 0; j < 6; ++j) dma_w2(src + rr * 64 * (DM * 2), vo[j], sm0 + (rr % 3) * BUF + (wave * 6 + j) * 1024);
    }, [&](auto rc) {
      constexpr int rr = decltype(rc)::value;
#pragma unroll
      for (int kl = 0; kl < 2; ++kl)
#pragma unroll
        for (int m = 0; m < 3; ++m) {
          const char* a = rb + (rr % 3) * BUF + kl * 32 * 384 + ((m < 2) ? nq * 64 + p * 16 + m * 8 : 256 + nq * 32 + p * 8);
          W[2 * rr + kl][m] = tr_frag(a, a + 4 * 384);
        }
      tie(W[2 * rr][0], W[2 * rr][1], W[2 * rr][2], W[2 * rr + 1][0], W[2 * rr + 1][1], W[2 * rr + 1][2]);
    }, [](auto) {});
  }
  const unsigned l16 = vopq(lane * 16);
  const unsigned la[2] = {vopq(sm0 + l16 + kq * 12288), vopq(sm0 + l16 + kq * 12288 + 2 * HB)};
  const unsigned ea = vopq(sm0 + EX + nq * 3072 + l16);
  f32x4 acc[3];
  auto mm = [&](auto kc, auto jc, const bf16x8& x) {
    constexpr int k = decltype(kc)::value;
    if constexpr (decltype(jc)::value < 3) mf<k == 0>(acc[decltype(jc)::value % 3], W[k][decltype(jc)::value % 3], x);
  };
  if (wave == 3) STAMP_AT(70);
  if (wave < 4) {
    Ring<12, PDD, GRD> bq;
    const char* hs = cx.ws + WS_H + (size_t)(cx.g * 6 + ks) * 64 * HB + nq * 6144;
    const char* fr = cx.fl + HLAG * 128;
    const unsigned l3 = vopq(l16 + 3072), mb = sm0 + nq * 6144, pv = vopq((F_H + 6 * ks + min(lane, 5)) * FSTR);
    auto fetch = [&](auto sc) { dma<decltype(sc)::value * HB, 3>(hs, l16, mb); dma<decltype(sc)::value * HB + 3072, 3>(hs, l3, mb); };
    const unsigned lm[3] = {l16, vopq(l16 + 2048), vopq(l16 + 4096)};
    unsigned dl = cx.dl, fw;
    poll(fr, pv, tag, dl); fetch(IC<0>{});
    poll(fr + 128, pv, tag, dl);
    adv(fr, 256); fw = vopq(~tag);
    adv(hs, HB); fetch(IC<1>{});
    prio<PR_A>();
    bar_bare();
    if (wave == 3) STAMP_AT(55);
    periods([&](auto jc, int it) {
      constexpr int j = decltype(jc)::value;
      prime<(j & 1) * HB>(bq, la[j >> 1]);
      if constexpr (FRD) gld4<j * 128>(fw, fr, pv);
      stream<(j & 1) * HB, -1, 0>(bq, la[j >> 1], mm, [&](auto kc) {
        constexpr int k = decltype(kc)::value;
        if constexpr (k == 0) TR(1, 1);
        if constexpr (k == 11) TR(1, 2);
        if constexpr (k == 6) adv(hs, HB);
        if constexpr (k == 7 && j == 2) dl = sopq((it == 15) ? 0u : dl);
      });
      prio<3>();
      check_flags<FRD ? 0 : 6, j * 128>(fw, fr, pv, tag, dl);
      TR(1, 6);
      if constexpr (!FRD) gld4<(j + 1) * 128>(fw, fr, pv); else asm volatile("s_nop 1");
      prio<EXP>();
      static_for<0, 3>([&](auto mc) {
        constexpr int m = decltype(mc)::value;
        dma<((j + 2) & 3) * HB + m * 2048, 2>(hs, lm[m], mb);
        lds_wr<(j & 1) * 12288 + m * 1024>(ea, acc[m]);
      });
      STAMP_IT();
      prio<PR_A>();
      TR(1, 3);
      if constexpr (j == 3) adv(fr, 512);
      LWAIT(7);
      TR(1, 7);
      bar_raw();
      TR(0, 0); TR(1, 4);
    });
    TR_OUT();
    bar_bare();
    wait_vm<0>(); tie(fw);
    STAMP_OUT();
  } else {
    Ring<12, PDB, GRB> bq;
    const char* pd = cx.ws + WS_P + (size_t)(cx.g * 18 + 6 * ns + nq) * 64 * PB + ks * 1024 - PB;
    const unsigned hv = vopq((g >> 1) * 256 + r * 16 + (4 + (nq >> 1) - nq) * 64 * PB + (nq & 1) * 512), vt = vopq(tag);
    const char* fp = cx.fp - 512;
    bar_bare();
    prime<0>(bq, la[0]);
    wait_lg<0>();
    periods([&](auto jc, int it) {
      constexpr int j = decltype(jc)::value, j1 = (j + 1) & 3;
      f32x4 e[3];
      stream<(j & 1) * HB, BARD, VMBX(2)>(bq, la[j >> 1], mm, [&](auto kc) {
        constexpr int k = decltype(kc)::value;
        if constexpr (k == 0) TR(1, 6);
        if constexpr (k == BARD - 1) TR(1, 7);
        if constexpr (k == BARD) { TR(0, 0); TR(1, 8); }
        if constexpr (k == 5 && j == 0) adv(fp, 512);
        if constexpr (k == 6) adv(pd, PB);
        if constexpr (k == EK) static_for<0, 3>([&](auto mc) { constexpr int m = decltype(mc)::value; lds_rd<(j & 1) * 12288 + m * 1024>(e[m], ea); });
      });
      TR(0, 1);
      flag_by<(j + 1) * 128>(cx.w0, fp, l16, vt);
      asm volatile("s_nop 3");
      wait_lg<0>();
      TR(0, 2);
      tie(acc[0], acc[1], acc[2], e[0], e[1], e[2]);
      gst16(pd, l16, pack8(e[0] + acc[0], e[1] + acc[1]));
      TR(0, 3);
      e[2] += acc[2];
      gst16m(pd, hv, widen(cvt2(e[2][0], e[2][1]), cvt2(e[2][2], e[2][3])), HALF);
      TR(0, 4);
      prio<0>();
      prime<(j1 & 1) * HB>(bq, la[j1 >> 1]);
      TR(0, 5);
      if constexpr (j == 3) wait_lg<0>();
    });
    TR_OUT();
    wait_vm<0>();
    bar_bare();
    flag_by<640>(cx.w0, fp, l16, vt); flag_by<768>(cx.w0, fp, l16, vt);
  }
}

template <bool A> __device__ __forceinline__ void role_wo(char* smem, const u16* wo, const Ctx& cx, int jw) {
  const int wave = cx.wave, lane = cx.lane;
  const unsigned sm0 = lds_addr(smem);
  unsigned tag;
  const int r = lane & 15, g = lane >> 4, q = r >> 2, p = lane & 3;
  constexpr int RS = 4 * TB, RT = 10240, EX = RS + 4 * RT;
  TR_DECL();
  bf16x8 W[18][2], WX[9];
  {
    constexpr int BUF = 40960;
    const char* src = (const char*)wo + jw * 576;
    unsigned vo[5], vx[5];
#pragma unroll
    for (int i = 0; i < 5; ++i) {
      const unsigned ch = min((wave * 5 + i) * 64 + lane, 2303), row = ch / 36, cc = (ch - row * 36) * 16;
      const unsigned kl = row >> 5, gg = (row >> 3) & 3, e = row & 7;
      vo[i] = (36 * kl + 20 * (e >> 2) + 4 * gg + (e & 3)) * (DM * 2) + cc;
      vx[i] = (72 * (4 * kl + gg) + 16 + 36 * (e >> 2) + (e & 3)) * (DM * 2) + cc;
    }
    const char* rb = smem + (8 * g + q) * 576 + p * 16;
    load_w<9, 5>(smem, cx, tag, [&](auto rc) {
      constexpr int rr = decltype(rc)::value;
#pragma unroll
      for (int i = 0; i < 5; ++i) dma_w(src + (rr < 8 ? rr * 72 * (DM * 2) : 0), rr < 8 ? vo[i] : vx[i], sm0 + (rr % 3) * BUF + (wave * 5 + i) * 1024);
    }, [&](auto rc) {
      constexpr int rr = decltype(rc)::value;
      bf16x8 wx[2];
#pragma unroll
      for (int kl = 0; kl < 2; ++kl) {
        const char* a = rb + (rr % 3) * BUF + kl * 32 * 576;
#pragma unroll
        for (int m = 0; m < 2; ++m) W[2 * rr + kl][m] = tr_frag(a + wave * 64 + m * 8, a + wave * 64 + m * 8 + 4 * 576);
        if constexpr (A) wx[kl] = tr_frag(a + 512 + (wave & 1) * 8, a + 512 + (wave & 1) * 8 + 4 * 576);
      }
      tie(W[2 * rr][0], W[2 * rr][1], W[2 * rr + 1][0], W[2 * rr + 1][1]);
      if constexpr (A) {
#pragma unroll
        for (int kl = 0; kl < 2; ++kl) {
          const int k = 2 * rr + kl;
          if (k < 9) WX[k % 9] = wx[kl]; else if (wave & 2) WX[k % 9] = wx[kl];
          tie(WX[k % 9]);
        }
      }
    }, [](auto) {});
  }
  const unsigned l16 = vopq(lane * 16), l3 = vopq(l16 + 3072);
  const unsigned la[2] = {vopq(sm0 + l16), vopq(sm0 + l16 + 2 * TB)};
  const unsigned ra = vopq(sm0 + RS + wave * 1024 + l16), mr = sm0 + RS + wave * 1024;
  const char* xo = cx.ws + WS_X1 + (size_t)cx.g * 64 * TB + jw * 9216 + wave * 1024 - TB;
  const char* rs = cx.ws + WS_X0 + (size_t)((cx.g & 1) * 64 + top(cx.g)) * TB + (9 * jw + wave) * 1024;
  constexpr long RSW = TB - 32l * TB - (long)(WS_X0 - WS_X2);
  poll(cx.ws + WS_F2 + ((cx.g & 1) * 72 + top(cx.g)) * F2ROW, F_X0 * 128 + (lane & 1) * F2ROW, tag, cx.dl);
  f32x4 a0, a1, ax;
  Ring<18, PDG, GRW> bq;
  auto init = [&](const u32x4& x) {
    a0 = f32x4{blo(x[0]), bhi(x[0]), blo(x[1]), bhi(x[1])}; a1 = f32x4{blo(x[2]), bhi(x[2]), blo(x[3]), bhi(x[3])};
    tie(a0, a1);
  };
  const unsigned l8 = vopq(l16 + (8 - wave) * 1024), r8 = vopq(ra + 8192 + (wave & 1) * 8);
  auto init_x = [&](const u32x2& x) { ax = f32x4{blo(x[0]), bhi(x[0]), blo(x[1]), bhi(x[1])}; tie(ax); };
  dma<0, 1>(rs, l16, mr); if (wave < 2) dma<8192, 1>(rs, l8, mr);
  adv(rs, TB);
  dma<RT, 1>(rs, l16, mr); if (wave < 2) dma<RT + 8192, 1>(rs, l8, mr);
  wait_vm<0>();
  init(*(const u32x4*)(smem + RS + wave * 1024 + lane * 16));
  if constexpr (A) init_x(*(const u32x2*)(smem + RS + 8192 + (wave & 1) * 1032 + lane * 16));
  if (wave == 3) STAMP_AT(70);
  if constexpr (A) {
    const unsigned ea = vopq(sm0 + EX + wave * 1024 + l16);
    prio<PR_A>();
    bar_bare();
    if (wave == 3) STAMP_AT(55);
    auto loop = [&](auto hc) {
      constexpr int KH = decltype(hc)::value;
      auto mm = [&](auto kc, auto jc, const bf16x8& x) {
        constexpr int k = decltype(kc)::value;
        if constexpr (decltype(jc)::value == 0) mf<false>(a0, W[k][0], x);
        if constexpr (decltype(jc)::value == 1) mf<false>(a1, W[k][1], x);
        if constexpr (decltype(jc)::value == 2 && k / 9 == KH) mf<k == 9>(ax, WX[k % 9], x);
      };
      periods([&](auto jc, int it) {
        constexpr int j = decltype(jc)::value;
        u32x4 x; u32x2 x8;
        prime<(j & 1) * TB>(bq, la[j >> 1]);
        stream<(j & 1) * TB, -1, 0>(bq, la[j >> 1], mm, [&](auto kc) {
          constexpr int k = decltype(kc)::value;
          if constexpr (k == 0) TR(1, 1);
          if constexpr (k == 17) TR(1, 2);
          if constexpr (k == 12) adv(rs, (j == 2 && it == 7) ? RSW : TB);
          if constexpr (k == 13) adv(xo, TB);
          if constexpr (k == 16) { LWAIT(0); lds_rd<((j + 1) & 3) * RT>(x, ra); }
          if constexpr (KH == 0 && k == 17) lds_rd8<((j + 1) & 3) * RT>(x8, r8);
        });
        prio<3>();
        dma<((j + 2) & 3) * RT, 1>(rs, l16, mr);
        if constexpr (KH == 0) dma<((j + 2) & 3) * RT + 8192, 1>(rs, l8, mr);
        asm volatile("s_nop 3" : "+v"(a0), "+v"(a1), "+v"(ax) :: "memory");
        gst16(xo, l16, pack8(a0, a1));
        asm volatile("s_waitcnt lgkmcnt(0)" : "+v"(x) :: "memory");
        init(x);
        prio<0>();
        lds_wr<(j & 1) * 4096>(ea, ax);
        if constexpr (KH == 0) { asm volatile("s_waitcnt lgkmcnt(0)" : "+v"(x8) :: "memory"); init_x(x8); }
        STAMP_IT();
        prio<PR_A>();
        TR(1, 3);
        bar_raw();
        TR(0, 0); TR(1, 4);
      });
      wait_vm<0>();
      bar_bare();
      STAMP_OUT();
      TR_OUT();
    };
    if (wave < 2) loop(IC<0>{}); else loop(IC<1>{});
  } else {
    const int b = (wave == 4) ? 0 : wave - 5;
    const char* os = cx.ws + WS_O + (size_t)(cx.g * 68 + 3) * TB + b * 6144;
    const unsigned mb = sm0 + b * 6144, ea = vopq(sm0 + EX + l16), pv = vopq((F_O + min(lane, 23)) * FSTR);
    auto mm = [&](auto kc, auto jc, const bf16x8& x) {
      constexpr int k = decltype(kc)::value;
      if constexpr (decltype(jc)::value == 0) mf<false>(a0, W[k][0], x);
      if constexpr (decltype(jc)::value == 1) mf<false>(a1, W[k][1], x);
    };
    auto loop = [&](auto pc) {
      constexpr bool SH = decltype(pc)::value;
      auto fetch = [&](auto sc) { dma<decltype(sc)::value * TB, 3>(os, l16, mb); dma<decltype(sc)::value * TB + 3072, 3>(os, l3, mb); };
      const char *fr = cx.fl + OLAG * 128, *fp = cx.fp - 512;
      const unsigned vt = vopq(tag);
      unsigned dl = cx.dl;
      if constexpr (!SH) { poll(fr, pv, tag, dl); fetch(IC<0>{}); wait_vm<0>(); }
      bar_bare();
      if constexpr (!SH) { poll(fr + 128, pv, tag, dl); adv(os, TB); fetch(IC<1>{}); }
      adv(fr, 256);
      unsigned fw;
      if constexpr (!SH && FKW < 0) gld4<0>(fw, fr, pv);
      prime<0>(bq, la[0]);
      wait_lg<0>();
      periods([&](auto jc, int it) {
        constexpr int j = decltype(jc)::value, j1 = (j + 1) & 3;
        u32x4 x; f32x4 e[4];
        TR(1, 5);
        stream<(j & 1) * TB, BARG, VMBX(SH ? 0 : 1)>(bq, la[j >> 1], mm, [&](auto kc) {
          constexpr int k = decltype(kc)::value;
          if constexpr (k == 0) TR(1, 6);
          if constexpr (k == 13) TR(1, 7);
          if constexpr (k == 14) { TR(0, 0); TR(1, 8); }
          if constexpr (!SH && k == FKW) gld4<j * 128>(fw, fr, pv);
          if constexpr (!SH && k == 10 && j == 2) dl = sopq((it == 15) ? 0u : dl);
          if constexpr (SH && k == 10 && j == 0) adv(fp, 512);
          if constexpr (SH && k == BARG) gst4m<1, (j + 1) * 128>(fp, l16, vt);
          if constexpr (k == 11) adv(rs, (j == 2 && it == 7) ? RSW : TB);
          if constexpr (k == 12) adv(xo, TB);
          if constexpr (!SH && k == 9) adv(os, (j == 2 && it == 7) ? 2 * TB : TB);
        });
        TR(0, 1);
        if constexpr (!SH) { check_flags<0, j * 128>(fw, fr, pv, tag, dl); fetch(IC<(j + 2) & 3>{}); }
        dma<((j + 2) & 3) * RT, 1>(rs, l16, mr);
        TR(0, 2);
        prio<0>();
        if constexpr (SH) {
          static_for<0, 4>([&](auto mc) { constexpr int m = decltype(mc)::value; lds_rd<(j & 1) * 4096 + m * 1024>(e[m], ea); });
        }
        lds_rd<j1 * RT>(x, ra);
        prio<3>();
        tie(a0, a1);
        gst16(xo, l16, pack8(a0, a1));
        TR(0, 3);
        asm volatile("s_waitcnt lgkmcnt(0)" : "+v"(x) :: "memory");
        if constexpr (SH) {
          tie(e[0], e[1], e[2], e[3]);
          gst16(xo + 3072, l16, pack8(e[0] + e[2], e[1] + e[3]));
        }
        init(x);
        TR(0, 4);
        prio<0>();
        prime<(j1 & 1) * TB>(bq, la[j1 >> 1]);
        if constexpr (j == 1) adv(fr, (it == 7) ? 128 : 0);
        if constexpr (!SH && FKW < 0) gld4<(j + 1) * 128>(fw, fr, pv);
        if constexpr (j == 3) { adv(fr, 512); wait_lg<0>(); }
      });
      wait_vm<0>();
      if constexpr (!SH) tie(fw);
      bar_bare();
      if constexpr (SH) gst4m<1, 640>(fp, l16, vt);
      TR_OUT();
    };
    if (wave == 5) loop(std::true_type{}); else loop(std::false_type{});
  }
}

template <class F> __device__ __forceinline__ void qa_periods(F&& f) {
#pragma nounroll
  for (int it = 0; it < 16; ++it) static_for<0, 4>([&](auto jc) { f(jc, IC<0>{}, it); });
  static_for<0, 4>([&](auto jc) { f(jc, IC<1>{}, 16); });
}
template <bool A> __device__ __forceinline__ void role_qa(char* smem, const u16* wq, const Ctx& cx, int jq) {
  const int wave = cx.wave, lane = cx.lane;
  const unsigned sm0 = lds_addr(smem);
  unsigned tag;
  const int r = lane & 15, g = lane >> 4, q = r >> 2, p = lane & 3;
  constexpr int XR = 4 * TB, PP = XR + 2048, ST = PP + 8192;
  const f32x4 z4 = {0.f, 0.f, 0.f, 0.f};
  const unsigned l16 = vopq(lane * 16);
  const bool low = cx.g < 2;
  TR_DECL();
  const char* xs = cx.ws + WS_X0 + (size_t)((cx.g & 1) * 64 + top(cx.g)) * TB + (wave & 3) * 4096;
  const char* fr = cx.ws + WS_F2 + ((cx.g & 1) * 72 + top(cx.g)) * F2ROW;
  const unsigned mb = sm0 + (wave & 3) * 4096, l4 = vopq(l16 + 4096);
  auto fetch = [&](auto sc, auto nc) {
    dma<decltype(sc)::value * TB, 4>(xs, l16, mb);
    if constexpr (decltype(nc)::value == 6) dma<decltype(sc)::value * TB + 4096, 2>(xs, l4, mb);
  };
  bf16x8 W[18][2];
  f32x4 cs[2] = {z4, z4};
  bf16x8 one = ones8();
  tie(one);
  {
    constexpr int BUF = 41472, B0 = 2 * TB;
    const char* src = (const char*)wq + jq * 144;
    unsigned vo[6];
#pragma unroll
    for (int i = 0; i < 6; ++i) {
      const unsigned ch = min((i < 5 ? wave * 5 + i : 40) * 64 + lane, 2591), row = ch / 27, cc = ch - row * 27, sec = cc / 9;
      vo[i] = row * (NQKV * 2) + sec * (DM * 2 - 144) + cc * 16;
    }
    int c0, c1;
    if (!A) { c0 = 36 * (wave & 3) + (wave & 1) * 4 + p * 8; c1 = c0 + 144; }
    else if (wave < 2) { c0 = 288 + 72 * wave + p * 8; c1 = c0 + 40; }
    else { c0 = (p >> 1) * 144 + 32 + (p & 1) * 72; c1 = 288 + 32 + (p & 1) * 72; }
    const char* rb = smem + B0 + (8 * g + q) * 432;
    unsigned f0; const unsigned f0v = vopq(F_X0 * 128 + (lane & 3) * F2ROW);
    load_w<6, 6, 5>(smem, cx, tag, [&](auto rc) {
      constexpr int rr = decltype(rc)::value;
      if constexpr (rr == F0R) gld4<0>(f0, fr, f0v);
#pragma unroll
      for (int i = 0; i < 5; ++i) dma_w(src + rr * 96 * (NQKV * 2), vo[i], sm0 + B0 + (rr % 3) * BUF + (wave * 5 + i) * 1024);
      dma_wh(src + rr * 96 * (NQKV * 2), vo[5], sm0 + B0 + (rr % 3) * BUF + 40960);
    }, [&](auto rc) {
      constexpr int rr = decltype(rc)::value;
#pragma unroll
      for (int kl = 0; kl < 3; ++kl)
#pragma unroll
        for (int m = 0; m < 2; ++m) {
          const char* a = rb + (rr % 3) * BUF + kl * 32 * 432 + (m ? c1 : c0);
          W[3 * rr + kl][m] = tr_frag(a, a + 4 * 432);
        }
      tie(W[3 * rr][0], W[3 * rr][1], W[3 * rr + 1][0], W[3 * rr + 1][1], W[3 * rr + 2][0], W[3 * rr + 2][1]);
    }, [&](auto rc) {
      if constexpr (decltype(rc)::value == 3) {
        check_flags<6 * (6 - F0R)>(f0, fr, f0v, tag, cx.dl);
        const char* x2 = xs - (wave & 3) * 4096 + wave * 4608;
        dma<0, 4>(x2, l16, sm0 + wave * 4608); dma<4096, 0>(x2, l4, sm0 + wave * 4608);
      }
#pragma unroll
      for (int k = 0; k < 6; ++k) cs[k & 1] = MFMA(one, W[3 * decltype(rc)::value + (k >> 1)][k & 1], cs[k & 1]);
      tie(cs[0], cs[1]);
    });
  }
  auto quad = [&](const f32x4& c) {
    f32x4 o;
#pragma unroll
    for (int j = 0; j < 4; ++j) o[j] = u2f(__builtin_amdgcn_ds_bpermute((4 * g + j) * 4, f2i(c[0])));
    return o;
  };
  const unsigned la[2] = {vopq(sm0 + l16), vopq(sm0 + l16 + 2 * TB)};
  if constexpr (A) {
    Ring<18, PDG, GRQ> bq;
    unsigned pv = vopq(F_X0 * 128), dl = cx.dl, fw = vopq(tag);
    adv(fr, 2 * F2ROW); adv(xs, 2 * TB);
    auto feed = [&](auto jc, auto nc, int it) {
      constexpr int j = decltype(jc)::value;
      const char* xn = xs + TB;
      adv(fr, F2ROW);
      if constexpr (j == 1) { const bool st = low && it == 7; dl = st ? 0u : dl; xn = st ? xs : xn; }
      if constexpr (j == 2) {
        const bool st = low && it == 15; dl = st ? 0u : cx.dl; xn = st ? xs : xn;
        if (it == 7) { xn = cx.ws + WS_X2 + (size_t)((cx.g & 1) * 64 + top(cx.g)) * TB + wave * 4096; fr -= 33 * F2ROW; pv = vopq(min(lane, 17) * 128); }
      }
      gld4<0>(fw, fr, pv);
      fetch(IC<(j + 2) & 3>{}, nc);
      xs = xn; adv(xs, 0);
    };
    if (wave == 3) {
      STAMP_AT(70);
      f32x4 a0, a1;
      const unsigned sa = vopq(sm0 + ST + ((g == q) ? r * 8 : 256));
      auto mm = [&](auto kc, auto jc, const bf16x8& x) {
        constexpr int k = decltype(kc)::value;
        if constexpr (decltype(jc)::value == 0) mf<k == 0>(a0, one, x);
        if constexpr (decltype(jc)::value == 1) mf<k == 0>(a1, x, x);
      };
      prio<PR_A>();
      bar_raw();
      STAMP_AT(55);
      qa_periods([&](auto jc, auto ec, int it) {
        constexpr int j = decltype(jc)::value; constexpr bool END = decltype(ec)::value;
        if constexpr (!END || j < 2) {
          prime<(j & 1) * TB>(bq, la[j >> 1]);
          stream<(j & 1) * TB, -1, 0>(bq, la[j >> 1], mm, [&](auto kc) {
            constexpr int k = decltype(kc)::value;
            if constexpr (k == 0) TR(1, 1);
            if constexpr (k == 17) TR(1, 4);
          });
          prio<0>();
          if constexpr (!END) { check_flags<6>(fw, fr, pv, tag, dl); feed(jc, IC<6>{}, it); }
          else { wait_vm<0>(); tie(fw); settle(); }
          tie(a0, a1);
          lds_wr8<(j & 1) * 128>(sa, row_stat(a0[0], diag(a1, p)));
          prio<PR_A>();
          if constexpr (!END) LWAIT(7);
        }
        STAMP_IT();
        TR(1, 6);
        bar_raw();
        TR(0, 0); TR(1, 7);
      });
      TR_OUT();
      STAMP_OUT();
      if (cx.g == 0 && jq == 0) {
        for (int g2 = 0; g2 < 4; ++g2) poll(cx.ws + WS_FL + g2 * FGRP + 128, lane * FSTR, tag, cx.dl);
        gst4m<1>(cx.ws, l16, vopq((tag - 0x7F4A7C15u) * 244002641u + 1u));
      }
    } else {
      const unsigned sa = vopq(sm0 + ST + g * 32), xa = vopq(sm0 + XR + l16), vt = vopq(tag);
      auto loop = [&](auto xc) {
        constexpr bool X = decltype(xc)::value;
        if constexpr (X) *(f32x4*)(smem + XR + 1024 + lane * 16) = quad(cs[0]);
        const float cv[2] = {cs[0][0], cs[1][0]};
        f32x4 a[2][2] = {{z4, z4}, {z4, z4}};
        u32x4 vw[2] = {{0u, 0u, 0u, 0u}, {0u, 0u, 0u, 0u}};
        const unsigned pa = vopq(sm0 + PP + (X ? 0 : wave * 2048) + l16);
        const char* ob = cx.ws + WS_O + (size_t)cx.g * 68 * TB + (X ? 16384 + jq * 256 : (2 * jq + wave) * 1024) - TB;
        const char* fp = cx.fp + wave * 8 * FSTR;
        prio<PR_A>();
        bar_raw();
        qa_periods([&](auto jc, auto ec, int it) {
          constexpr int j = decltype(jc)::value, e = j & 1, o = 1 - e; constexpr bool END = decltype(ec)::value;
          bf16x8 pf[4]; f32x4 d[4], s0, s1;
          auto reads = [&](auto ic) {
            constexpr int i = decltype(ic)::value;
            if constexpr (i < (X ? 4 : 2)) lds_rd<o * 4096 + i * 1024>(pf[i], pa);
            if constexpr (i == 4) lds_rd<o * 128>(s0, sa);
            if constexpr (i == 5) lds_rd<o * 128 + 16>(s1, sa);
          };
          auto mm = [&](auto kc, auto ic, const bf16x8& x) {
            constexpr int k = decltype(kc)::value;
            if constexpr (decltype(ic)::value == 0) { if constexpr (X) mf<k == 0>(a[e][0], W[k][0], x); else mf<k == 0>(a[e][0], x, W[k][0]); }
            if constexpr (decltype(ic)::value == 1) mf<k == 0>(a[e][1], x, W[k][1]);
          };
          if constexpr (!END || j < 2) {
            prime<e * TB>(bq, la[j >> 1]);
            stream<e * TB, -1, 0>(bq, la[j >> 1], mm, [&](auto kc) {
              constexpr int k = decltype(kc)::value;
              if constexpr (k == 0) TR(1, 1);
              if constexpr (k == 6) TR(1, 2);
              if constexpr (k == 12) TR(1, 3);
              if constexpr (k == 17) TR(1, 4);
              if constexpr (k >= RK0 && k < RK0 + 6) reads(IC<k - RK0>{});
              if constexpr (k == 13) adv(ob, TB);
            });
          } else { adv(ob, TB); static_for<0, 6>([&](auto ic) { reads(ic); }); }
          if constexpr (END && j >= 2) wait_frag<0>();
          static_for<0, X ? 4 : 2>([&](auto hc) { constexpr int h = decltype(hc)::value; mf<true>(d[h], asb(vw[X ? 1 : h]), pf[h]); });
          asm volatile("s_setprio 0" : "+v"(s0), "+v"(s1) :: "memory");
          if constexpr (!END) check_flags<4>(fw, fr, pv, tag, dl); else { wait_vm<0>(); tie(fw); settle(); }
          gst4m<1, j * 128>(fp, l16, vt);
          if constexpr (X) {
            if constexpr (!END || j < 2) lds_wr<e * 1024>(xa, a[e][0]);
            tie(d[0], d[1], d[2], d[3]);
            unsigned e2 = cvt2(d[2][0], d[2][1]), e3 = cvt2(d[3][2], d[3][3]), e0 = cvt2(d[0][0], d[0][1]), e1 = cvt2(d[1][2], d[1][3]), n2, n3;
            asm volatile("s_nop 0\n\tv_permlane16_swap_b32 %0, %2\n\tv_permlane16_swap_b32 %1, %3" : "+v"(e2), "+v"(e3), "=&v"(n2), "=&v"(n3) : "v"(e0), "v"(e1));
            gst16m(ob, l16, u32x4{e0, e1, n2, n3}, 0xFFFFull);
          } else { tie(d[0], d[1]); gst16(ob, l16, pack8(d[0], d[1])); }
          TR(1, 5);
          if constexpr (!END) feed(jc, IC<4>{}, it);
#pragma unroll
          for (int m = X ? 1 : 0; m < 2; ++m) {
            const f32x4 w = a[o][m];
            vw[m][2 * o] = cvt2(fmaf(cv[m], s0[0], w[0]) * s0[1], fmaf(cv[m], s0[2], w[1]) * s0[3]);
            vw[m][2 * o + 1] = cvt2(fmaf(cv[m], s1[0], w[2]) * s1[1], fmaf(cv[m], s1[2], w[3]) * s1[3]);
            tie(vw[m]);
          }
          prio<PR_A>();
          if constexpr (j == 3) adv(fp, 512);
          if constexpr (!END) LWAIT(7);
          TR(1, 6);
          bar_raw();
          TR(0, 0); TR(1, 7);
        });
        wait_vm<0>();
        gst4m<1>(fp, l16, vt);
        TR_OUT();
      };
      if (wave == 2) loop(std::true_type{}); else loop(std::false_type{});
    }
  } else {
    const int h = wave - 4;
    f32x4 cq = quad(cs[0]), ck = quad(cs[1]);
    tie(cq, ck);
    const float ninf = -__builtin_inff();
    f32x4 bo, bp;
#pragma unroll
    for (int e = 0; e < 4; ++e) { const int m = 4 * g + e; bo[e] = (m <= r && m >= r - 7) ? 0.f : ninf; bp[e] = (m >= r + 9) ? 0.f : ninf; }
    Soft f = {};
#pragma unroll
    for (int e = 0; e < 2; ++e) f.mo[e] = vopq((4 * g + 2 * e <= r ? 0xFFFFu : 0u) | (4 * g + 2 * e + 1 <= r ? 0xFFFF0000u : 0u));
    const unsigned xa = vopq(sm0 + XR + (h >> 1) * 256 + r * 16 + (h & 1) * 8), sa = vopq(sm0 + ST + r * 8), pa = vopq(sm0 + PP + h * 1024 + l16);
    const float g0 = (g == 0) ? 1.f : 0.f;
    bar_raw();
    f32x2 cxq, cxk;
    lds_rd8<1024>(cxq, xa); lds_rd8<1536>(cxk, xa);
    bf16x8 x[18];
    auto first = [&](auto oc, unsigned a) { static_for<0, 8>([&](auto kc) { lds_rd<decltype(oc)::value + decltype(kc)::value * 1024>(x[decltype(kc)::value], a); }); };
    first(IC<0>{}, la[0]);
    wait_lg<0>();
    tie(cxq, cxk);
    f32x4 a0 = z4, a1 = z4, so = z4;
    u32x4 qf = {0u, 0u, 0u, 0u}, kf = qf;
    float sc = 0.f, scn = 0.f;
    tie(qf, kf, bo, bp);
    qa_periods([&](auto jc, auto ec, int it) {
      constexpr int j = decltype(jc)::value, e = j & 1, j1 = (j + 1) & 3; constexpr bool END = decltype(ec)::value;
      auto put = [&]() { lds_wr<e * 4096>(pa, f32x4{f.s[0], f.s[1], f.s[2], f.s[3]}); };
      if constexpr (END && j >= 2) {
        if constexpr (j == 2) { soft3(f); put(); }
        bar_raw();
      } else {
        prio<PR_B>();
        static_for<0, 5>([&](auto pc) {
          constexpr int k = 2 * decltype(pc)::value;
          wait_frag<6>();
          qk_pair<k / 2, e, e * TB + (k + 8) * 1024>(a0, a1, W[k], W[k + 1], x[k], x[k + 1], x[k + 8], x[k + 9], la[j >> 1], f);
          if constexpr (k == 0) TR(1, 7);
          if constexpr (k == 4) TR(1, 8);
        });
        TR(1, 9);
        wait_frag<4>();
        mf<false>(a0, W[10][0], x[10]);
        put();
        mf<false>(a1, W[10][1], x[10]);
        static_for<11, 14>([&](auto kc) {
          constexpr int k = decltype(kc)::value;
          wait_frag<5>();
          mf<false>(a0, W[k][0], x[k]);
          if constexpr (k == 13) asm volatile("s_setprio 3\n\ts_waitcnt lgkmcnt(0)" ::: "memory");
          mf<false>(a1, W[k][1], x[k]);
        });
        TR(1, 10);
        bar_bare();
        TR(0, 0); TR(1, 11);
        f32x2 st, xq, xk;
        mf8<e>(a0, a1, W[14], W[15], W[16], W[17], x[14], x[15], x[16], x[17], st, xk, xq, sa, xa);
        asm volatile("s_waitcnt lgkmcnt(0)" : "+v"(st), "+v"(xq), "+v"(xk) :: "memory");
        TR(0, 1);
        const float ix = st[1] * g0;
        kf[2] = cvt2(fmaf(cxk[0], st[0], xk[0]) * ix, fmaf(cxk[1], st[0], xk[1]) * ix);
        sc = scn; scn = st[1] * (0.23570226039551584f * 1.4426950408889634f);
        asm volatile("" : "+v"(kf), "+v"(scn), "+v"(a0), "+v"(a1));
        kf[0] = cvt2(fmaf(ck[0], st[0], a1[0]) * st[1], fmaf(ck[1], st[0], a1[1]) * st[1]);
        kf[1] = cvt2(fmaf(ck[2], st[0], a1[2]) * st[1], fmaf(ck[3], st[0], a1[3]) * st[1]);
        f32x4 sp;
        if constexpr ((!END && j == 0) || (END && j == 1)) {
          const float off = (low && (END || it == 8)) ? ninf : 0.f;
          sp = MFMA(asb(kf), asb(qf), bp + off);
        } else sp = MFMA(asb(kf), asb(qf), bp);
        __builtin_amdgcn_sched_barrier(0);
        qf[0] = cvt2(fmaf(cq[0], st[0], a0[0]), fmaf(cq[1], st[0], a0[1]));
        qf[1] = cvt2(fmaf(cq[2], st[0], a0[2]), fmaf(cq[3], st[0], a0[3]));
        qf[2] = cvt2(fmaf(cxq[0], st[0], xq[0]), fmaf(cxq[1], st[0], xq[1]));
        asm volatile("" : "+v"(qf), "+v"(sp));
        TR(0, 3);
        max4(f.s, so, sp);
        if constexpr (!(END && j == 1)) mfc(so, asb(kf), asb(qf), bo);
        soft1(f, sc);
        if constexpr (END) tie(bo);
        TR(0, 4);
        soft2<(END && j == 1) ? -1 : (j1 & 1) * TB>(f, x, la[j1 >> 1]);
        TR(0, 6);
        if constexpr (j == 3) wait_lg<0>();
      }
    });
    TR_OUT();
  }
}

__global__ __launch_bounds__(512, 2) void k_main(const u16* x0, const u16* wqkv, const u16* wo,
                                                 const u16* wg, const u16* wu, const u16* wd,
                                                 char* ws, u16* out) {
  __shared__ __attribute__((aligned(1024))) char smem[SMEM_BYTES];
  Ctx cx;
  const unsigned dm = lds_addr(smem) + 162816;
  {
    unsigned long long pc; unsigned len;
    asm volatile("s_getpc_b64 %0\n.Lkbeg:\n\ts_mov_b32 %1, .Lkend-.Lkbeg" : "=s"(pc), "=s"(len));
    cx.code = (const char*)pc + min(((blockIdx.x & 8) * 64 + threadIdx.x) * 128, len);
    const unsigned i0 = blockIdx.x >> 3;
    if (i0 >= 22 && i0 < 30) touch(cx.code, dm);
    if (i0 >= 5 && i0 < 30) {
      const unsigned ln = min((i0 - 5) * 512 + threadIdx.x, 11519u), sg = (ln * 13108u) >> 16;
      const char* bw = (i0 * 512 + (threadIdx.x & 448) < 5 * 512 + 8640) ? (const char*)wqkv : (const char*)wo - 1728 * 1152;
      touch(bw + (sg * (1152 - 640) + ((blockIdx.x & 1) * 4 + ln) * 128), dm);
    }
  }
  cx.ws = ws;
  cx.wave = __builtin_amdgcn_readfirstlane(threadIdx.x >> 6);
  cx.lane = threadIdx.x & 63;
  asm volatile("s_cmp_eq_u32 %1, 4\n\ts_cselect_b64 %0, 1, 0" : "=s"(cx.w0) : "s"(cx.wave) : "scc");
  cx.dl = ((unsigned)__builtin_amdgcn_s_memtime() + SPIN) | 1u;
  const unsigned t0 = (unsigned)__builtin_amdgcn_s_memrealtime();
  const int bid = sopq(blockIdx.x), xh = bid & 1, x4 = bid & 3, qd = (bid >> 2) & 1, i = bid >> 3, lane = cx.lane, wave = cx.wave;
  const int d = i - 23, tri = d / 3;
  const int c = x4 * 9 + ((i - 5) >> 1), ks = (tri < 2) ? x4 : 4 + (x4 >> 1), ns = d - 3 * tri;
  cx.g = (i < 4) ? 2 * (1 - qd) + 1 - (x4 >> 1) : (i == 4) ? 2 * (1 - qd) + (x4 >> 1) : (i < 23) ? 2 * qd + ((i - 5) & 1) : 2 * (1 - qd) + (tri < 2 ? tri : xh);
  cx.fl = ws + WS_FL + cx.g * FGRP;
  cx.fp = cx.fl + (i < 4 ? F_O + xh * 4 + i : i == 4 ? F_X1 + xh : i < 23 ? F_H + c : F_P + ns * 6 + ks) * FSTR;
  const bool fast = i >= 30;
  auto warm = [&]() {
    if (wave < 2) touch(ws + min((size_t)(wave * 64 + lane) << 20, WS_END - 4), dm);
    else if (wave == 2) touch((const char*)out + min(lane << 20, 2359292), dm);
    else if (wave == 3 && i >= 5 && i < 20) {
      const int ln = min(((i - 5) * 2 + xh) * 64 + lane, 26 * 72 - 1);
      touch(ws + WS_FL + (size_t)((bid >> 1) & 3) * FGRP + F_X1 * FSTR + ln * 128, dm);
    }
  };
  if (!fast) warm();
  if (i >= 23) {
    u32x4 v[5];
    const int g0 = (bid >> 1) & 3, n0 = (fast ? i - 30 : 2 + (wave >> 2) * 7 + i - 23) * 2 + xh;
    const int k0 = fast ? 2 * wave : 4 * (wave & 3), nk = fast ? 2 : 4, wl = fast ? wave : wave & 3;
    const char* src = (const char*)x0 + (size_t)((g0 >> 1) * 1024 + 32 * (31 - n0) + (g0 & 1)) * (DM * 2);
    const char* dst = ws + WS_X0 + (size_t)((g0 & 1) * 64 + top(g0) + n0) * TB;
    const unsigned vr = (lane & 15) * 2304 + (lane >> 4) * 16;
    gld16(v[0], src + k0 * 64, vr); gld16(v[1], src + k0 * 64 + 64, vr); gld16(v[4], src + (16 + (wl & 1)) * 64, vr);
    gld16(v[2], src + (k0 + nk - 2) * 64, vr); gld16(v[3], src + (k0 + nk - 1) * 64, vr);
    const unsigned tag = get_tag(ws);
    wait_vm<0>();
    if (wave == 3) STAMP_AT(68);
    tie(v[0], v[1], v[2], v[3], v[4]);
    gst16(dst + k0 * 1024, lane * 16, v[0]); gst16(dst + k0 * 1024 + 1024, lane * 16, v[1]);
    if (wl < 2) gst16(dst + (16 + wl) * 1024, lane * 16, v[4]);
    if (!fast) { gst16(dst + k0 * 1024 + 2048, lane * 16, v[2]); gst16(dst + k0 * 1024 + 3072, lane * 16, v[3]); }
    wait_vm<0>();
    bar_bare();
    if (wave == 3) STAMP_AT(69);
    if (wl == 0) gst4m<-1>(ws + WS_F2 + ((g0 & 1) * 72 + top(g0) + n0) * F2ROW + F_X0 * 128, lane * 4, vopq(tag));
    if (fast) warm();
  }
  if (i >= 5) {
    const unsigned z = ~get_tag(ws);
    gst16(cx.fp + wave * 1024, lane * 16, u32x4{z, z, z, z});
    if (wave == 0) gst16(cx.fp + 8192, lane * 16, u32x4{z, z, z, z});
  }
  if (i >= 5) { const unsigned dly = (i < 23) ? DLY_G : DLY_D; while ((unsigned)__builtin_amdgcn_s_memrealtime() - t0 < dly) __builtin_amdgcn_s_sleep(4); }
  if (i < 4) { if (wave < 4) role_qa<true>(smem, wqkv, cx, xh * 4 + i); else role_qa<false>(smem, wqkv, cx, xh * 4 + i); }
  else if (i == 4) { if (wave < 4) role_wo<true>(smem, wo, cx, xh); else role_wo<false>(smem, wo, cx, xh); }
  else if (i < 23) role_gu(smem, wg, wu, out, cx, c);
  else role_dn(smem, wd, cx, ks, ns);
  asm volatile(".subsection 2\n.Lkend:\n\t.fill 64, 4, 0xBF800000\n\t.subsection 0");
}
#if defined(__HIP_DEVICE_COMPILE__)
#pragma clang attribute pop
#endif

extern "C" void kernel_launch(void* const* d_in, const int* in_sizes, int n_in,
                              void* d_out, int out_size, void* d_ws, size_t ws_size,
                              hipStream_t stream) {
  k_main<<<256, 512, 0, stream>>>((const u16*)d_in[0], (const u16*)d_in[1], (const u16*)d_in[2], (const u16*)d_in[4],
                                  (const u16*)d_in[5], (const u16*)d_in[6], (char*)d_ws, (u16*)d_out);
}
```

```cpp
#include <hip/hip_runtime.h>
#include <stdint.h>
#include <type_traits>

#if defined(__HIP_DEVICE_COMPILE__)
#pragma clang attribute push(__attribute__((target("no-packed-fp32-ops"))), apply_to = function)
#endif
typedef __attribute__((ext_vector_type(8))) short bf16x8;
typedef __attribute__((ext_vector_type(4))) short bf16x4;
typedef __attribute__((ext_vector_type(4))) float f32x4;
typedef __attribute__((ext_vector_type(2))) float f32x2;
typedef __attribute__((ext_vector_type(4))) unsigned u32x4;
typedef __attribute__((ext_vector_type(2))) unsigned u32x2;
typedef __bf16 bf2v __attribute__((ext_vector_type(2)));
typedef unsigned short u16;

#define MFMA(b, a, c) __builtin_amdgcn_mfma_f32_16x16x32_bf16(b, a, c, 0, 0, 0)
constexpr int DM = 576, FD = 4608, NQKV = 3 * DM;
constexpr int TB = 18432;
constexpr int HB = 24576;
constexpr int PB = 6144;
constexpr int FSTR = 72 * 128, FGRP = 80 * FSTR, F2ROW = 19 * 128;
constexpr size_t WS_FL = 4096;
constexpr size_t WS_IS = WS_FL + 4 * FGRP;
constexpr size_t WS_F2 = WS_IS + 4 * 64 * 128;
constexpr size_t WS_O = WS_F2 + 2 * 72 * F2ROW;
constexpr size_t WS_X1 = WS_O + (size_t)4 * 68 * TB;
constexpr size_t WS_X2 = WS_X1 + (size_t)256 * TB;
constexpr size_t WS_X0 = WS_X2 + (size_t)128 * TB;
constexpr size_t WS_P = WS_X0 + (size_t)128 * TB;
constexpr size_t WS_H = WS_P + (size_t)72 * 64 * PB;
constexpr size_t WS_END = WS_H + (size_t)(24 * 64 + 4) * HB;
static_assert(WS_O % 1024 == 0 && WS_END <= ((size_t)256 << 20), "workspace");
constexpr int F_H = 0, F_P = 36, F_X1 = 54, F_O = 56, F_X0 = 18;
constexpr int TLAG = 2, PLAG = 3, OLAG = 4;
constexpr int SMEM_BYTES = 163840;
constexpr int PDG = 6, PDD = 5;
constexpr int BARG = 14, BARD = 10;
constexpr int PR_A = 2, PR_B = 1;
constexpr int DLY_G = 120;
constexpr int DLY_D = 500;
constexpr int LAGF = 8;
#define SLP "s_sleep 2\n\t"

__device__ __forceinline__ float u2f(unsigned u) { return __builtin_bit_cast(float, u); }
__device__ __forceinline__ int f2i(float f) { return __builtin_bit_cast(int, f); }
__device__ __forceinline__ float rbf(float f) { __bf16 b = (__bf16)f; return u2f((unsigned)__builtin_bit_cast(u16, b) << 16); }
__device__ __forceinline__ unsigned cvt2(float a, float b) { return __builtin_bit_cast(unsigned, __builtin_convertvector(f32x2{a, b}, bf2v)); }
__device__ __forceinline__ float blo(unsigned w) { return u2f(w << 16); }
__device__ __forceinline__ float bhi(unsigned w) { return u2f(w & 0xffff0000u); }
typedef __attribute__((address_space(3))) bf16x4* lds_b4p;
__device__ __forceinline__ bf16x4 tr_read(const char* p) {
  return __builtin_amdgcn_ds_read_tr16_b64_v4i16((lds_b4p)(__attribute__((address_space(3))) void*)(p));
}
__device__ __forceinline__ bf16x8 tr_frag(const char* lo, const char* hi) {
  return __builtin_shufflevector(tr_read(lo), tr_read(hi), 0, 1, 2, 3, 4, 5, 6, 7);
}
__device__ __forceinline__ unsigned lds_addr(const void* p) { return (unsigned)(size_t)(__attribute__((address_space(3))) const char*)p; }
template <int I, int N, class F> __device__ __forceinline__ void static_for(F&& f) {
  if constexpr (I < N) { f(std::integral_constant<int, I>{}); static_for<I + 1, N>(f); }
}
template <int N> using IC = std::integral_constant<int, N>;
constexpr int imin(int a, int b) { return a < b ? a : b; }
template <int N> __device__ __forceinline__ void wait_vm() { asm volatile("s_waitcnt vmcnt(%0)" ::"n"(N) : "memory"); }
template <int N> __device__ __forceinline__ void wait_lg() { asm volatile("s_waitcnt lgkmcnt(%0)" ::"n"(N) : "memory"); }
template <int N> __device__ __forceinline__ void wait_frag() { __builtin_amdgcn_s_waitcnt(0xC07F | (N << 8)); }
__device__ __forceinline__ void bar_raw() { asm volatile("s_waitcnt lgkmcnt(0)\n\ts_barrier" ::: "memory"); }
__device__ __forceinline__ void bar_bare() { asm volatile("s_barrier" ::: "memory"); }
template <int P> __device__ __forceinline__ void prio() { asm volatile("s_setprio %0" :: "n"(P)); }
__device__ __forceinline__ void settle() { asm volatile("s_nop 7" ::: "memory"); }
__device__ __forceinline__ int sopq(int v) { asm volatile("" : "+s"(v)); return v; }
__device__ __forceinline__ unsigned vopq(unsigned v) { asm volatile("" : "+v"(v)); return v; }
template <class A> __device__ __forceinline__ void tie(A& a) { asm volatile("" : "+v"(a)); }
template <class A, class... R> __device__ __forceinline__ void tie(A& a, R&... r) { tie(a); tie(r...); }
__device__ __forceinline__ void adv(const char*& p, long d) { p += d; asm volatile("" : "+s"(p)); }

__device__ __forceinline__ void dma_w(const void* s, unsigned v, unsigned m) {
  asm volatile("s_mov_b32 m0, %2\n\ts_nop 0\n\tglobal_load_lds_dwordx4 %1, %0" :: "s"(s), "v"(v), "s"(m) : "memory");
}
__device__ __forceinline__ void dma_w54(const void* s, unsigned v, unsigned m) {
  asm volatile("s_mov_b32 m0, %2\n\ts_mov_b32 exec_hi, 0x3fffff\n\tglobal_load_lds_dwordx4 %1, %0\n\ts_mov_b32 exec_hi, -1" :: "s"(s), "v"(v), "s"(m) : "memory");
}
#define DMA_AT(O) "\n\tglobal_load_lds_dwordx4 %1, %0 offset:" #O " sc1"
#define DMA_N(BODY) asm volatile("s_add_u32 m0, %2, %3\n\ts_nop 0" BODY :: "s"(s), "v"(v), "s"(m), "n"(MO) : "memory", "scc")
template <int MO, int N> __device__ __forceinline__ void dma(const void* s, unsigned v, unsigned m) {
  if constexpr (N == 0) DMA_N("\n\ts_mov_b32 exec_hi, 0" DMA_AT(0) "\n\ts_mov_b32 exec_hi, -1");
  if constexpr (N == 1) DMA_N(DMA_AT(0));
  if constexpr (N == 2) DMA_N(DMA_AT(0) DMA_AT(1024));
  if constexpr (N == 3) DMA_N(DMA_AT(0) DMA_AT(1024) DMA_AT(2048));
  if constexpr (N == 4) DMA_N(DMA_AT(0) DMA_AT(1024) DMA_AT(2048) DMA_AT(3072));
}
__device__ __forceinline__ void touch(const void* p, unsigned lds_dummy) {
  asm volatile("s_mov_b32 m0, %1\n\ts_nop 0\n\tglobal_load_lds_dword %0, off" :: "v"(p), "s"(lds_dummy) : "memory");
}
__device__ __forceinline__ void dma_w2(const void* s, unsigned v, unsigned m) {
  asm volatile("s_mov_b32 m0, %2\n\ts_nop 0\n\tglobal_load_lds_dwordx4 %1, %0 nt" :: "s"(s), "v"(v), "s"(m) : "memory");
}
__device__ __forceinline__ void gld16(u32x4& d, const void* s, unsigned v) { asm volatile("global_load_dwordx4 %0, %1, %2 sc1" : "=v"(d) : "v"(v), "s"(s) : "memory"); }
template <int OFF> __device__ __forceinline__ void gld4(unsigned& d, const void* s, unsigned v) { asm volatile("global_load_dword %0, %1, %2 offset:%3 sc1" : "=v"(d) : "v"(v), "s"(s), "n"(OFF) : "memory"); }
__device__ __forceinline__ void gst16(const void* s, unsigned v, u32x4 d) {
  asm volatile("global_store_dwordx4 %0, %1, %2 sc1\n\ts_nop 1" :: "v"(v), "v"(d), "s"(s) : "memory");
}
__device__ __forceinline__ void gst16m(const void* s, unsigned v, u32x4 d, unsigned long long m) {
  asm volatile("s_mov_b64 exec, %3\n\tglobal_store_dwordx4 %0, %1, %2 sc1\n\ts_mov_b64 exec, -1\n\ts_nop 0" :: "v"(v), "v"(d), "s"(s), "s"(m) : "memory");
}
template <int LO, int OFF = 0> __device__ __forceinline__ void gst4m(const void* s, unsigned v, unsigned d) {
  asm volatile("s_mov_b32 exec_lo, %3\n\ts_mov_b32 exec_hi, 0\n\tglobal_store_dword %0, %1, %2 offset:%4 sc1\n\ts_mov_b64 exec, -1" :: "v"(v), "v"(d), "s"(s), "n"(LO), "n"(OFF) : "memory");
}
#define COLD(L, BACK, BODY) BACK ":\n\t.subsection 1\n" L ":\n\t" BODY "s_branch " BACK "\n\t.subsection 0"
template <int OFF> __device__ __forceinline__ void flag_by(unsigned long long who, const void* s, unsigned l16, unsigned tag) {
  asm volatile("s_mov_b64 exec, %3\n\tglobal_store_dword %0, %1, %2 offset:%4 sc1\n\ts_mov_b64 exec, -1" :: "v"(l16), "v"(tag), "s"(s), "s"(who), "n"(OFF) : "memory");
}
#define TIME_UP(DL, L) SLP "s_memtime vcc\n\ts_waitcnt lgkmcnt(0)\n\ts_sub_u32 vcc_lo, vcc_lo, " DL "\n\ts_cmp_ge_i32 vcc_lo, 0\n\ts_cbranch_scc1 " L "\n\t"
template <int N, int OFF = 0> __device__ __forceinline__ void check_flags(unsigned& v, const void* s, unsigned voff, unsigned tag, unsigned deadline) {
  asm volatile("s_waitcnt vmcnt(%5)\n\tv_cmp_ne_u32 vcc, %4, %0\n\ts_cbranch_vccnz .Lsl%=\n"
               COLD(".Lsl%=", ".Lok%=", "s_cmp_eq_u32 %1, 0\n\ts_cbranch_scc1 .Lok%=\n"
                    ".Lsp%=:\n\tglobal_load_dword %0, %2, %3 offset:%6 sc1\n\ts_waitcnt vmcnt(0)\n\tv_cmp_ne_u32 vcc, %4, %0\n\ts_cbranch_vccz .Lok%=\n\t"
                    TIME_UP("%1", ".Lok%=") "s_branch .Lsp%=\n\t")
               : "+v"(v) : "s"(deadline), "v"(voff), "s"(s), "s"(tag), "n"(N), "n"(OFF) : "memory", "vcc", "scc");
}
__device__ __forceinline__ void poll(const void* s, unsigned voff, unsigned tag, unsigned deadline) {
  unsigned v; gld4<0>(v, s, voff); check_flags<0>(v, s, voff, tag, deadline);
}
__device__ __forceinline__ unsigned make_tag(unsigned nonce) { return nonce * 2654435761u + 0x7F4A7C15u; }
struct Red { u32x4 pp[6], xp; unsigned i2, fw; const char *rp, *rx, *ri, *rf, *f2, *ob; };
#define RED_M(D, S, P, C) "v_mfma_f32_16x16x32_bf16 " D ", " S ", " P ", " C "\n\t"
__device__ __forceinline__ void reduce_ask(Red& u, unsigned l16, unsigned iv, unsigned pv, unsigned vt, unsigned tag, unsigned deadline, int step) {
  asm volatile("s_cmp_lt_u32 %20, 4\n\ts_cbranch_scc1 .Ls%=\n"
               COLD(".Ls%=", ".Lz%=", "s_cmp_eq_u32 %20, 1\n\ts_cbranch_scc1 .Lc%=\n\ts_cmp_eq_u32 %20, 2\n\ts_cbranch_scc1 .Lz%=\n\ts_cmp_eq_u32 %20, 3\n\ts_cbranch_scc1 .Lp%=\n\t"
               "global_load_dword %8, %16, %12 sc1\n\ts_branch .Lz%=\n"
               ".Lp%=:\n\ts_mov_b64 exec, 1\n\tglobal_store_dword %14, %17, %13 sc1\n\ts_mov_b64 exec, -1\n\ts_branch .Lz%=\n"
               ".Lc%=:\n\t"
               "v_cmp_ne_u32 vcc, %18, %8\n\ts_cbranch_vccz .Lr%=\n\t" TIME_UP("%19", ".Lr%=")
               "global_load_dword %8, %16, %12 sc1\n\ts_waitcnt vmcnt(0)\n\ts_branch .Lc%=\n"
               ".Lr%=:\n\tglobal_load_dwordx4 %0, %14, %9 offset:-3072 sc1\n\tglobal_load_dwordx4 %1, %14, %9 offset:-2048 sc1\n\t"
               "global_load_dwordx4 %2, %14, %9 offset:-1024 sc1\n\tglobal_load_dwordx4 %3, %14, %9 sc1\n\tglobal_load_dwordx4 %4, %14, %9 offset:1024 sc1\n\t"
               "global_load_dwordx4 %5, %14, %9 offset:2048 sc1\n\tglobal_load_dwordx4 %6, %14, %10 sc1\n\tglobal_load_dword %7, %15, %11 sc1\n\t")
               : "+v"(u.pp[0]), "+v"(u.pp[1]), "+v"(u.pp[2]), "+v"(u.pp[3]), "+v"(u.pp[4]), "+v"(u.pp[5]), "+v"(u.xp), "+v"(u.i2), "+v"(u.fw)
               : "s"(u.rp), "s"(u.rx), "s"(u.ri), "s"(u.rf), "s"(u.f2), "v"(l16), "v"(iv), "v"(pv), "v"(vt), "s"(tag), "s"(deadline), "s"(step) : "memory", "vcc", "scc");
}
__device__ __forceinline__ void reduce_sum(const Red& u, const u32x4& s0, const u32x4& s1, f32x4& v0, f32x4& v1, float (&x)[8]) {
  asm volatile(RED_M("%0", "%10", "%12", "0") RED_M("%1", "%11", "%12", "0") "v_lshlrev_b32 %2, 16, %18\n\tv_and_b32 %3, 0xffff0000, %18\n\t"
               RED_M("%0", "%10", "%13", "%0") RED_M("%1", "%11", "%13", "%1") "v_lshlrev_b32 %4, 16, %19\n\tv_and_b32 %5, 0xffff0000, %19\n\t"
               RED_M("%0", "%10", "%14", "%0") RED_M("%1", "%11", "%14", "%1") "v_lshlrev_b32 %6, 16, %20\n\tv_and_b32 %7, 0xffff0000, %20\n\t"
               RED_M("%0", "%10", "%15", "%0") RED_M("%1", "%11", "%15", "%1") "v_lshlrev_b32 %8, 16, %21\n\tv_and_b32 %9, 0xffff0000, %21\n\t"
               RED_M("%0", "%10", "%16", "%0") RED_M("%1", "%11", "%16", "%1") RED_M("%0", "%10", "%17", "%0") RED_M("%1", "%11", "%17", "%1")
               : "=&v"(v0), "=&v"(v1), "=&v"(x[0]), "=&v"(x[1]), "=&v"(x[2]), "=&v"(x[3]), "=&v"(x[4]), "=&v"(x[5]), "=&v"(x[6]), "=&v"(x[7])
               : "v"(s0), "v"(s1), "v"(u.pp[0]), "v"(u.pp[1]), "v"(u.pp[2]), "v"(u.pp[3]), "v"(u.pp[4]), "v"(u.pp[5]), "v"(u.xp[0]), "v"(u.xp[1]), "v"(u.xp[2]), "v"(u.xp[3]));
}
__device__ __forceinline__ void wait_store(int step) {
  asm volatile("s_cmp_lt_u32 %0, 4\n\ts_cbranch_scc1 .La%=\n\ts_waitcnt vmcnt(0)\n"
               COLD(".La%=", ".Lb%=", "s_waitcnt vmcnt(8)\n\ts_cmp_eq_u32 %0, 1\n\ts_cbranch_scc1 .Lb%=\n\ts_waitcnt vmcnt(1)\n\t") :: "s"(step) : "memory", "scc");
}

template <bool Z> __device__ __forceinline__ void mf(f32x4& c, const bf16x8& a, const bf16x8& b) {
  if (Z) asm volatile("v_mfma_f32_16x16x32_bf16 %0, %1, %2, 0" : "=&v"(c) : "v"(a), "v"(b));
  else asm volatile("v_mfma_f32_16x16x32_bf16 %0, %1, %2, %0" : "+v"(c) : "v"(a), "v"(b));
}
__device__ __forceinline__ void mfc(f32x4& d, const bf16x8& a, const bf16x8& b, const f32x4& c) {
  asm volatile("v_mfma_f32_16x16x32_bf16 %0, %1, %2, %3" : "=&v"(d) : "v"(a), "v"(b), "v"(c));
}
template <int OFF, class T> __device__ __forceinline__ void lds_rd(T& d, unsigned a) { asm volatile("ds_read_b128 %0, %1 offset:%2" : "=v"(d) : "v"(a), "n"(OFF)); }
template <int OFF, class T> __device__ __forceinline__ void lds_rd8(T& d, unsigned a) { asm volatile("ds_read_b64 %0, %1 offset:%2" : "=v"(d) : "v"(a), "n"(OFF)); }
template <int OFF, class T> __device__ __forceinline__ void lds_wr(unsigned a, const T& d) { asm volatile("ds_write_b128 %0, %1 offset:%2" :: "v"(a), "v"(d), "n"(OFF) : "memory"); }
template <int OFF, class T> __device__ __forceinline__ void lds_wr8(unsigned a, const T& d) { asm volatile("ds_write_b64 %0, %1 offset:%2" :: "v"(a), "v"(d), "n"(OFF) : "memory"); }
template <int NK, int PD, int G> struct Ring { bf16x8 q[NK]; };
template <int O, int NK, int PD, int G> __device__ __forceinline__ void prime(Ring<NK, PD, G>& b, unsigned la) {
  static_for<0, PD>([&](auto kc) { lds_rd<O + decltype(kc)::value * 1024>(b.q[decltype(kc)::value], la); });
}
template <int O, int BAR, int VMB, int NK, int PD, int G, class F, class H> __device__ __forceinline__ void stream(Ring<NK, PD, G>& b, unsigned la, F&& f, H&& hook) {
  constexpr int NT = 36 / NK;
  static_assert((BAR < 0 || BAR + PD >= NK) && NK % G == 0, "barrier before the last read");
  if constexpr (BAR >= 0) prio<PR_B>();
  static_for<0, 36>([&](auto sc) {
    constexpr int s = decltype(sc)::value, c = s / NT, grp = s / (G * NT), in = s % (G * NT);
    constexpr int k = grp * G + in % G, t = (G > 1 && (grp & 1)) ? NT - 1 - in / G : in / G;
    constexpr int KZ = (BAR >= 0) ? BAR : NK - PD + 2;
    if constexpr (s == BAR * NT - 1) asm volatile("s_setprio 3\n\ts_waitcnt vmcnt(%0) lgkmcnt(0)" :: "n"(VMB) : "memory");
    if constexpr (s == BAR * NT) { asm volatile("" ::: "memory"); __builtin_amdgcn_s_barrier(); asm volatile("" ::: "memory"); }
    if constexpr (BAR < 0 && s == KZ * NT) wait_frag<0>();
    if constexpr (c < KZ && in < G) wait_frag<imin(NK - 1, PD - 1 + (s + NT - 1) / NT) - k>();
    f(IC<k>{}, IC<t>{}, b.q[k]);
    if constexpr (s % NT == 0 && c + PD < NK) lds_rd<O + (c + PD) * 1024>(b.q[c + PD], la);
    if constexpr (in == G * NT - 1) static_for<grp * G, grp * G + G>([&](auto kc) { f(kc, IC<NT>{}, b.q[decltype(kc)::value]); });
    if constexpr (s % NT == NT - 1) hook(IC<c>{});
  });
}
template <class F> __device__ __forceinline__ void periods(F&& f) {
#pragma nounroll
  for (int it = 0; it < 16; ++it) static_for<0, 4>([&](auto jc) { f(jc, it); });
}

__device__ __forceinline__ void max4(float (&s)[4], const f32x4& a, const f32x4& b) {
  asm volatile("v_max_f32 %0, %4, %8\n\tv_max_f32 %1, %5, %9\n\tv_max_f32 %2, %6, %10\n\tv_max_f32 %3, %7, %11" : "=&v"(s[0]), "=&v"(s[1]), "=&v"(s[2]), "=&v"(s[3])
               : "v"(a[0]), "v"(a[1]), "v"(a[2]), "v"(a[3]), "v"(b[0]), "v"(b[1]), "v"(b[2]), "v"(b[3]));
}
struct Soft { float s[4], t0, t1; unsigned mo[2]; };
__device__ __forceinline__ void soft1(Soft& f, float sc) {
  asm volatile("v_max3_f32 %4, %0, %1, %2\n\tv_max_f32 %5, %4, %3\n\tv_max_f32 %4, %4, %3\n\ts_nop 1\n\tv_permlane16_swap_b32 %4, %5\n\t"
               "v_max_f32 %4, %4, %5\n\tv_mov_b32 %5, %4\n\ts_nop 1\n\tv_permlane32_swap_b32 %4, %5\n\tv_max_f32 %4, %4, %5\n\tv_mul_f32 %4, %4, %6\n\t"
               "v_fma_f32 %0, %0, %6, -%4\n\tv_fma_f32 %1, %1, %6, -%4\n\tv_fma_f32 %2, %2, %6, -%4\n\tv_fma_f32 %3, %3, %6, -%4"
               : "+v"(f.s[0]), "+v"(f.s[1]), "+v"(f.s[2]), "+v"(f.s[3]), "=&v"(f.t0), "=&v"(f.t1) : "v"(sc));
}
#define SOFT2(R0, R1, R2, R3, R4, R5, R6, R7, PAD) \
  "v_exp_f32 %0, %0\n\t" R0 "v_exp_f32 %1, %1\n\t" R1 "v_exp_f32 %2, %2\n\t" R2 "v_exp_f32 %3, %3\n\t" R3 \
  "v_add_f32 %4, %0, %1\n\tv_add_f32 %5, %2, %3\n\t" R4 "v_add_f32 %4, %4, %5\n\tv_mov_b32 %5, %4\n\t" R5 PAD \
  "v_permlane16_swap_b32 %4, %5\n\t" R6 "v_add_f32 %4, %4, %5\n\tv_mov_b32 %5, %4\n\t" R7 PAD \
  "v_permlane32_swap_b32 %4, %5\n\tv_add_f32 %4, %4, %5\n\tv_rcp_f32 %4, %4"
#define SOFT_RD(N, K) "ds_read_b128 %" #N ", %14 offset:%15+" #K "\n\t"
template <int OFF> __device__ __forceinline__ void soft2(Soft& f, bf16x8 (&x)[18], unsigned la) {
  if constexpr (OFF < 0)
    asm volatile(SOFT2("", "", "", "", "", "", "", "", "s_nop 1\n\t") : "+v"(f.s[0]), "+v"(f.s[1]), "+v"(f.s[2]), "+v"(f.s[3]), "=&v"(f.t0), "=&v"(f.t1));
  else
    asm volatile(SOFT2(SOFT_RD(6, 0), SOFT_RD(7, 1024), SOFT_RD(8, 2048), SOFT_RD(9, 3072), SOFT_RD(10, 4096), SOFT_RD(11, 5120), SOFT_RD(12, 6144), SOFT_RD(13, 7168), "s_nop 0\n\t")
                 : "+v"(f.s[0]), "+v"(f.s[1]), "+v"(f.s[2]), "+v"(f.s[3]), "=&v"(f.t0), "=&v"(f.t1),
                   "=&v"(x[0]), "=&v"(x[1]), "=&v"(x[2]), "=&v"(x[3]), "=&v"(x[4]), "=&v"(x[5]), "=&v"(x[6]), "=&v"(x[7]) : "v"(la), "n"(OFF < 0 ? 0 : OFF));
}
#define QK_M(D, W, X, C) "v_mfma_f32_16x16x32_bf16 " D ", " W ", " X ", " C "\n\t"
#define QK_G3 "v_mul_f32 %4, %4, %8\n\t"
#define QK_G4 "v_mul_f32 %5, %5, %8\n\t"
#define QK_G5 "v_mul_f32 %6, %6, %8\n\t"
#define QK_G6 "v_mul_f32 %7, %7, %8\n\t"
#define QK_G7 "v_cvt_pk_bf16_f32 %8, %4, %5\n\t"
#define QK_G8 "v_cvt_pk_bf16_f32 %9, %6, %7\n\t"
#define QK_G9 "v_and_b32 %4, %8, %17\n\t"
#define QK_GA "v_and_b32 %5, %9, %18\n\t"
#define QK_GB "v_xor_b32 %6, %8, %4\n\t"
#define QK_GC "v_xor_b32 %7, %9, %5\n\t"
#define QK_OUT(A, B, C, D) "+v"(f.s[A]), "+v"(f.s[B]), "+v"(f.s[C]), "+v"(f.s[D]), "+v"(f.t0), "+v"(f.t1)
#define QK_PAIR(C0, C1, GA, GB, S) asm volatile(QK_M("%0", "%10", "%14", C0) "ds_read_b128 %2, %16 offset:%19\n\t" QK_M("%1", "%11", "%14", C1) GA \
    QK_M("%0", "%12", "%15", "%0") "ds_read_b128 %3, %16 offset:%20\n\t" QK_M("%1", "%13", "%15", "%1") GB \
    : "+v"(a0), "+v"(a1), "=&v"(y0), "=&v"(y1), QK_OUT(S, S + 1, 2 - S, 3 - S) \
    : "v"(w0[0]), "v"(w0[1]), "v"(w1[0]), "v"(w1[1]), "v"(x0), "v"(x1), "v"(la), "v"(f.mo[0]), "v"(f.mo[1]), "n"(OFF), "n"(OFF + 1024))
template <int P, int E, int OFF> __device__ __forceinline__ void qk_pair(f32x4& a0, f32x4& a1, const bf16x8 (&w0)[2], const bf16x8 (&w1)[2], bf16x8 x0, bf16x8 x1,
                                                                         bf16x8& y0, bf16x8& y1, unsigned la, Soft& f) {
  if constexpr (P == 0) QK_PAIR("0", "0", QK_G3, QK_G4, 0);
  if constexpr (P == 1) QK_PAIR("%0", "%1", QK_G5, QK_G6, 0);
  if constexpr (P == 2) QK_PAIR("%0", "%1", QK_G7, QK_G8, 0);
  if constexpr (P == 3) QK_PAIR("%0", "%1", QK_G9, QK_GA, 2 * E);
  if constexpr (P == 4) QK_PAIR("%0", "%1", QK_GB, QK_GC, 2 * E);
}
__device__ __forceinline__ void soft3(Soft& f) {
  int u = 0;
  asm volatile("s_nop 0\n\t" QK_G3 QK_G4 QK_G5 QK_G6 QK_G7 QK_G8 QK_G9 QK_GA QK_GB QK_GC : "+s"(u), "+s"(u), "+s"(u), "+s"(u), QK_OUT(0, 1, 2, 3)
               : "n"(0), "n"(0), "n"(0), "n"(0), "n"(0), "n"(0), "n"(0), "v"(f.mo[0]), "v"(f.mo[1]));
}
template <int E> __device__ __forceinline__ void mf8(f32x4& a0, f32x4& a1, const bf16x8 (&w0)[2], const bf16x8 (&w1)[2], const bf16x8 (&w2)[2], const bf16x8 (&w3)[2],
                                                     const bf16x8& x0, const bf16x8& x1, const bf16x8& x2, const bf16x8& x3, f32x2& st, f32x2& xk, f32x2& xq, unsigned sa, unsigned xa) {
  asm volatile(QK_M("%0", "%5", "%13", "%0") "ds_read_b64 %2, %17 offset:%19\n\tds_read_b64 %3, %18 offset:%20\n\tds_read_b64 %4, %18 offset:%21\n\t"
               QK_M("%1", "%6", "%13", "%1") QK_M("%1", "%8", "%14", "%1") QK_M("%0", "%7", "%14", "%0")
               QK_M("%1", "%10", "%15", "%1") QK_M("%0", "%9", "%15", "%0") QK_M("%1", "%12", "%16", "%1") QK_M("%0", "%11", "%16", "%0")
               : "+v"(a0), "+v"(a1), "=&v"(st), "=&v"(xk), "=&v"(xq)
               : "v"(w0[0]), "v"(w0[1]), "v"(w1[0]), "v"(w1[1]), "v"(w2[0]), "v"(w2[1]), "v"(w3[0]), "v"(w3[1]), "v"(x0), "v"(x1), "v"(x2), "v"(x3),
                 "v"(sa), "v"(xa), "n"(E * 128), "n"(E * 1024 + 512), "n"(E * 1024));
}

constexpr unsigned SPIN = 400000000u;
struct Ctx { char* ws; int g, wave, lane; unsigned dl; const char *fl, *fp, *code; unsigned long long w0; };
constexpr unsigned long long HALF = 0x0000FFFF0000FFFFull;

__device__ __forceinline__ unsigned get_tag(const char* ws) {
  unsigned nonce;
  asm volatile("s_load_dword %0, %1, 0x0\n\ts_waitcnt lgkmcnt(0)" : "=s"(nonce) : "s"(ws) : "memory");
  return make_tag(nonce);
}
template <int R, int NP, int NX = 0, class I, class Q, class P> __device__ __forceinline__ void load_w(char* smem, const Ctx& cx, unsigned& tag, I&& issue, Q&& rd, P&& then) {
  const int wave = cx.wave;
  static_for<0, 3>([&](auto rc) { issue(rc); });
  tag = get_tag(cx.ws);
  static_for<0, R>([&](auto rc) {
    constexpr int r = decltype(rc)::value;
    wait_vm<imin(2, R - 1 - r) * NP + (r == R - 2 ? NX : 0)>();
    bar_bare();
    rd(rc);
    if constexpr (r + 3 < R || r + 1 == R) bar_raw();
    if constexpr (r + 3 < R) issue(IC<r + 3>{});
    if constexpr (r + 3 == R && NX == 0) { if (wave == ((blockIdx.x >> 4) & 7)) touch(cx.code, lds_addr(smem) + 162816); }
    then(rc);
  });
}
__device__ __forceinline__ bf16x8 asb(u32x4 v) { return __builtin_bit_cast(bf16x8, v); }
__device__ __forceinline__ bf16x8 ones8() { return asb(u32x4{0x3F803F80u, 0x3F803F80u, 0x3F803F80u, 0x3F803F80u}); }
__device__ __forceinline__ u32x4 widen(unsigned w0, unsigned w1) {
  const auto s0 = __builtin_amdgcn_permlane16_swap(w0, w0, false, false);
  const auto s1 = __builtin_amdgcn_permlane16_swap(w1, w1, false, false);
  return u32x4{s0[0], s1[0], s0[1], s1[1]};
}
__device__ __forceinline__ u32x4 pack8(const f32x4& a, const f32x4& b) { return u32x4{cvt2(a[0], a[1]), cvt2(a[2], a[3]), cvt2(b[0], b[1]), cvt2(b[2], b[3])}; }
__device__ __forceinline__ f32x2 row_stat(float sum, float sq) {
  const float mean = sum * (1.0f / 576.0f);
  const float var = fmaxf(sq * (1.0f / 576.0f) - mean * mean, 0.0f);
  return f32x2{-rbf(mean), __builtin_amdgcn_rcpf(rbf(__builtin_amdgcn_sqrtf(rbf(rbf(var) + 1e-5f))))};
}
__device__ __forceinline__ float diag(const f32x4& a, int p) { return (p == 0) ? a[0] : (p == 1) ? a[1] : (p == 2) ? a[2] : a[3]; }
__device__ __forceinline__ int top(int g) { return (1 - (g >> 1)) * 32; }

__device__ __forceinline__ void role_gu(char* smem, const u16* wg, const u16* wu, u16* out, const Ctx& cx, int c) {
  const int wave = cx.wave, lane = cx.lane;
  const unsigned sm0 = lds_addr(smem);
  unsigned tag;
  const int r = lane & 15, g = lane >> 4, q = r >> 2, p = lane & 3;
  constexpr int SUMS = 4 * TB;
  bf16x8 W[18][2];
  u32x4 sel0, sel1;
#pragma unroll
  for (int e = 0; e < 2; ++e) {
    sel0[e] = sel1[2 + e] = (r == 4 * g + 2 * e ? 0x3F80u : 0u) | (r == 4 * g + 2 * e + 1 ? 0x3F800000u : 0u);
    sel0[2 + e] = sel1[e] = 0u;
  }
  tie(sel0, sel1);
  {
    f32x4 cs[2] = {{0.f, 0.f, 0.f, 0.f}, {0.f, 0.f, 0.f, 0.f}};
    constexpr int BUF = 48 * 1056;
    const char* src = (const char*)((wave >> 2) ? wu : wg) + c * 256;
    const unsigned vo = (4 * (g & 1) + 16 * (g >> 1)) * (FD * 2) + r * 16;
    const char* rb = smem + ((g & 1) * 4 + q) * 1056 + (g >> 1) * 512 + wave * 32 + p * 8;
    load_w<6, 6>(smem, cx, tag, [&](auto rc) {
      constexpr int rr = decltype(rc)::value;
#pragma unroll
      for (int j = 0; j < 6; ++j) {
        const int idx = 6 * (wave & 3) + j, id = idx & 7;
        dma_w2(src + (size_t)(32 * (3 * rr + (idx >> 3)) + 8 * (id >> 2) + (id & 3)) * (FD * 2), vo, sm0 + (rr % 3) * BUF + ((wave >> 2) * 24 + idx) * 1056);
      }
    }, [&](auto rc) {
      constexpr int rr = decltype(rc)::value;
#pragma unroll
      for (int kl = 0; kl < 3; ++kl)
#pragma unroll
        for (int m = 0; m < 2; ++m) { const char* a = rb + (rr % 3) * BUF + (m * 24 + kl * 8) * 1056; W[3 * rr + kl][m] = tr_frag(a, a + 256); }
      tie(W[3 * rr][0], W[3 * rr][1], W[3 * rr + 1][0], W[3 * rr + 1][1], W[3 * rr + 2][0], W[3 * rr + 2][1]);
    }, [&](auto rc) {
#pragma unroll
      for (int k = 0; k < 6; ++k) cs[k & 1] = MFMA(ones8(), W[3 * decltype(rc)::value + (k >> 1)][k & 1], cs[k & 1]);
      tie(cs[0], cs[1]);
    });
#pragma unroll
    for (int m = 0; m < 2; ++m) {
      const float cm = cs[m][0] * (-1.0f / 576.0f);
      const f32x4 c4 = {cm, cm, cm, cm};
#pragma unroll
      for (int k = 0; k < 18; ++k) { W[k][m] = asb(pack8(MFMA(asb(sel0), W[k][m], c4), MFMA(asb(sel1), W[k][m], c4))); tie(W[k][m]); }
    }
  }
  const char* hb = cx.ws + WS_H + (size_t)(cx.g * 6 + c / 6) * 64 * HB + (c % 6) * 4096 + wave * 512 - HB;
  const unsigned l16 = vopq(lane * 16), hv = vopq((g >> 1) * 256 + r * 16);
  const unsigned la[2] = {vopq(sm0 + l16), vopq(sm0 + l16 + 2 * TB)};
  f32x4 ag, au;
  Ring<18, PDG, 2> bq;
  auto mm = [&](auto kc, auto jc, const bf16x8& x) {
    constexpr int k = decltype(kc)::value;
    if constexpr (decltype(jc)::value == 0) mf<k == 0>(ag, W[k][0], x);
    if constexpr (decltype(jc)::value == 1) mf<k == 0>(au, W[k][1], x);
  };
  auto epi = [&]() {
    tie(ag, au);
    return widen(cvt2(ag[0] * au[0], ag[1] * au[1]), cvt2(ag[2] * au[2], ag[3] * au[3]));
  };
  if (wave < 4) {
    const char* xs = cx.ws + WS_X1 + (size_t)cx.g * 64 * TB + wave * 4608;
    const char* fr = cx.fl + TLAG * 128;
    const unsigned pv = vopq((F_X1 + (lane & 1)) * FSTR), l4 = vopq(l16 + 4096), mb = sm0 + wave * 4608;
    auto fetch = [&](auto sc) { dma<decltype(sc)::value * TB, 4>(xs, l16, mb); dma<decltype(sc)::value * TB + 4096, 0>(xs, l4, mb); };
    unsigned dl = cx.dl;
    int tgt = (wave == 1 && c < 32) ? c + 2 : 99;
    auto scale = [&]() {
      f32x2 t = *(const f32x2*)(smem + SUMS + lane * 8);
#pragma unroll
      for (int e = 0; e < 2; ++e) {
        const auto a = __builtin_amdgcn_permlane16_swap(f2i(t[e]), f2i(t[e]), false, false);
        const float h = u2f(a[0]) + u2f(a[1]);
        const auto b = __builtin_amdgcn_permlane32_swap(f2i(h), f2i(h), false, false);
        t[e] = u2f(b[0]) + u2f(b[1]);
      }
      const float is = row_stat(t[0], t[1])[1];
      gst4m<-1>(cx.ws + WS_IS + (cx.g * 64 + tgt - 2) * 128, lane * 4, f2i(is * is));
      tgt += 32;
    };
    poll(fr, pv, tag, dl); fetch(IC<0>{});
    poll(fr + 128, pv, tag, dl);
    adv(fr, 256);
    adv(xs, TB); fetch(IC<1>{});
    prio<PR_A>();
    bar_bare();
    periods([&](auto jc, int it) {
      constexpr int j = decltype(jc)::value;
      unsigned fw;
      prime<(j & 1) * TB>(bq, la[j >> 1]);
      gld4<j * 128>(fw, fr, pv);
      stream<(j & 1) * TB, -1, 0>(bq, la[j >> 1], mm, [&](auto kc) {
        constexpr int k = decltype(kc)::value;
        if constexpr (k == 11) adv(xs, TB);
        if constexpr (k == 12 && j == 2) dl = sopq((it == 15) ? 0u : dl);
        if constexpr (k == 13) adv(hb, HB);
      });
      prio<3>();
      check_flags<0, j * 128>(fw, fr, pv, tag, dl);
      fetch(IC<(j + 2) & 3>{});
      gst16m(hb, hv, epi(), HALF);
      if constexpr (j == 3) adv(fr, 512);
      if (__builtin_expect(4 * it + j == tgt, 0)) scale();
      prio<PR_A>();
      bar_bare();
    });
    if (c == 31) bar_bare();
    if (tgt < 66) scale();
    wait_vm<0>();
    bar_bare();
  } else {
    const int kb = 2 * (c % 9) + (wave & 1);
    int tau = 2 * (c / 9) + ((wave >> 1) & 1);
    Red u = {};
    const unsigned vt = vopq(tag), iv = vopq(r * 4), pq = vopq((F_P + (kb / 6) * 6 + min(lane, 5)) * FSTR), vrow = vopq(r * 2304 + g * 16);
    u.rp = cx.ws + WS_P + (size_t)((cx.g * 18 + kb) * 64 + tau) * PB + 3072;
    u.rx = cx.ws + WS_X1 + (size_t)(cx.g * 64 + tau) * TB + kb * 1024;
    u.ri = cx.ws + WS_IS + (cx.g * 64 + tau) * 128;
    u.rf = cx.fl + (tau + PLAG) * 128;
    u.f2 = cx.ws + WS_F2 + ((cx.g & 1) * 72 + top(cx.g) + tau - 8) * F2ROW + kb * 128;
    u.ob = cx.ws + WS_X2 + (size_t)((cx.g & 1) * 64 + top(cx.g) + tau) * TB + kb * 1024;
    const char* ob2 = (const char*)out + (size_t)((cx.g >> 1) * 1024 + 32 * (31 - tau) + (cx.g & 1)) * (DM * 2) + kb * 64;
    gst16m(u.f2 + 8 * F2ROW, (lane >> 3) * (8 * F2ROW) + (lane & 7) * 16, u32x4{~tag, ~tag, ~tag, ~tag}, 0xFFFFFFFFull);
    auto ask = [&](int step) { reduce_ask(u, l16, iv, pq, vt, tag, cx.dl, step); };
    auto finish = [&](int tau_) {
      f32x4 v0, v1; float x[8];
      reduce_sum(u, sel0, sel1, v0, v1, x);
      adv(u.rp, 0); adv(u.rx, 0); adv(u.ri, 0); adv(u.rf, 0); adv(u.f2, 0); adv(u.ob, 0);
      const int tau = sopq(tau_);
      const char* o = u.ob;
      const unsigned ov = vopq((tau >= 32) ? vrow : l16);
      adv(u.rp, 8 * PB); adv(u.rx, 8 * TB); adv(u.ri, 8 * 128); adv(u.rf, 8 * 128);
      adv(u.f2, (tau < 32) ? 8 * F2ROW : 0);
      u.ob = ((tau >> 3) == 3) ? ob2 : u.ob + ((tau < 32) ? 8l * TB : -8l * 32 * DM * 2); adv(u.ob, 0);
      tie(v0, v1);
      const float sc = u2f(u.i2);
      gst16(o, ov, u32x4{cvt2(fmaf(v0[0], sc, x[0]), fmaf(v0[1], sc, x[1])), cvt2(fmaf(v0[2], sc, x[2]), fmaf(v0[3], sc, x[3])),
                         cvt2(fmaf(v1[0], sc, x[4]), fmaf(v1[1], sc, x[5])), cvt2(fmaf(v1[2], sc, x[6]), fmaf(v1[3], sc, x[7]))});
    };
    int step = -1, tgt = (c < 32) ? c : 99;
    const char* fp = cx.fp - 512;
    bar_bare();
    prime<0>(bq, la[0]);
    wait_lg<0>();
    periods([&](auto jc, int it) {
      constexpr int j = decltype(jc)::value, j1 = (j + 1) & 3;
      stream<(j & 1) * TB, BARG, 63>(bq, la[j >> 1], mm, [&](auto kc) {
        constexpr int k = decltype(kc)::value;
        if constexpr (k == 9 && j == 0) adv(fp, 512);
        if constexpr (k == 10) adv(hb, HB);
        if constexpr (k == BARG - 2) wait_store(step);
      });
      wait_vm<0>();
      flag_by<(j + 1) * 128>(cx.w0, fp, l16, vt);
      asm volatile("s_nop 3");
      u32x4 h = epi();
      gst16m(hb, hv, h, HALF);
      step = 4 * it + j - LAGF - (tau | 1);
      ask(step);
      if (__builtin_expect(step == 2, 0)) finish(tau);
      tau += (step == 3) ? 8 : 0;
      if (__builtin_expect(4 * it + j == tgt, 0)) {
        f32x4 s1 = {0.f, 0.f, 0.f, 0.f}, s2 = s1;
        bf16x8 x[5];
#pragma unroll
        for (int k = 0; k < 5; ++k) x[k] = *(const bf16x8*)(smem + j * TB + (k < 4 ? 4 * (wave - 4) + k : 16 + (wave & 1)) * 1024 + lane * 16);
        if (wave >= 6) x[4] = bf16x8{0, 0, 0, 0, 0, 0, 0, 0};
#pragma unroll
        for (int k = 0; k < 5; ++k) { s1 = MFMA(ones8(), x[k], s1); s2 = MFMA(x[k], x[k], s2); }
        if (g == q) *(f32x2*)(smem + SUMS + (wave - 4) * 128 + r * 8) = f32x2{s1[0], diag(s2, p)};
        tgt += 32;
      }
      prio<0>();
      prime<(j1 & 1) * TB>(bq, la[j1 >> 1]);
      if constexpr (j == 3) wait_lg<0>();
    });
    if (c == 31) bar_bare();
    wait_vm<0>();
    bar_bare();
    flag_by<640>(cx.w0, fp, l16, vt);
    if (step == 2) tau += 8;
#pragma nounroll
    while (tau < 64) {
      ask(0); wait_vm<0>(); ask(1); wait_vm<0>(); ask(2);
      finish(tau); tau += 8;
    }
  }
}

__device__ __forceinline__ void role_dn(char* smem, const u16* wd, const Ctx& cx, int ks, int ns) {
  const int wave = cx.wave, lane = cx.lane;
  const unsigned sm0 = lds_addr(smem);
  unsigned tag;
  const int r = lane & 15, g = lane >> 4, q = r >> 2, p = lane & 3;
  const int kq = wave >> 2, nq = wave & 3;
  constexpr int EX = 4 * HB;
  bf16x8 W[12][3];
  {
    constexpr int BUF = 49152;
    const char* src = (const char*)wd + (size_t)(768 * ks) * (DM * 2) + ns * 384;
    unsigned vo[6];
#pragma unroll
    for (int j = 0; j < 6; ++j) {
      const unsigned ch = (wave * 6 + j) * 64 + lane, row = ch / 24;
      vo[j] = ((row >> 6) * 384 + (row & 63)) * (DM * 2) + (ch - row * 24) * 16;
    }
    const char* rb = smem + (kq * 64 + 8 * g + q) * 384;
    load_w<6, 6>(smem, cx, tag, [&](auto rc) {
      constexpr int rr = decltype(rc)::value;
#pragma unroll
      for (int j = 0; j < 6; ++j) dma_w2(src + rr * 64 * (DM * 2), vo[j], sm0 + (rr % 3) * BUF + (wave * 6 + j) * 1024);
    }, [&](auto rc) {
      constexpr int rr = decltype(rc)::value;
#pragma unroll
      for (int kl = 0; kl < 2; ++kl)
#pragma unroll
        for (int m = 0; m < 3; ++m) {
          const char* a = rb + (rr % 3) * BUF + kl * 32 * 384 + ((m < 2) ? nq * 64 + p * 16 + m * 8 : 256 + nq * 32 + p * 8);
          W[2 * rr + kl][m] = tr_frag(a, a + 4 * 384);
        }
      tie(W[2 * rr][0], W[2 * rr][1], W[2 * rr][2], W[2 * rr + 1][0], W[2 * rr + 1][1], W[2 * rr + 1][2]);
    }, [](auto) {});
  }
  const unsigned l16 = vopq(lane * 16);
  const unsigned la[2] = {vopq(sm0 + l16 + kq * 12288), vopq(sm0 + l16 + kq * 12288 + 2 * HB)};
  const unsigned ea = vopq(sm0 + EX + nq * 3072 + l16);
  f32x4 acc[3];
  auto mm = [&](auto kc, auto jc, const bf16x8& x) {
    constexpr int k = decltype(kc)::value;
    if constexpr (decltype(jc)::value < 3) mf<k == 0>(acc[decltype(jc)::value % 3], W[k][decltype(jc)::value % 3], x);
  };
  if (wave < 4) {
    Ring<12, PDD, 2> bq;
    const char* hs = cx.ws + WS_H + (size_t)(cx.g * 6 + ks) * 64 * HB + nq * 6144;
    const char* fr = cx.fl + TLAG * 128;
    const unsigned l3 = vopq(l16 + 3072), mb = sm0 + nq * 6144, pv = vopq((F_H + 6 * ks + min(lane, 5)) * FSTR);
    auto fetch = [&](auto sc) { dma<decltype(sc)::value * HB, 3>(hs, l16, mb); dma<decltype(sc)::value * HB + 3072, 3>(hs, l3, mb); };
    const unsigned lm[3] = {l16, vopq(l16 + 2048), vopq(l16 + 4096)};
    unsigned dl = cx.dl;
    poll(fr, pv, tag, dl); fetch(IC<0>{});
    poll(fr + 128, pv, tag, dl);
    adv(fr, 256);
    adv(hs, HB); fetch(IC<1>{});
    prio<PR_A>();
    bar_bare();
    periods([&](auto jc, int it) {
      constexpr int j = decltype(jc)::value;
      unsigned fw;
      prime<(j & 1) * HB>(bq, la[j >> 1]);
      gld4<j * 128>(fw, fr, pv);
      stream<(j & 1) * HB, -1, 0>(bq, la[j >> 1], mm, [&](auto kc) {
        constexpr int k = decltype(kc)::value;
        if constexpr (k == 6) adv(hs, HB);
        if constexpr (k == 7 && j == 2) dl = sopq((it == 15) ? 0u : dl);
      });
      prio<3>();
      check_flags<0, j * 128>(fw, fr, pv, tag, dl);
      asm volatile("s_nop 1");
      static_for<0, 3>([&](auto mc) {
        constexpr int m = decltype(mc)::value;
        dma<((j + 2) & 3) * HB + m * 2048, 2>(hs, lm[m], mb);
        lds_wr<(j & 1) * 12288 + m * 1024>(ea, acc[m]);
      });
      prio<PR_A>();
      if constexpr (j == 3) adv(fr, 512);
      bar_raw();
    });
    bar_bare();
    wait_vm<0>();
  } else {
    Ring<12, 12, 12> bq;
    const char* pd = cx.ws + WS_P + (size_t)(cx.g * 18 + 6 * ns + nq) * 64 * PB + ks * 1024 - PB;
    const unsigned hv = vopq((g >> 1) * 256 + r * 16 + (4 + (nq >> 1) - nq) * 64 * PB + (nq & 1) * 512), vt = vopq(tag);
    const char* fp = cx.fp - 512;
    bar_bare();
    prime<0>(bq, la[0]);
    wait_lg<0>();
    periods([&](auto jc, int it) {
      constexpr int j = decltype(jc)::value, j1 = (j + 1) & 3;
      f32x4 e[3];
      stream<(j & 1) * HB, BARD, (2)>(bq, la[j >> 1], mm, [&](auto kc) {
        constexpr int k = decltype(kc)::value;
        if constexpr (k == 5 && j == 0) adv(fp, 512);
        if constexpr (k == 6) adv(pd, PB);
        if constexpr (k == BARD) static_for<0, 3>([&](auto mc) { constexpr int m = decltype(mc)::value; lds_rd<(j & 1) * 12288 + m * 1024>(e[m], ea); });
      });
      flag_by<(j + 1) * 128>(cx.w0, fp, l16, vt);
      asm volatile("s_nop 3");
      wait_lg<0>();
      tie(acc[0], acc[1], acc[2], e[0], e[1], e[2]);
      gst16(pd, l16, pack8(e[0] + acc[0], e[1] + acc[1]));
      e[2] += acc[2];
      gst16m(pd, hv, widen(cvt2(e[2][0], e[2][1]), cvt2(e[2][2], e[2][3])), HALF);
      prio<0>();
      prime<(j1 & 1) * HB>(bq, la[j1 >> 1]);
      if constexpr (j == 3) wait_lg<0>();
    });
    wait_vm<0>();
    bar_bare();
    flag_by<640>(cx.w0, fp, l16, vt); flag_by<768>(cx.w0, fp, l16, vt);
  }
}

template <bool A> __device__ __forceinline__ void role_wo(char* smem, const u16* wo, const Ctx& cx, int jw) {
  const int wave = cx.wave, lane = cx.lane;
  const unsigned sm0 = lds_addr(smem);
  unsigned tag;
  const int r = lane & 15, g = lane >> 4, q = r >> 2, p = lane & 3;
  constexpr int RS = 4 * TB, RT = 10240, EX = RS + 4 * RT;
  bf16x8 W[18][2], WX[9];
  {
    constexpr int BUF = 40960;
    const char* src = (const char*)wo + jw * 576;
    unsigned vo[5], vx[5];
#pragma unroll
    for (int i = 0; i < 5; ++i) {
      const unsigned ch = min((wave * 5 + i) * 64 + lane, 2303), row = ch / 36, cc = (ch - row * 36) * 16;
      const unsigned kl = row >> 5, gg = (row >> 3) & 3, e = row & 7;
      vo[i] = (36 * kl + 20 * (e >> 2) + 4 * gg + (e & 3)) * (DM * 2) + cc;
      vx[i] = (72 * (4 * kl + gg) + 16 + 36 * (e >> 2) + (e & 3)) * (DM * 2) + cc;
    }
    const char* rb = smem + (8 * g + q) * 576 + p * 16;
    load_w<9, 5>(smem, cx, tag, [&](auto rc) {
      constexpr int rr = decltype(rc)::value;
#pragma unroll
      for (int i = 0; i < 5; ++i) dma_w(src + (rr < 8 ? rr * 72 * (DM * 2) : 0), rr < 8 ? vo[i] : vx[i], sm0 + (rr % 3) * BUF + (wave * 5 + i) * 1024);
    }, [&](auto rc) {
      constexpr int rr = decltype(rc)::value;
      bf16x8 wx[2];
#pragma unroll
      for (int kl = 0; kl < 2; ++kl) {
        const char* a = rb + (rr % 3) * BUF + kl * 32 * 576;
#pragma unroll
        for (int m = 0; m < 2; ++m) W[2 * rr + kl][m] = tr_frag(a + wave * 64 + m * 8, a + wave * 64 + m * 8 + 4 * 576);
        if constexpr (A) wx[kl] = tr_frag(a + 512 + (wave & 1) * 8, a + 512 + (wave & 1) * 8 + 4 * 576);
      }
      tie(W[2 * rr][0], W[2 * rr][1], W[2 * rr + 1][0], W[2 * rr + 1][1]);
      if constexpr (A) {
#pragma unroll
        for (int kl = 0; kl < 2; ++kl) {
          const int k = 2 * rr + kl;
          if (k < 9) WX[k % 9] = wx[kl]; else if (wave & 2) WX[k % 9] = wx[kl];
          tie(WX[k % 9]);
        }
      }
    }, [](auto) {});
  }
  const unsigned l16 = vopq(lane * 16), l3 = vopq(l16 + 3072);
  const unsigned la[2] = {vopq(sm0 + l16), vopq(sm0 + l16 + 2 * TB)};
  const unsigned ra = vopq(sm0 + RS + wave * 1024 + l16), mr = sm0 + RS + wave * 1024;
  const char* xo = cx.ws + WS_X1 + (size_t)cx.g * 64 * TB + jw * 9216 + wave * 1024 - TB;
  const char* rs = cx.ws + WS_X0 + (size_t)((cx.g & 1) * 64 + top(cx.g)) * TB + (9 * jw + wave) * 1024;
  constexpr long RSW = TB - 32l * TB - (long)(WS_X0 - WS_X2);
  poll(cx.ws + WS_F2 + ((cx.g & 1) * 72 + top(cx.g)) * F2ROW, F_X0 * 128 + (lane & 1) * F2ROW, tag, cx.dl);
  f32x4 a0, a1, ax;
  Ring<18, PDG, 1> bq;
  auto init = [&](const u32x4& x) {
    a0 = f32x4{blo(x[0]), bhi(x[0]), blo(x[1]), bhi(x[1])}; a1 = f32x4{blo(x[2]), bhi(x[2]), blo(x[3]), bhi(x[3])};
    tie(a0, a1);
  };
  const unsigned l8 = vopq(l16 + (8 - wave) * 1024), r8 = vopq(ra + 8192 + (wave & 1) * 8);
  auto init_x = [&](const u32x2& x) { ax = f32x4{blo(x[0]), bhi(x[0]), blo(x[1]), bhi(x[1])}; tie(ax); };
  dma<0, 1>(rs, l16, mr); if (wave < 2) dma<8192, 1>(rs, l8, mr);
  adv(rs, TB);
  dma<RT, 1>(rs, l16, mr); if (wave < 2) dma<RT + 8192, 1>(rs, l8, mr);
  wait_vm<0>();
  init(*(const u32x4*)(smem + RS + wave * 1024 + lane * 16));
  if constexpr (A) init_x(*(const u32x2*)(smem + RS + 8192 + (wave & 1) * 1032 + lane * 16));
  if constexpr (A) {
    const unsigned ea = vopq(sm0 + EX + wave * 1024 + l16);
    prio<PR_A>();
    bar_bare();
    auto loop = [&](auto hc) {
      constexpr int KH = decltype(hc)::value;
      auto mm = [&](auto kc, auto jc, const bf16x8& x) {
        constexpr int k = decltype(kc)::value;
        if constexpr (decltype(jc)::value == 0) mf<false>(a0, W[k][0], x);
        if constexpr (decltype(jc)::value == 1) mf<false>(a1, W[k][1], x);
        if constexpr (decltype(jc)::value == 2 && k / 9 == KH) mf<k == 9>(ax, WX[k % 9], x);
      };
      periods([&](auto jc, int it) {
        constexpr int j = decltype(jc)::value;
        u32x4 x; u32x2 x8;
        prime<(j & 1) * TB>(bq, la[j >> 1]);
        stream<(j & 1) * TB, -1, 0>(bq, la[j >> 1], mm, [&](auto kc) {
          constexpr int k = decltype(kc)::value;
          if constexpr (k == 12) adv(rs, (j == 2 && it == 7) ? RSW : TB);
          if constexpr (k == 13) adv(xo, TB);
          if constexpr (k == 16) { wait_vm<0>(); lds_rd<((j + 1) & 3) * RT>(x, ra); }
          if constexpr (KH == 0 && k == 17) lds_rd8<((j + 1) & 3) * RT>(x8, r8);
        });
        prio<3>();
        dma<((j + 2) & 3) * RT, 1>(rs, l16, mr);
        if constexpr (KH == 0) dma<((j + 2) & 3) * RT + 8192, 1>(rs, l8, mr);
        asm volatile("s_nop 3" : "+v"(a0), "+v"(a1), "+v"(ax) :: "memory");
        gst16(xo, l16, pack8(a0, a1));
        asm volatile("s_waitcnt lgkmcnt(0)" : "+v"(x) :: "memory");
        init(x);
        prio<0>();
        lds_wr<(j & 1) * 4096>(ea, ax);
        if constexpr (KH == 0) { asm volatile("s_waitcnt lgkmcnt(0)" : "+v"(x8) :: "memory"); init_x(x8); }
        prio<PR_A>();
        bar_raw();
      });
      wait_vm<0>();
      bar_bare();
    };
    if (wave < 2) loop(IC<0>{}); else loop(IC<1>{});
  } else {
    const int b = (wave == 4) ? 0 : wave - 5;
    const char* os = cx.ws + WS_O + (size_t)(cx.g * 68 + 3) * TB + b * 6144;
    const unsigned mb = sm0 + b * 6144, ea = vopq(sm0 + EX + l16), pv = vopq((F_O + min(lane, 23)) * FSTR);
    auto mm = [&](auto kc, auto jc, const bf16x8& x) {
      constexpr int k = decltype(kc)::value;
      if constexpr (decltype(jc)::value == 0) mf<false>(a0, W[k][0], x);
      if constexpr (decltype(jc)::value == 1) mf<false>(a1, W[k][1], x);
    };
    auto loop = [&](auto pc) {
      constexpr bool SH = decltype(pc)::value;
      auto fetch = [&](auto sc) { dma<decltype(sc)::value * TB, 3>(os, l16, mb); dma<decltype(sc)::value * TB + 3072, 3>(os, l3, mb); };
      const char *fr = cx.fl + OLAG * 128, *fp = cx.fp - 512;
      const unsigned vt = vopq(tag);
      unsigned dl = cx.dl;
      if constexpr (!SH) { poll(fr, pv, tag, dl); fetch(IC<0>{}); wait_vm<0>(); }
      bar_bare();
      if constexpr (!SH) { poll(fr + 128, pv, tag, dl); adv(os, TB); fetch(IC<1>{}); }
      adv(fr, 256);
      unsigned fw;
      if constexpr (!SH) gld4<0>(fw, fr, pv);
      prime<0>(bq, la[0]);
      wait_lg<0>();
      periods([&](auto jc, int it) {
        constexpr int j = decltype(jc)::value, j1 = (j + 1) & 3;
        u32x4 x; f32x4 e[4];
        stream<(j & 1) * TB, BARG, (SH ? 0 : 1)>(bq, la[j >> 1], mm, [&](auto kc) {
          constexpr int k = decltype(kc)::value;
          if constexpr (!SH && k == 10 && j == 2) dl = sopq((it == 15) ? 0u : dl);
          if constexpr (SH && k == 10 && j == 0) adv(fp, 512);
          if constexpr (SH && k == BARG) gst4m<1, (j + 1) * 128>(fp, l16, vt);
          if constexpr (k == 11) adv(rs, (j == 2 && it == 7) ? RSW : TB);
          if constexpr (k == 12) adv(xo, TB);
          if constexpr (!SH && k == 9) adv(os, (j == 2 && it == 7) ? 2 * TB : TB);
        });
        if constexpr (!SH) { check_flags<0, j * 128>(fw, fr, pv, tag, dl); fetch(IC<(j + 2) & 3>{}); }
        dma<((j + 2) & 3) * RT, 1>(rs, l16, mr);
        prio<0>();
        if constexpr (SH) {
          static_for<0, 4>([&](auto mc) { constexpr int m = decltype(mc)::value; lds_rd<(j & 1) * 4096 + m * 1024>(e[m], ea); });
        }
        lds_rd<j1 * RT>(x, ra);
        prio<3>();
        tie(a0, a1);
        gst16(xo, l16, pack8(a0, a1));
        asm volatile("s_waitcnt lgkmcnt(0)" : "+v"(x) :: "memory");
        if constexpr (SH) {
          tie(e[0], e[1], e[2], e[3]);
          gst16(xo + 3072, l16, pack8(e[0] + e[2], e[1] + e[3]));
        }
        init(x);
        prio<0>();
        prime<(j1 & 1) * TB>(bq, la[j1 >> 1]);
        if constexpr (j == 1) adv(fr, (it == 7) ? 128 : 0);
        if constexpr (!SH) gld4<(j + 1) * 128>(fw, fr, pv);
        if constexpr (j == 3) { adv(fr, 512); wait_lg<0>(); }
      });
      wait_vm<0>();
      if constexpr (!SH) tie(fw);
      bar_bare();
      if constexpr (SH) gst4m<1, 640>(fp, l16, vt);
    };
    if (wave == 5) loop(std::true_type{}); else loop(std::false_type{});
  }
}

template <class F> __device__ __forceinline__ void qa_periods(F&& f) {
#pragma nounroll
  for (int it = 0; it < 16; ++it) static_for<0, 4>([&](auto jc) { f(jc, IC<0>{}, it); });
  static_for<0, 4>([&](auto jc) { f(jc, IC<1>{}, 16); });
}
template <bool A> __device__ __forceinline__ void role_qa(char* smem, const u16* wq, const Ctx& cx, int jq) {
  const int wave = cx.wave, lane = cx.lane;
  const unsigned sm0 = lds_addr(smem);
  unsigned tag;
  const int r = lane & 15, g = lane >> 4, q = r >> 2, p = lane & 3;
  constexpr int XR = 4 * TB, PP = XR + 2048, ST = PP + 8192;
  const f32x4 z4 = {0.f, 0.f, 0.f, 0.f};
  const unsigned l16 = vopq(lane * 16);
  const bool low = cx.g < 2;
  const char* xs = cx.ws + WS_X0 + (size_t)((cx.g & 1) * 64 + top(cx.g)) * TB + (wave & 3) * 4096;
  const char* fr = cx.ws + WS_F2 + ((cx.g & 1) * 72 + top(cx.g)) * F2ROW;
  const unsigned mb = sm0 + (wave & 3) * 4096, l4 = vopq(l16 + 4096);
  auto fetch = [&](auto sc, auto nc) {
    dma<decltype(sc)::value * TB, 4>(xs, l16, mb);
    if constexpr (decltype(nc)::value == 6) dma<decltype(sc)::value * TB + 4096, 2>(xs, l4, mb);
  };
  bf16x8 W[18][2];
  f32x4 cs[2] = {z4, z4};
  bf16x8 one = ones8();
  tie(one);
  {
    constexpr int BUF = 41472, B0 = 2 * TB;
    const char* src = (const char*)wq + jq * 144 + wave * 12 * (NQKV * 2);
    const unsigned sg = (min(lane, 53) * 57) >> 9, vo = sg * (DM * 2 - 144) + min(lane, 53) * 16;
    int c0, c1;
    if (!A) { c0 = 36 * (wave & 3) + (wave & 1) * 4 + p * 8; c1 = c0 + 144; }
    else if (wave < 2) { c0 = 288 + 72 * wave + p * 8; c1 = c0 + 40; }
    else { c0 = (p >> 1) * 144 + 32 + (p & 1) * 72; c1 = 288 + 32 + (p & 1) * 72; }
    const char* rb = smem + B0 + (8 * g + q) * 432;
    unsigned f0; const unsigned f0v = vopq(F_X0 * 128 + (lane & 3) * F2ROW);
    load_w<6, 6, 5>(smem, cx, tag, [&](auto rc) {
      constexpr int rr = decltype(rc)::value;
      if constexpr (rr == 5) gld4<0>(f0, fr, f0v);
#pragma unroll
      for (int i = 0; i < 6; ++i) dma_w54(src + (rr * 96 + 2 * i) * (NQKV * 2), vo, sm0 + B0 + (rr % 3) * BUF + (wave * 6 + i) * 864);
    }, [&](auto rc) {
      constexpr int rr = decltype(rc)::value;
#pragma unroll
      for (int kl = 0; kl < 3; ++kl)
#pragma unroll
        for (int m = 0; m < 2; ++m) {
          const char* a = rb + (rr % 3) * BUF + kl * 32 * 432 + (m ? c1 : c0);
          W[3 * rr + kl][m] = tr_frag(a, a + 4 * 432);
        }
      tie(W[3 * rr][0], W[3 * rr][1], W[3 * rr + 1][0], W[3 * rr + 1][1], W[3 * rr + 2][0], W[3 * rr + 2][1]);
    }, [&](auto rc) {
      if constexpr (decltype(rc)::value == 3) {
        check_flags<6>(f0, fr, f0v, tag, cx.dl);
        const char* x2 = xs - (wave & 3) * 4096 + wave * 4608;
        dma<0, 4>(x2, l16, sm0 + wave * 4608); dma<4096, 0>(x2, l4, sm0 + wave * 4608);
      }
#pragma unroll
      for (int k = 0; k < 6; ++k) cs[k & 1] = MFMA(one, W[3 * decltype(rc)::value + (k >> 1)][k & 1], cs[k & 1]);
      tie(cs[0], cs[1]);
    });
  }
  auto quad = [&](const f32x4& c) {
    f32x4 o;
#pragma unroll
    for (int j = 0; j < 4; ++j) o[j] = u2f(__builtin_amdgcn_ds_bpermute((4 * g + j) * 4, f2i(c[0])));
    return o;
  };
  const unsigned la[2] = {vopq(sm0 + l16), vopq(sm0 + l16 + 2 * TB)};
  if constexpr (A) {
    Ring<18, PDG, 1> bq;
    unsigned pv = vopq(F_X0 * 128), dl = cx.dl, fw = vopq(tag);
    adv(fr, 2 * F2ROW); adv(xs, 2 * TB);
    auto feed = [&](auto jc, auto nc, int it) {
      constexpr int j = decltype(jc)::value;
      const char* xn = xs + TB;
      adv(fr, F2ROW);
      if constexpr (j == 1) { const bool st = low && it == 7; dl = st ? 0u : dl; xn = st ? xs : xn; }
      if constexpr (j == 2) {
        const bool st = low && it == 15; dl = st ? 0u : cx.dl; xn = st ? xs : xn;
        if (it == 7) { xn = cx.ws + WS_X2 + (size_t)((cx.g & 1) * 64 + top(cx.g)) * TB + wave * 4096; fr -= 33 * F2ROW; pv = vopq(min(lane, 17) * 128); }
      }
      gld4<0>(fw, fr, pv);
      fetch(IC<(j + 2) & 3>{}, nc);
      xs = xn; adv(xs, 0);
    };
    if (wave == 3) {
      f32x4 a0, a1;
      const unsigned sa = vopq(sm0 + ST + ((g == q) ? r * 8 : 256));
      auto mm = [&](auto kc, auto jc, const bf16x8& x) {
        constexpr int k = decltype(kc)::value;
        if constexpr (decltype(jc)::value == 0) mf<k == 0>(a0, one, x);
        if constexpr (decltype(jc)::value == 1) mf<k == 0>(a1, x, x);
      };
      prio<PR_A>();
      bar_raw();
      qa_periods([&](auto jc, auto ec, int it) {
        constexpr int j = decltype(jc)::value; constexpr bool END = decltype(ec)::value;
        if constexpr (!END || j < 2) {
          prime<(j & 1) * TB>(bq, la[j >> 1]);
          stream<(j & 1) * TB, -1, 0>(bq, la[j >> 1], mm, [&](auto kc) {
            constexpr int k = decltype(kc)::value;
          });
          prio<0>();
          if constexpr (!END) { check_flags<6>(fw, fr, pv, tag, dl); feed(jc, IC<6>{}, it); }
          else { wait_vm<0>(); tie(fw); settle(); }
          tie(a0, a1);
          lds_wr8<(j & 1) * 128>(sa, row_stat(a0[0], diag(a1, p)));
          prio<PR_A>();
          if constexpr (!END) wait_vm<7>();
        }
        bar_raw();
      });
      if (cx.g == 0 && jq == 0) {
        for (int g2 = 0; g2 < 4; ++g2) poll(cx.ws + WS_FL + g2 * FGRP + 128, lane * FSTR, tag, cx.dl);
        gst4m<1>(cx.ws, l16, vopq((tag - 0x7F4A7C15u) * 244002641u + 1u));
      }
    } else {
      const unsigned sa = vopq(sm0 + ST + g * 32), xa = vopq(sm0 + XR + l16), vt = vopq(tag);
      auto loop = [&](auto xc) {
        constexpr bool X = decltype(xc)::value;
        if constexpr (X) *(f32x4*)(smem + XR + 1024 + lane * 16) = quad(cs[0]);
        const float cv[2] = {cs[0][0], cs[1][0]};
        f32x4 a[2][2] = {{z4, z4}, {z4, z4}};
        u32x4 vw[2] = {{0u, 0u, 0u, 0u}, {0u, 0u, 0u, 0u}};
        const unsigned pa = vopq(sm0 + PP + (X ? 0 : wave * 2048) + l16);
        const char* ob = cx.ws + WS_O + (size_t)cx.g * 68 * TB + (X ? 16384 + jq * 256 : (2 * jq + wave) * 1024) - TB;
        const char* fp = cx.fp + wave * 8 * FSTR;
        prio<PR_A>();
        bar_raw();
        qa_periods([&](auto jc, auto ec, int it) {
          constexpr int j = decltype(jc)::value, e = j & 1, o = 1 - e; constexpr bool END = decltype(ec)::value;
          bf16x8 pf[4]; f32x4 d[4], s0, s1;
          auto reads = [&](auto ic) {
            constexpr int i = decltype(ic)::value;
            if constexpr (i < (X ? 4 : 2)) lds_rd<o * 4096 + i * 1024>(pf[i], pa);
            if constexpr (i == 4) lds_rd<o * 128>(s0, sa);
            if constexpr (i == 5) lds_rd<o * 128 + 16>(s1, sa);
          };
          auto mm = [&](auto kc, auto ic, const bf16x8& x) {
            constexpr int k = decltype(kc)::value;
            if constexpr (decltype(ic)::value == 0) { if constexpr (X) mf<k == 0>(a[e][0], W[k][0], x); else mf<k == 0>(a[e][0], x, W[k][0]); }
            if constexpr (decltype(ic)::value == 1) mf<k == 0>(a[e][1], x, W[k][1]);
          };
          if constexpr (!END || j < 2) {
            prime<e * TB>(bq, la[j >> 1]);
            stream<e * TB, -1, 0>(bq, la[j >> 1], mm, [&](auto kc) {
              constexpr int k = decltype(kc)::value;
              if constexpr (k >= 3 && k < 9) reads(IC<k - 3>{});
              if constexpr (k == 13) adv(ob, TB);
            });
          } else { adv(ob, TB); static_for<0, 6>([&](auto ic) { reads(ic); }); }
          if constexpr (END && j >= 2) wait_frag<0>();
          static_for<0, X ? 4 : 2>([&](auto hc) { constexpr int h = decltype(hc)::value; mf<true>(d[h], asb(vw[X ? 1 : h]), pf[h]); });
          asm volatile("s_setprio 0" : "+v"(s0), "+v"(s1) :: "memory");
          if constexpr (!END) check_flags<4>(fw, fr, pv, tag, dl); else { wait_vm<0>(); tie(fw); settle(); }
          gst4m<1, j * 128>(fp, l16, vt);
          if constexpr (X) {
            if constexpr (!END || j < 2) lds_wr<e * 1024>(xa, a[e][0]);
            tie(d[0], d[1], d[2], d[3]);
            unsigned e2 = cvt2(d[2][0], d[2][1]), e3 = cvt2(d[3][2], d[3][3]), e0 = cvt2(d[0][0], d[0][1]), e1 = cvt2(d[1][2], d[1][3]), n2, n3;
            asm volatile("s_nop 0\n\tv_permlane16_swap_b32 %0, %2\n\tv_permlane16_swap_b32 %1, %3" : "+v"(e2), "+v"(e3), "=&v"(n2), "=&v"(n3) : "v"(e0), "v"(e1));
            gst16m(ob, l16, u32x4{e0, e1, n2, n3}, 0xFFFFull);
          } else { tie(d[0], d[1]); gst16(ob, l16, pack8(d[0], d[1])); }
          if constexpr (!END) feed(jc, IC<4>{}, it);
#pragma unroll
          for (int m = X ? 1 : 0; m < 2; ++m) {
            const f32x4 w = a[o][m];
            vw[m][2 * o] = cvt2(fmaf(cv[m], s0[0], w[0]) * s0[1], fmaf(cv[m], s0[2], w[1]) * s0[3]);
            vw[m][2 * o + 1] = cvt2(fmaf(cv[m], s1[0], w[2]) * s1[1], fmaf(cv[m], s1[2], w[3]) * s1[3]);
            tie(vw[m]);
          }
          prio<PR_A>();
          if constexpr (j == 3) adv(fp, 512);
          if constexpr (!END) wait_vm<7>();
          bar_raw();
        });
        wait_vm<0>();
        gst4m<1>(fp, l16, vt);
      };
      if (wave == 2) loop(std::true_type{}); else loop(std::false_type{});
    }
  } else {
    const int h = wave - 4;
    f32x4 cq = quad(cs[0]), ck = quad(cs[1]);
    tie(cq, ck);
    const float ninf = -__builtin_inff();
    f32x4 bo, bp;
#pragma unroll
    for (int e = 0; e < 4; ++e) { const int m = 4 * g + e; bo[e] = (m <= r && m >= r - 7) ? 0.f : ninf; bp[e] = (m >= r + 9) ? 0.f : ninf; }
    Soft f = {};
#pragma unroll
    for (int e = 0; e < 2; ++e) f.mo[e] = vopq((4 * g + 2 * e <= r ? 0xFFFFu : 0u) | (4 * g + 2 * e + 1 <= r ? 0xFFFF0000u : 0u));
    const unsigned xa = vopq(sm0 + XR + (h >> 1) * 256 + r * 16 + (h & 1) * 8), sa = vopq(sm0 + ST + r * 8), pa = vopq(sm0 + PP + h * 1024 + l16);
    const float g0 = (g == 0) ? 1.f : 0.f;
    bar_raw();
    f32x2 cxq, cxk;
    lds_rd8<1024>(cxq, xa); lds_rd8<1536>(cxk, xa);
    bf16x8 x[18];
    auto first = [&](auto oc, unsigned a) { static_for<0, 8>([&](auto kc) { lds_rd<decltype(oc)::value + decltype(kc)::value * 1024>(x[decltype(kc)::value], a); }); };
    first(IC<0>{}, la[0]);
    wait_lg<0>();
    tie(cxq, cxk);
    f32x4 a0 = z4, a1 = z4, so = z4;
    u32x4 qf = {0u, 0u, 0u, 0u}, kf = qf;
    float sc = 0.f, scn = 0.f;
    tie(qf, kf, bo, bp);
    qa_periods([&](auto jc, auto ec, int it) {
      constexpr int j = decltype(jc)::value, e = j & 1, j1 = (j + 1) & 3; constexpr bool END = decltype(ec)::value;
      auto put = [&]() { lds_wr<e * 4096>(pa, f32x4{f.s[0], f.s[1], f.s[2], f.s[3]}); };
      if constexpr (END && j >= 2) {
        if constexpr (j == 2) { soft3(f); put(); }
        bar_raw();
      } else {
        prio<PR_B>();
        static_for<0, 5>([&](auto pc) {
          constexpr int k = 2 * decltype(pc)::value;
          wait_frag<6>();
          qk_pair<k / 2, e, e * TB + (k + 8) * 1024>(a0, a1, W[k], W[k + 1], x[k], x[k + 1], x[k + 8], x[k + 9], la[j >> 1], f);
        });
        wait_frag<4>();
        mf<false>(a0, W[10][0], x[10]);
        put();
        mf<false>(a1, W[10][1], x[10]);
        static_for<11, 14>([&](auto kc) {
          constexpr int k = decltype(kc)::value;
          wait_frag<5>();
          mf<false>(a0, W[k][0], x[k]);
          if constexpr (k == 13) asm volatile("s_setprio 3\n\ts_waitcnt lgkmcnt(0)" ::: "memory");
          mf<false>(a1, W[k][1], x[k]);
        });
        bar_bare();
        f32x2 st, xq, xk;
        mf8<e>(a0, a1, W[14], W[15], W[16], W[17], x[14], x[15], x[16], x[17], st, xk, xq, sa, xa);
        asm volatile("s_waitcnt lgkmcnt(0)" : "+v"(st), "+v"(xq), "+v"(xk) :: "memory");
        const float ix = st[1] * g0;
        kf[2] = cvt2(fmaf(cxk[0], st[0], xk[0]) * ix, fmaf(cxk[1], st[0], xk[1]) * ix);
        sc = scn; scn = st[1] * (0.23570226039551584f * 1.4426950408889634f);
        asm volatile("" : "+v"(kf), "+v"(scn), "+v"(a0), "+v"(a1));
        kf[0] = cvt2(fmaf(ck[0], st[0], a1[0]) * st[1], fmaf(ck[1], st[0], a1[1]) * st[1]);
        kf[1] = cvt2(fmaf(ck[2], st[0], a1[2]) * st[1], fmaf(ck[3], st[0], a1[3]) * st[1]);
        f32x4 sp;
        if constexpr ((!END && j == 0) || (END && j == 1)) {
          const float off = (low && (END || it == 8)) ? ninf : 0.f;
          sp = MFMA(asb(kf), asb(qf), bp + off);
        } else sp = MFMA(asb(kf), asb(qf), bp);
        __builtin_amdgcn_sched_barrier(0);
        qf[0] = cvt2(fmaf(cq[0], st[0], a0[0]), fmaf(cq[1], st[0], a0[1]));
        qf[1] = cvt2(fmaf(cq[2], st[0], a0[2]), fmaf(cq[3], st[0], a0[3]));
        qf[2] = cvt2(fmaf(cxq[0], st[0], xq[0]), fmaf(cxq[1], st[0], xq[1]));
        asm volatile("" : "+v"(qf), "+v"(sp));
        max4(f.s, so, sp);
        if constexpr (!(END && j == 1)) mfc(so, asb(kf), asb(qf), bo);
        soft1(f, sc);
        if constexpr (END) tie(bo);
        soft2<(END && j == 1) ? -1 : (j1 & 1) * TB>(f, x, la[j1 >> 1]);
        if constexpr (j == 3) wait_lg<0>();
      }
    });
  }
}

__global__ __launch_bounds__(512, 2) void k_main(const u16* x0, const u16* wqkv, const u16* wo,
                                                 const u16* wg, const u16* wu, const u16* wd,
                                                 char* ws, u16* out) {
  __shared__ __attribute__((aligned(1024))) char smem[SMEM_BYTES];
  Ctx cx;
  const unsigned dm = lds_addr(smem) + 162816;
  {
    unsigned long long pc; unsigned len;
    asm volatile("s_getpc_b64 %0\n.Lkbeg:\n\ts_mov_b32 %1, .Lkend-.Lkbeg" : "=s"(pc), "=s"(len));
    cx.code = (const char*)pc + min(((blockIdx.x & 8) * 64 + threadIdx.x) * 128, len);
    const unsigned i0 = blockIdx.x >> 3;
    if (i0 >= 22 && i0 < 30) touch(cx.code, dm);
    if (i0 >= 5 && i0 < 30) {
      const unsigned ln = min((i0 - 5) * 512 + threadIdx.x, 11519u), sg = (ln * 13108u) >> 16;
      const char* bw = (i0 * 512 + (threadIdx.x & 448) < 5 * 512 + 8640) ? (const char*)wqkv : (const char*)wo - 1728 * 1152;
      touch(bw + (sg * (1152 - 640) + ((blockIdx.x & 1) * 4 + ln) * 128), dm);
    }
  }
  cx.ws = ws;
  cx.wave = __builtin_amdgcn_readfirstlane(threadIdx.x >> 6);
  cx.lane = threadIdx.x & 63;
  asm volatile("s_cmp_eq_u32 %1, 4\n\ts_cselect_b64 %0, 1, 0" : "=s"(cx.w0) : "s"(cx.wave) : "scc");
  cx.dl = ((unsigned)__builtin_amdgcn_s_memtime() + SPIN) | 1u;
  const unsigned t0 = (unsigned)__builtin_amdgcn_s_memrealtime();
  const int bid = sopq(blockIdx.x), xh = bid & 1, x4 = bid & 3, qd = (bid >> 2) & 1, i = bid >> 3, lane = cx.lane, wave = cx.wave;
  const int d = i - 23, tri = d / 3;
  const int c = x4 * 9 + ((i - 5) >> 1), ks = (tri < 2) ? x4 : 4 + (x4 >> 1), ns = d - 3 * tri;
  cx.g = (i < 4) ? 2 * (1 - qd) + 1 - (x4 >> 1) : (i == 4) ? 2 * (1 - qd) + (x4 >> 1) : (i < 23) ? 2 * qd + ((i - 5) & 1) : 2 * (1 - qd) + (tri < 2 ? tri : xh);
  cx.fl = ws + WS_FL + cx.g * FGRP;
  cx.fp = cx.fl + (i < 4 ? F_O + xh * 4 + i : i == 4 ? F_X1 + xh : i < 23 ? F_H + c : F_P + ns * 6 + ks) * FSTR;
  const bool fast = i >= 30;
  auto warm = [&]() {
    if (wave < 2) touch(ws + min((size_t)(wave * 64 + lane) << 20, WS_END - 4), dm);
    else if (wave == 2) touch((const char*)out + min(lane << 20, 2359292), dm);
    else if (wave == 3 && i >= 5 && i < 20) {
      const int ln = min(((i - 5) * 2 + xh) * 64 + lane, 26 * 72 - 1);
      touch(ws + WS_FL + (size_t)((bid >> 1) & 3) * FGRP + F_X1 * FSTR + ln * 128, dm);
    }
  };
  if (!fast) warm();
  if (i >= 23) {
    u32x4 v[5];
    const int g0 = (bid >> 1) & 3, n0 = (fast ? i - 30 : 2 + (wave >> 2) * 7 + i - 23) * 2 + xh;
    const int k0 = fast ? 2 * wave : 4 * (wave & 3), nk = fast ? 2 : 4, wl = fast ? wave : wave & 3;
    const char* src = (const char*)x0 + (size_t)((g0 >> 1) * 1024 + 32 * (31 - n0) + (g0 & 1)) * (DM * 2);
    const char* dst = ws + WS_X0 + (size_t)((g0 & 1) * 64 + top(g0) + n0) * TB;
    const unsigned vr = (lane & 15) * 2304 + (lane >> 4) * 16;
    gld16(v[0], src + k0 * 64, vr); gld16(v[1], src + k0 * 64 + 64, vr); gld16(v[4], src + (16 + (wl & 1)) * 64, vr);
    gld16(v[2], src + (k0 + nk - 2) * 64, vr); gld16(v[3], src + (k0 + nk - 1) * 64, vr);
    const unsigned tag = get_tag(ws);
    wait_vm<0>();
    tie(v[0], v[1], v[2], v[3], v[4]);
    gst16(dst + k0 * 1024, lane * 16, v[0]); gst16(dst + k0 * 1024 + 1024, lane * 16, v[1]);
    if (wl < 2) gst16(dst + (16 + wl) * 1024, lane * 16, v[4]);
    if (!fast) { gst16(dst + k0 * 1024 + 2048, lane * 16, v[2]); gst16(dst + k0 * 1024 + 3072, lane * 16, v[3]); }
    wait_vm<0>();
    bar_bare();
    if (wl == 0) gst4m<-1>(ws + WS_F2 + ((g0 & 1) * 72 + top(g0) + n0) * F2ROW + F_X0 * 128, lane * 4, vopq(tag));
    if (fast) warm();
  }
  if (i >= 5) {
    const unsigned z = ~get_tag(ws);
    gst16(cx.fp + wave * 1024, lane * 16, u32x4{z, z, z, z});
    if (wave == 0) gst16(cx.fp + 8192, lane * 16, u32x4{z, z, z, z});
  }
  if (i >= 5) { const unsigned dly = (i < 23) ? DLY_G : DLY_D; while ((unsigned)__builtin_amdgcn_s_memrealtime() - t0 < dly) __builtin_amdgcn_s_sleep(4); }
  if (i < 4) { if (wave < 4) role_qa<true>(smem, wqkv, cx, xh * 4 + i); else role_qa<false>(smem, wqkv, cx, xh * 4 + i); }
  else if (i == 4) { if (wave < 4) role_wo<true>(smem, wo, cx, xh); else role_wo<false>(smem, wo, cx, xh); }
  else if (i < 23) role_gu(smem, wg, wu, out, cx, c);
  else role_dn(smem, wd, cx, ks, ns);
  asm volatile(".subsection 2\n.Lkend:\n\t.fill 64, 4, 0xBF800000\n\t.subsection 0");
}
#if defined(__HIP_DEVICE_COMPILE__)
#pragma clang attribute pop
#endif

extern "C" void kernel_launch(void* const* d_in, const int* in_sizes, int n_in,
                              void* d_out, int out_size, void* d_ws, size_t ws_size,
                              hipStream_t stream) {
  k_main<<<256, 512, 0, stream>>>((const u16*)d_in[0], (const u16*)d_in[1], (const u16*)d_in[2], (const u16*)d_in[4],
                                  (const u16*)d_in[5], (const u16*)d_in[6], (char*)d_ws, (u16*)d_out);
}
```

```cpp
#include <hip/hip_runtime.h>
#include <stdint.h>
#include <type_traits>

#if defined(__HIP_DEVICE_COMPILE__)
#pragma clang attribute push(__attribute__((target("no-packed-fp32-ops"))), apply_to = function)
#endif
typedef __attribute__((ext_vector_type(8))) short bf16x8;
typedef __attribute__((ext_vector_type(4))) short bf16x4;
typedef __attribute__((ext_vector_type(4))) float f32x4;
typedef __attribute__((ext_vector_type(2))) float f32x2;
typedef __attribute__((ext_vector_type(4))) unsigned u32x4;
typedef __attribute__((ext_vector_type(2))) unsigned u32x2;
typedef __bf16 bf2v __attribute__((ext_vector_type(2)));
typedef unsigned short u16;

#define MFMA(b, a, c) __builtin_amdgcn_mfma_f32_16x16x32_bf16(b, a, c, 0, 0, 0)
constexpr int DM = 576, FD = 4608, NQKV = 3 * DM;
constexpr int TB = 18432;
constexpr int HB = 24576;
constexpr int PB = 6144;
constexpr int FSTR = 72 * 128, FGRP = 80 * FSTR, F2ROW = 19 * 128;
constexpr size_t WS_FL = 4096;
constexpr size_t WS_IS = WS_FL + 4 * FGRP;
constexpr size_t WS_F2 = WS_IS + 4 * 64 * 128;
constexpr size_t WS_O = WS_F2 + 2 * 72 * F2ROW;
constexpr size_t WS_X1 = WS_O + (size_t)4 * 68 * TB;
constexpr size_t WS_X2 = WS_X1 + (size_t)256 * TB;
constexpr size_t WS_X0 = WS_X2 + (size_t)128 * TB;
constexpr size_t WS_P = WS_X0 + (size_t)128 * TB;
constexpr size_t WS_H = WS_P + (size_t)72 * 64 * PB;
constexpr size_t WS_END = WS_H + (size_t)(24 * 64 + 4) * HB;
static_assert(WS_O % 1024 == 0 && WS_END <= ((size_t)256 << 20), "workspace");
constexpr int F_H = 0, F_P = 36, F_X1 = 54, F_O = 56, F_X0 = 18;
constexpr int TLAG = 2, PLAG = 3, OLAG = 4;
constexpr int SMEM_BYTES = 163840;
constexpr int PDG = 6, PDD = 5;
constexpr int BARG = 14, BARD = 10;
constexpr int PR_A = 2, PR_B = 1;
constexpr int DLY_G = 120;
constexpr int DLY_D = 500;
constexpr int LAGF = 8;

__device__ __forceinline__ float u2f(unsigned u) { return __builtin_bit_cast(float, u); }
__device__ __forceinline__ int f2i(float f) { return __builtin_bit_cast(int, f); }
__device__ __forceinline__ float rbf(float f) { __bf16 b = (__bf16)f; return u2f((unsigned)__builtin_bit_cast(u16, b) << 16); }
__device__ __forceinline__ unsigned cvt2(float a, float b) { return __builtin_bit_cast(unsigned, __builtin_convertvector(f32x2{a, b}, bf2v)); }
__device__ __forceinline__ float blo(unsigned w) { return u2f(w << 16); }
__device__ __forceinline__ float bhi(unsigned w) { return u2f(w & 0xffff0000u); }
typedef __attribute__((address_space(3))) bf16x4* lds_b4p;
__device__ __forceinline__ bf16x4 tr_read(const char* p) {
  return __builtin_amdgcn_ds_read_tr16_b64_v4i16((lds_b4p)(__attribute__((address_space(3))) void*)(p));
}
__device__ __forceinline__ bf16x8 tr_frag(const char* lo, const char* hi) {
  return __builtin_shufflevector(tr_read(lo), tr_read(hi), 0, 1, 2, 3, 4, 5, 6, 7);
}
__device__ __forceinline__ unsigned lds_addr(const void* p) { return (unsigned)(size_t)(__attribute__((address_space(3))) const char*)p; }
template <int I, int N, class F> __device__ __forceinline__ void static_for(F&& f) {
  if constexpr (I < N) { f(std::integral_constant<int, I>{}); static_for<I + 1, N>(f); }
}
template <int N> using IC = std::integral_constant<int, N>;
constexpr int imin(int a, int b) { return a < b ? a : b; }
template <int N> __device__ __forceinline__ void wait_vm() { asm volatile("s_waitcnt vmcnt(%0)" ::"n"(N) : "memory"); }
template <int N> __device__ __forceinline__ void wait_lg() { asm volatile("s_waitcnt lgkmcnt(%0)" ::"n"(N) : "memory"); }
template <int N> __device__ __forceinline__ void wait_frag() { __builtin_amdgcn_s_waitcnt(0xC07F | (N << 8)); }
__device__ __forceinline__ void bar_raw() { asm volatile("s_waitcnt lgkmcnt(0)\n\ts_barrier" ::: "memory"); }
__device__ __forceinline__ void bar_bare() { asm volatile("s_barrier" ::: "memory"); }
template <int P> __device__ __forceinline__ void prio() { asm volatile("s_setprio %0" :: "n"(P)); }
__device__ __forceinline__ void settle() { asm volatile("s_nop 7" ::: "memory"); }
__device__ __forceinline__ int sopq(int v) { asm volatile("" : "+s"(v)); return v; }
__device__ __forceinline__ unsigned vopq(unsigned v) { asm volatile("" : "+v"(v)); return v; }
template <class A> __device__ __forceinline__ void tie(A& a) { asm volatile("" : "+v"(a)); }
template <class A, class... R> __device__ __forceinline__ void tie(A& a, R&... r) { tie(a); tie(r...); }
__device__ __forceinline__ void adv(const char*& p, long d) { p += d; asm volatile("" : "+s"(p)); }

__device__ __forceinline__ void dma_w(const void* s, unsigned v, unsigned m) {
  asm volatile("s_mov_b32 m0, %2\n\ts_nop 0\n\tglobal_load_lds_dwordx4 %1, %0" :: "s"(s), "v"(v), "s"(m) : "memory");
}
__device__ __forceinline__ void dma_w54(const void* s, unsigned v, unsigned m) {
  asm volatile("s_mov_b32 m0, %2\n\ts_mov_b32 exec_hi, 0x3fffff\n\tglobal_load_lds_dwordx4 %1, %0\n\ts_mov_b32 exec_hi, -1" :: "s"(s), "v"(v), "s"(m) : "memory");
}
#define DMA_AT(O) "\n\tglobal_load_lds_dwordx4 %1, %0 offset:" #O " sc1"
#define DMA_N(BODY) asm volatile("s_add_u32 m0, %2, %3\n\ts_nop 0" BODY :: "s"(s), "v"(v), "s"(m), "n"(MO) : "memory", "scc")
template <int MO, int N> __device__ __forceinline__ void dma(const void* s, unsigned v, unsigned m) {
  if constexpr (N == 0) DMA_N("\n\ts_mov_b32 exec_hi, 0" DMA_AT(0) "\n\ts_mov_b32 exec_hi, -1");
  if constexpr (N == 1) DMA_N(DMA_AT(0));
  if constexpr (N == 2) DMA_N(DMA_AT(0) DMA_AT(1024));
  if constexpr (N == 3) DMA_N(DMA_AT(0) DMA_AT(1024) DMA_AT(2048));
  if constexpr (N == 4) DMA_N(DMA_AT(0) DMA_AT(1024) DMA_AT(2048) DMA_AT(3072));
}
__device__ __forceinline__ void touch(const void* p, unsigned lds_dummy) {
  asm volatile("s_mov_b32 m0, %1\n\ts_nop 0\n\tglobal_load_lds_dword %0, off" :: "v"(p), "s"(lds_dummy) : "memory");
}
__device__ __forceinline__ void dma_w2(const void* s, unsigned v, unsigned m) {
  asm volatile("s_mov_b32 m0, %2\n\ts_nop 0\n\tglobal_load_lds_dwordx4 %1, %0 nt" :: "s"(s), "v"(v), "s"(m) : "memory");
}
__device__ __forceinline__ void gld16(u32x4& d, const void* s, unsigned v) { asm volatile("global_load_dwordx4 %0, %1, %2 sc1" : "=v"(d) : "v"(v), "s"(s) : "memory"); }
template <int OFF> __device__ __forceinline__ void gld4(unsigned& d, const void* s, unsigned v) { asm volatile("global_load_dword %0, %1, %2 offset:%3 sc1" : "=v"(d) : "v"(v), "s"(s), "n"(OFF) : "memory"); }
__device__ __forceinline__ void gst16(const void* s, unsigned v, u32x4 d) {
  asm volatile("global_store_dwordx4 %0, %1, %2 sc1\n\ts_nop 1" :: "v"(v), "v"(d), "s"(s) : "memory");
}
__device__ __forceinline__ void gst16m(const void* s, unsigned v, u32x4 d, unsigned long long m) {
  asm volatile("s_mov_b64 exec, %3\n\tglobal_store_dwordx4 %0, %1, %2 sc1\n\ts_mov_b64 exec, -1\n\ts_nop 0" :: "v"(v), "v"(d), "s"(s), "s"(m) : "memory");
}
template <int LO, int OFF = 0> __device__ __forceinline__ void gst4m(const void* s, unsigned v, unsigned d) {
  asm volatile("s_mov_b32 exec_lo, %3\n\ts_mov_b32 exec_hi, 0\n\tglobal_store_dword %0, %1, %2 offset:%4 sc1\n\ts_mov_b64 exec, -1" :: "v"(v), "v"(d), "s"(s), "n"(LO), "n"(OFF) : "memory");
}
#define COLD(L, BACK, BODY) BACK ":\n\t.subsection 1\n" L ":\n\t" BODY "s_branch " BACK "\n\t.subsection 0"
template <int OFF> __device__ __forceinline__ void flag_by(unsigned long long who, const void* s, unsigned l16, unsigned tag) {
  asm volatile("s_mov_b64 exec, %3\n\tglobal_store_dword %0, %1, %2 offset:%4 sc1\n\ts_mov_b64 exec, -1" :: "v"(l16), "v"(tag), "s"(s), "s"(who), "n"(OFF) : "memory");
}
#define TIME_UP(DL, L) "s_memtime vcc\n\ts_waitcnt lgkmcnt(0)\n\ts_sub_u32 vcc_lo, vcc_lo, " DL "\n\ts_cmp_ge_i32 vcc_lo, 0\n\ts_cbranch_scc1 " L "\n\t"
template <int N, int OFF = 0> __device__ __forceinline__ void check_flags(unsigned& v, const void* s, unsigned voff, unsigned tag, unsigned deadline) {
  asm volatile("s_waitcnt vmcnt(%5)\n\tv_cmp_ne_u32 vcc, %4, %0\n\ts_cbranch_vccnz .Lsl%=\n"
               COLD(".Lsl%=", ".Lok%=", "s_cmp_eq_u32 %1, 0\n\ts_cbranch_scc1 .Lok%=\n"
                    ".Lsq%=:\n\ts_mov_b32 m0, 63\n"
                    ".Lsp%=:\n\tglobal_load_dword %0, %2, %3 offset:%6 sc1\n\ts_waitcnt vmcnt(0)\n\tv_cmp_ne_u32 vcc, %4, %0\n\ts_cbranch_vccz .Lok%=\n\t"
                    "s_sub_u32 m0, m0, 1\n\ts_cbranch_scc0 .Lsp%=\n\t" TIME_UP("%1", ".Lok%=") "s_branch .Lsq%=\n\t")
               : "+v"(v) : "s"(deadline), "v"(voff), "s"(s), "s"(tag), "n"(N), "n"(OFF) : "memory", "vcc", "scc");
}
__device__ __forceinline__ void poll(const void* s, unsigned voff, unsigned tag, unsigned deadline) {
  unsigned v; gld4<0>(v, s, voff); check_flags<0>(v, s, voff, tag, deadline);
}
__device__ __forceinline__ unsigned make_tag(unsigned nonce) { return nonce * 2654435761u + 0x7F4A7C15u; }
struct Red { u32x4 pp[6], xp; unsigned i2, fw; const char *rp, *rx, *ri, *rf, *f2, *ob; };
#define RED_M(D, S, P, C) "v_mfma_f32_16x16x32_bf16 " D ", " S ", " P ", " C "\n\t"
__device__ __forceinline__ void reduce_ask(Red& u, unsigned l16, unsigned iv, unsigned pv, unsigned vt, unsigned tag, unsigned deadline, int step) {
  asm volatile("s_cmp_lt_u32 %20, 4\n\ts_cbranch_scc1 .Ls%=\n"
               COLD(".Ls%=", ".Lz%=", "s_cmp_eq_u32 %20, 1\n\ts_cbranch_scc1 .Lc%=\n\ts_cmp_eq_u32 %20, 2\n\ts_cbranch_scc1 .Lz%=\n\ts_cmp_eq_u32 %20, 3\n\ts_cbranch_scc1 .Lp%=\n\t"
               "global_load_dword %8, %16, %12 sc1\n\ts_branch .Lz%=\n"
               ".Lp%=:\n\ts_mov_b64 exec, 1\n\tglobal_store_dword %14, %17, %13 sc1\n\ts_mov_b64 exec, -1\n\ts_branch .Lz%=\n"
               ".Lc%=:\n\t"
               "s_mov_b32 m0, 63\n.Ld%=:\n\tv_cmp_ne_u32 vcc, %18, %8\n\ts_cbranch_vccz .Lr%=\n\t"
               "global_load_dword %8, %16, %12 sc1\n\ts_sub_u32 m0, m0, 1\n\ts_waitcnt vmcnt(0)\n\ts_cbranch_scc0 .Ld%=\n\t" TIME_UP("%19", ".Lr%=") "s_branch .Lc%=\n"
               ".Lr%=:\n\tglobal_load_dwordx4 %0, %14, %9 offset:-3072 sc1\n\tglobal_load_dwordx4 %1, %14, %9 offset:-2048 sc1\n\t"
               "global_load_dwordx4 %2, %14, %9 offset:-1024 sc1\n\tglobal_load_dwordx4 %3, %14, %9 sc1\n\tglobal_load_dwordx4 %4, %14, %9 offset:1024 sc1\n\t"
               "global_load_dwordx4 %5, %14, %9 offset:2048 sc1\n\tglobal_load_dwordx4 %6, %14, %10 sc1\n\tglobal_load_dword %7, %15, %11 sc1\n\t")
               : "+v"(u.pp[0]), "+v"(u.pp[1]), "+v"(u.pp[2]), "+v"(u.pp[3]), "+v"(u.pp[4]), "+v"(u.pp[5]), "+v"(u.xp), "+v"(u.i2), "+v"(u.fw)
               : "s"(u.rp), "s"(u.rx), "s"(u.ri), "s"(u.rf), "s"(u.f2), "v"(l16), "v"(iv), "v"(pv), "v"(vt), "s"(tag), "s"(deadline), "s"(step) : "memory", "vcc", "scc");
}
__device__ __forceinline__ void reduce_sum(const Red& u, const u32x4& s0, const u32x4& s1, f32x4& v0, f32x4& v1, float (&x)[8]) {
  asm volatile(RED_M("%0", "%10", "%12", "0") RED_M("%1", "%11", "%12", "0") "v_lshlrev_b32 %2, 16, %18\n\tv_and_b32 %3, 0xffff0000, %18\n\t"
               RED_M("%0", "%10", "%13", "%0") RED_M("%1", "%11", "%13", "%1") "v_lshlrev_b32 %4, 16, %19\n\tv_and_b32 %5, 0xffff0000, %19\n\t"
               RED_M("%0", "%10", "%14", "%0") RED_M("%1", "%11", "%14", "%1") "v_lshlrev_b32 %6, 16, %20\n\tv_and_b32 %7, 0xffff0000, %20\n\t"
               RED_M("%0", "%10", "%15", "%0") RED_M("%1", "%11", "%15", "%1") "v_lshlrev_b32 %8, 16, %21\n\tv_and_b32 %9, 0xffff0000, %21\n\t"
               RED_M("%0", "%10", "%16", "%0") RED_M("%1", "%11", "%16", "%1") RED_M("%0", "%10", "%17", "%0") RED_M("%1", "%11", "%17", "%1")
               : "=&v"(v0), "=&v"(v1), "=&v"(x[0]), "=&v"(x[1]), "=&v"(x[2]), "=&v"(x[3]), "=&v"(x[4]), "=&v"(x[5]), "=&v"(x[6]), "=&v"(x[7])
               : "v"(s0), "v"(s1), "v"(u.pp[0]), "v"(u.pp[1]), "v"(u.pp[2]), "v"(u.pp[3]), "v"(u.pp[4]), "v"(u.pp[5]), "v"(u.xp[0]), "v"(u.xp[1]), "v"(u.xp[2]), "v"(u.xp[3]));
}
__device__ __forceinline__ void wait_store(int step) {
  asm volatile("s_cmp_lt_u32 %0, 4\n\ts_cbranch_scc1 .La%=\n\ts_waitcnt vmcnt(0)\n"
               COLD(".La%=", ".Lb%=", "s_waitcnt vmcnt(8)\n\ts_cmp_eq_u32 %0, 1\n\ts_cbranch_scc1 .Lb%=\n\ts_waitcnt vmcnt(1)\n\t") :: "s"(step) : "memory", "scc");
}

template <bool Z> __device__ __forceinline__ void mf(f32x4& c, const bf16x8& a, const bf16x8& b) {
  if (Z) asm volatile("v_mfma_f32_16x16x32_bf16 %0, %1, %2, 0" : "=&v"(c) : "v"(a), "v"(b));
  else asm volatile("v_mfma_f32_16x16x32_bf16 %0, %1, %2, %0" : "+v"(c) : "v"(a), "v"(b));
}
__device__ __forceinline__ void mfc(f32x4& d, const bf16x8& a, const bf16x8& b, const f32x4& c) {
  asm volatile("v_mfma_f32_16x16x32_bf16 %0, %1, %2, %3" : "=&v"(d) : "v"(a), "v"(b), "v"(c));
}
template <int OFF, class T> __device__ __forceinline__ void lds_rd(T& d, unsigned a) { asm volatile("ds_read_b128 %0, %1 offset:%2" : "=v"(d) : "v"(a), "n"(OFF)); }
template <int OFF, class T> __device__ __forceinline__ void lds_rd8(T& d, unsigned a) { asm volatile("ds_read_b64 %0, %1 offset:%2" : "=v"(d) : "v"(a), "n"(OFF)); }
template <int OFF, class T> __device__ __forceinline__ void lds_wr(unsigned a, const T& d) { asm volatile("ds_write_b128 %0, %1 offset:%2" :: "v"(a), "v"(d), "n"(OFF) : "memory"); }
template <int OFF, class T> __device__ __forceinline__ void lds_wr8(unsigned a, const T& d) { asm volatile("ds_write_b64 %0, %1 offset:%2" :: "v"(a), "v"(d), "n"(OFF) : "memory"); }
template <int NK, int PD, int G> struct Ring { bf16x8 q[NK]; };
template <int O, int NK, int PD, int G> __device__ __forceinline__ void prime(Ring<NK, PD, G>& b, unsigned la) {
  static_for<0, PD>([&](auto kc) { lds_rd<O + decltype(kc)::value * 1024>(b.q[decltype(kc)::value], la); });
}
template <int O, int BAR, int VMB, int NK, int PD, int G, class F, class H> __device__ __forceinline__ void stream(Ring<NK, PD, G>& b, unsigned la, F&& f, H&& hook) {
  constexpr int NT = 36 / NK;
  static_assert((BAR < 0 || BAR + PD >= NK) && NK % G == 0, "barrier before the last read");
  if constexpr (BAR >= 0) prio<PR_B>();
  static_for<0, 36>([&](auto sc) {
    constexpr int s = decltype(sc)::value, c = s / NT, grp = s / (G * NT), in = s % (G * NT);
    constexpr int k = grp * G + in % G, t = (grp & 1) ? NT - 1 - in / G : in / G;
    constexpr int KZ = (BAR >= 0) ? BAR : NK - PD + 2;
    if constexpr (s == BAR * NT - 1) asm volatile("s_setprio 3\n\ts_waitcnt vmcnt(%0) lgkmcnt(0)" :: "n"(VMB) : "memory");
    if constexpr (s == BAR * NT) { asm volatile("" ::: "memory"); __builtin_amdgcn_s_barrier(); asm volatile("" ::: "memory"); }
    if constexpr (BAR < 0 && s == KZ * NT) wait_frag<0>();
    if constexpr (c < KZ && in < G) wait_frag<imin(NK - 1, PD - 1 + (s + NT - 1) / NT) - k>();
    f(IC<k>{}, IC<t>{}, b.q[k]);
    if constexpr (s % NT == 0 && c + PD < NK) lds_rd<O + (c + PD) * 1024>(b.q[c + PD], la);
    if constexpr (in == G * NT - 1) static_for<grp * G, grp * G + G>([&](auto kc) { f(kc, IC<NT>{}, b.q[decltype(kc)::value]); });
    if constexpr (s % NT == NT - 1) hook(IC<c>{});
  });
}
template <class F> __device__ __forceinline__ void periods(F&& f) {
#pragma nounroll
  for (int it = 0; it < 16; ++it) static_for<0, 4>([&](auto jc) { f(jc, it); });
}

__device__ __forceinline__ void max4(float (&s)[4], const f32x4& a, const f32x4& b) {
  asm volatile("v_max_f32 %0, %4, %8\n\tv_max_f32 %1, %5, %9\n\tv_max_f32 %2, %6, %10\n\tv_max_f32 %3, %7, %11" : "=&v"(s[0]), "=&v"(s[1]), "=&v"(s[2]), "=&v"(s[3])
               : "v"(a[0]), "v"(a[1]), "v"(a[2]), "v"(a[3]), "v"(b[0]), "v"(b[1]), "v"(b[2]), "v"(b[3]));
}
struct Soft { float s[4], t0, t1; unsigned mo[2]; };
__device__ __forceinline__ void soft1(Soft& f, float sc) {
  asm volatile("v_max3_f32 %4, %0, %1, %2\n\tv_max_f32 %5, %4, %3\n\tv_max_f32 %4, %4, %3\n\ts_nop 1\n\tv_permlane16_swap_b32 %4, %5\n\t"
               "v_max_f32 %4, %4, %5\n\tv_mov_b32 %5, %4\n\ts_nop 1\n\tv_permlane32_swap_b32 %4, %5\n\tv_max_f32 %4, %4, %5\n\tv_mul_f32 %4, %4, %6\n\t"
               "v_fma_f32 %0, %0, %6, -%4\n\tv_fma_f32 %1, %1, %6, -%4\n\tv_fma_f32 %2, %2, %6, -%4\n\tv_fma_f32 %3, %3, %6, -%4"
               : "+v"(f.s[0]), "+v"(f.s[1]), "+v"(f.s[2]), "+v"(f.s[3]), "=&v"(f.t0), "=&v"(f.t1) : "v"(sc));
}
#define SOFT2(R0, R1, R2, R3, R4, R5, R6, R7, PAD) \
  "v_exp_f32 %0, %0\n\t" R0 "v_exp_f32 %1, %1\n\t" R1 "v_exp_f32 %2, %2\n\t" R2 "v_exp_f32 %3, %3\n\t" R3 \
  "v_add_f32 %4, %0, %1\n\tv_add_f32 %5, %2, %3\n\t" R4 "v_add_f32 %4, %4, %5\n\tv_mov_b32 %5, %4\n\t" R5 PAD \
  "v_permlane16_swap_b32 %4, %5\n\t" R6 "v_add_f32 %4, %4, %5\n\tv_mov_b32 %5, %4\n\t" R7 PAD \
  "v_permlane32_swap_b32 %4, %5\n\tv_add_f32 %4, %4, %5\n\tv_rcp_f32 %4, %4"
#define SOFT_RD(N, K) "ds_read_b128 %" #N ", %14 offset:%15+" #K "\n\t"
template <int OFF> __device__ __forceinline__ void soft2(Soft& f, bf16x8 (&x)[18], unsigned la) {
  if constexpr (OFF < 0)
    asm volatile(SOFT2("", "", "", "", "", "", "", "", "s_nop 1\n\t") : "+v"(f.s[0]), "+v"(f.s[1]), "+v"(f.s[2]), "+v"(f.s[3]), "=&v"(f.t0), "=&v"(f.t1));
  else
    asm volatile(SOFT2(SOFT_RD(6, 0), SOFT_RD(7, 1024), SOFT_RD(8, 2048), SOFT_RD(9, 3072), SOFT_RD(10, 4096), SOFT_RD(11, 5120), SOFT_RD(12, 6144), SOFT_RD(13, 7168), "s_nop 0\n\t")
                 : "+v"(f.s[0]), "+v"(f.s[1]), "+v"(f.s[2]), "+v"(f.s[3]), "=&v"(f.t0), "=&v"(f.t1),
                   "=&v"(x[0]), "=&v"(x[1]), "=&v"(x[2]), "=&v"(x[3]), "=&v"(x[4]), "=&v"(x[5]), "=&v"(x[6]), "=&v"(x[7]) : "v"(la), "n"(OFF < 0 ? 0 : OFF));
}
#define QK_M(D, W, X, C) "v_mfma_f32_16x16x32_bf16 " D ", " W ", " X ", " C "\n\t"
#define QK_G3 "v_mul_f32 %4, %4, %8\n\t"
#define QK_G4 "v_mul_f32 %5, %5, %8\n\t"
#define QK_G5 "v_mul_f32 %6, %6, %8\n\t"
#define QK_G6 "v_mul_f32 %7, %7, %8\n\t"
#define QK_G7 "v_cvt_pk_bf16_f32 %8, %4, %5\n\t"
#define QK_G8 "v_cvt_pk_bf16_f32 %9, %6, %7\n\t"
#define QK_G9 "v_and_b32 %4, %8, %17\n\t"
#define QK_GA "v_and_b32 %5, %9, %18\n\t"
#define QK_GB "v_xor_b32 %6, %8, %4\n\t"
#define QK_GC "v_xor_b32 %7, %9, %5\n\t"
#define QK_OUT(A, B, C, D) "+v"(f.s[A]), "+v"(f.s[B]), "+v"(f.s[C]), "+v"(f.s[D]), "+v"(f.t0), "+v"(f.t1)
#define QK_PAIR(C0, C1, GA, GB, S) asm volatile(QK_M("%0", "%10", "%14", C0) "ds_read_b128 %2, %16 offset:%19\n\t" QK_M("%1", "%11", "%14", C1) GA \
    QK_M("%1", "%13", "%15", "%1") "ds_read_b128 %3, %16 offset:%20\n\t" QK_M("%0", "%12", "%15", "%0") GB \
    : "+v"(a0), "+v"(a1), "=&v"(y0), "=&v"(y1), QK_OUT(S, S + 1, 2 - S, 3 - S) \
    : "v"(w0[0]), "v"(w0[1]), "v"(w1[0]), "v"(w1[1]), "v"(x0), "v"(x1), "v"(la), "v"(f.mo[0]), "v"(f.mo[1]), "n"(OFF), "n"(OFF + 1024))
template <int P, int E, int OFF> __device__ __forceinline__ void qk_pair(f32x4& a0, f32x4& a1, const bf16x8 (&w0)[2], const bf16x8 (&w1)[2], bf16x8 x0, bf16x8 x1,
                                                                         bf16x8& y0, bf16x8& y1, unsigned la, Soft& f) {
  if constexpr (P == 0) QK_PAIR("0", "0", QK_G3, QK_G4, 0);
  if constexpr (P == 1) QK_PAIR("%0", "%1", QK_G5, QK_G6, 0);
  if constexpr (P == 2) QK_PAIR("%0", "%1", QK_G7, QK_G8, 0);
  if constexpr (P == 3) QK_PAIR("%0", "%1", QK_G9, QK_GA, 2 * E);
  if constexpr (P == 4) QK_PAIR("%0", "%1", QK_GB, QK_GC, 2 * E);
}
__device__ __forceinline__ void soft3(Soft& f) {
  int u = 0;
  asm volatile("s_nop 0\n\t" QK_G3 QK_G4 QK_G5 QK_G6 QK_G7 QK_G8 QK_G9 QK_GA QK_GB QK_GC : "+s"(u), "+s"(u), "+s"(u), "+s"(u), QK_OUT(0, 1, 2, 3)
               : "n"(0), "n"(0), "n"(0), "n"(0), "n"(0), "n"(0), "n"(0), "v"(f.mo[0]), "v"(f.mo[1]));
}
template <int E> __device__ __forceinline__ void mf8(f32x4& a0, f32x4& a1, const bf16x8 (&w0)[2], const bf16x8 (&w1)[2], const bf16x8 (&w2)[2], const bf16x8 (&w3)[2],
                                                     const bf16x8& x0, const bf16x8& x1, const bf16x8& x2, const bf16x8& x3, f32x2& st, f32x2& xk, f32x2& xq, unsigned sa, unsigned xa) {
  asm volatile(QK_M("%0", "%5", "%13", "%0") "ds_read_b64 %2, %17 offset:%19\n\tds_read_b64 %3, %18 offset:%20\n\tds_read_b64 %4, %18 offset:%21\n\t"
               QK_M("%1", "%6", "%13", "%1") QK_M("%1", "%8", "%14", "%1") QK_M("%0", "%7", "%14", "%0")
               QK_M("%1", "%10", "%15", "%1") QK_M("%0", "%9", "%15", "%0") QK_M("%1", "%12", "%16", "%1") QK_M("%0", "%11", "%16", "%0")
               : "+v"(a0), "+v"(a1), "=&v"(st), "=&v"(xk), "=&v"(xq)
               : "v"(w0[0]), "v"(w0[1]), "v"(w1[0]), "v"(w1[1]), "v"(w2[0]), "v"(w2[1]), "v"(w3[0]), "v"(w3[1]), "v"(x0), "v"(x1), "v"(x2), "v"(x3),
                 "v"(sa), "v"(xa), "n"(E * 128), "n"(E * 1024 + 512), "n"(E * 1024));
}

constexpr unsigned SPIN = 400000000u;
struct Ctx { char* ws; int g, wave, lane; unsigned dl; const char *fl, *fp, *code; unsigned long long w0; };
constexpr unsigned long long HALF = 0x0000FFFF0000FFFFull;

__device__ __forceinline__ unsigned get_tag(const char* ws) {
  unsigned nonce;
  asm volatile("s_load_dword %0, %1, 0x0\n\ts_waitcnt lgkmcnt(0)" : "=s"(nonce) : "s"(ws) : "memory");
  return make_tag(nonce);
}
template <int R, int NP, int NX = 0, class I, class Q, class P> __device__ __forceinline__ void load_w(char* smem, const Ctx& cx, unsigned& tag, I&& issue, Q&& rd, P&& then) {
  const int wave = cx.wave;
  static_for<0, 3>([&](auto rc) { issue(rc); });
  tag = get_tag(cx.ws);
  static_for<0, R>([&](auto rc) {
    constexpr int r = decltype(rc)::value;
    wait_vm<imin(2, R - 1 - r) * NP + (r >= R - 2 ? NX : 0)>();
    bar_bare();
    rd(rc);
    if constexpr (r + 3 < R || r + 1 == R) bar_raw();
    if constexpr (r + 3 < R) issue(IC<r + 3>{});
    if constexpr (r + 3 == R && NX == 0) { if (wave == ((blockIdx.x >> 4) & 7)) touch(cx.code, lds_addr(smem) + 162816); }
    then(rc);
  });
}
__device__ __forceinline__ bf16x8 asb(u32x4 v) { return __builtin_bit_cast(bf16x8, v); }
__device__ __forceinline__ bf16x8 ones8() { return asb(u32x4{0x3F803F80u, 0x3F803F80u, 0x3F803F80u, 0x3F803F80u}); }
__device__ __forceinline__ u32x4 widen(unsigned w0, unsigned w1) {
  unsigned n0, n1;
  asm volatile("s_nop 1\n\tv_permlane16_swap_b32 %0, %2\n\tv_permlane16_swap_b32 %1, %3" : "+v"(w0), "+v"(w1), "=&v"(n0), "=&v"(n1));
  return u32x4{w0, w1, n0, n1};
}
__device__ __forceinline__ u32x4 pack8(const f32x4& a, const f32x4& b) { return u32x4{cvt2(a[0], a[1]), cvt2(a[2], a[3]), cvt2(b[0], b[1]), cvt2(b[2], b[3])}; }
__device__ __forceinline__ f32x2 row_stat(float sum, float sq) {
  const float mean = sum * (1.0f / 576.0f);
  const float var = fmaxf(sq * (1.0f / 576.0f) - mean * mean, 0.0f);
  return f32x2{-rbf(mean), __builtin_amdgcn_rcpf(rbf(__builtin_amdgcn_sqrtf(rbf(rbf(var) + 1e-5f))))};
}
__device__ __forceinline__ float diag(const f32x4& a, int p) { return (p == 0) ? a[0] : (p == 1) ? a[1] : (p == 2) ? a[2] : a[3]; }
__device__ __forceinline__ int top(int g) { return (1 - (g >> 1)) * 32; }

__device__ __forceinline__ void role_gu(char* smem, const u16* wg, const u16* wu, u16* out, const Ctx& cx, int c) {
  const int wave = cx.wave, lane = cx.lane;
  const unsigned sm0 = lds_addr(smem);
  unsigned tag;
  const int r = lane & 15, g = lane >> 4, q = r >> 2, p = lane & 3;
  constexpr int SUMS = 4 * TB;
  bf16x8 W[18][2];
  u32x4 sel0, sel1;
#pragma unroll
  for (int e = 0; e < 2; ++e) {
    sel0[e] = sel1[2 + e] = (r == 4 * g + 2 * e ? 0x3F80u : 0u) | (r == 4 * g + 2 * e + 1 ? 0x3F800000u : 0u);
    sel0[2 + e] = sel1[e] = 0u;
  }
  tie(sel0, sel1);
  {
    f32x4 cs[2] = {{0.f, 0.f, 0.f, 0.f}, {0.f, 0.f, 0.f, 0.f}};
    constexpr int BUF = 48 * 1056;
    const char* src = (const char*)((wave >> 2) ? wu : wg) + c * 256;
    const unsigned vo = (4 * (g & 1) + 16 * (g >> 1)) * (FD * 2) + r * 16;
    const char* rb = smem + ((g & 1) * 4 + q) * 1056 + (g >> 1) * 512 + wave * 32 + p * 8;
    load_w<6, 6>(smem, cx, tag, [&](auto rc) {
      constexpr int rr = decltype(rc)::value;
#pragma unroll
      for (int j = 0; j < 6; ++j) {
        const int idx = 6 * (wave & 3) + j, id = idx & 7;
        dma_w2(src + (size_t)(32 * (3 * rr + (idx >> 3)) + 8 * (id >> 2) + (id & 3)) * (FD * 2), vo, sm0 + (rr % 3) * BUF + ((wave >> 2) * 24 + idx) * 1056);
      }
    }, [&](auto rc) {
      constexpr int rr = decltype(rc)::value;
#pragma unroll
      for (int kl = 0; kl < 3; ++kl)
#pragma unroll
        for (int m = 0; m < 2; ++m) { const char* a = rb + (rr % 3) * BUF + (m * 24 + kl * 8) * 1056; W[3 * rr + kl][m] = tr_frag(a, a + 256); }
      tie(W[3 * rr][0], W[3 * rr][1], W[3 * rr + 1][0], W[3 * rr + 1][1], W[3 * rr + 2][0], W[3 * rr + 2][1]);
    }, [&](auto rc) {
#pragma unroll
      for (int k = 0; k < 6; ++k) cs[k & 1] = MFMA(ones8(), W[3 * decltype(rc)::value + (k >> 1)][k & 1], cs[k & 1]);
      tie(cs[0], cs[1]);
    });
#pragma unroll
    for (int m = 0; m < 2; ++m) {
      const float cm = cs[m][0] * (-1.0f / 576.0f);
      const f32x4 c4 = {cm, cm, cm, cm};
#pragma unroll
      for (int k = 0; k < 18; ++k) { W[k][m] = asb(pack8(MFMA(asb(sel0), W[k][m], c4), MFMA(asb(sel1), W[k][m], c4))); tie(W[k][m]); }
    }
  }
  const char* hb = cx.ws + WS_H + (size_t)(cx.g * 6 + c / 6) * 64 * HB + (c % 6) * 4096 + wave * 512 - HB;
  const unsigned l16 = vopq(lane * 16), hv = vopq((g >> 1) * 256 + r * 16);
  const unsigned la[2] = {vopq(sm0 + l16), vopq(sm0 + l16 + 2 * TB)};
  f32x4 ag, au;
  Ring<18, PDG, 2> bq;
  auto mm = [&](auto kc, auto jc, const bf16x8& x) {
    constexpr int k = decltype(kc)::value;
    if constexpr (decltype(jc)::value == 0) mf<k == 0>(ag, W[k][0], x);
    if constexpr (decltype(jc)::value == 1) mf<k == 0>(au, W[k][1], x);
  };
  auto epi = [&]() {
    tie(ag, au);
    return widen(cvt2(ag[0] * au[0], ag[1] * au[1]), cvt2(ag[2] * au[2], ag[3] * au[3]));
  };
  if (wave < 4) {
    const char* xs = cx.ws + WS_X1 + (size_t)cx.g * 64 * TB + wave * 4608;
    const char* fr = cx.fl + TLAG * 128;
    const unsigned pv = vopq((F_X1 + (lane & 1)) * FSTR), l4 = vopq(l16 + 4096), mb = sm0 + wave * 4608;
    auto fetch = [&](auto sc) { dma<decltype(sc)::value * TB, 4>(xs, l16, mb); dma<decltype(sc)::value * TB + 4096, 0>(xs, l4, mb); };
    unsigned dl = cx.dl;
    int tgt = (wave == 1 && c < 32) ? c + 2 : 99;
    auto scale = [&]() {
      f32x2 t = *(const f32x2*)(smem + SUMS + lane * 8);
#pragma unroll
      for (int e = 0; e < 2; ++e) {
        const auto a = __builtin_amdgcn_permlane16_swap(f2i(t[e]), f2i(t[e]), false, false);
        const float h = u2f(a[0]) + u2f(a[1]);
        const auto b = __builtin_amdgcn_permlane32_swap(f2i(h), f2i(h), false, false);
        t[e] = u2f(b[0]) + u2f(b[1]);
      }
      const float is = row_stat(t[0], t[1])[1];
      gst4m<-1>(cx.ws + WS_IS + (cx.g * 64 + tgt - 2) * 128, lane * 4, f2i(is * is));
      tgt += 32;
    };
    poll(fr, pv, tag, dl); fetch(IC<0>{});
    poll(fr + 128, pv, tag, dl);
    adv(fr, 256);
    adv(xs, TB); fetch(IC<1>{});
    prio<PR_A>();
    bar_bare();
    periods([&](auto jc, int it) {
      constexpr int j = decltype(jc)::value;
      unsigned fw;
      prime<(j & 1) * TB>(bq, la[j >> 1]);
      gld4<j * 128>(fw, fr, pv);
      stream<(j & 1) * TB, -1, 0>(bq, la[j >> 1], mm, [&](auto kc) {
        constexpr int k = decltype(kc)::value;
        if constexpr (k == 11) adv(xs, TB);
        if constexpr (k == 12 && j == 2) dl = sopq((it == 15) ? 0u : dl);
        if constexpr (k == 13) adv(hb, HB);
      });
      prio<3>();
      check_flags<0, j * 128>(fw, fr, pv, tag, dl);
      fetch(IC<(j + 2) & 3>{});
      gst16m(hb, hv, epi(), HALF);
      if constexpr (j == 3) adv(fr, 512);
      if (__builtin_expect(4 * it + j == tgt, 0)) scale();
      prio<PR_A>();
      bar_bare();
    });
    if (c == 31) bar_bare();
    if (tgt < 66) scale();
    wait_vm<0>();
    bar_bare();
  } else {
    const int kb = 2 * (c % 9) + (wave & 1);
    int tau = 2 * (c / 9) + ((wave >> 1) & 1);
    Red u = {};
    const unsigned vt = vopq(tag), iv = vopq(r * 4), pq = vopq((F_P + (kb / 6) * 6 + min(lane, 5)) * FSTR), vrow = vopq(r * 2304 + g * 16);
    u.rp = cx.ws + WS_P + (size_t)((cx.g * 18 + kb) * 64 + tau) * PB + 3072;
    u.rx = cx.ws + WS_X1 + (size_t)(cx.g * 64 + tau) * TB + kb * 1024;
    u.ri = cx.ws + WS_IS + (cx.g * 64 + tau) * 128;
    u.rf = cx.fl + (tau + PLAG) * 128;
    u.f2 = cx.ws + WS_F2 + ((cx.g & 1) * 72 + top(cx.g) + tau - 8) * F2ROW + kb * 128;
    u.ob = cx.ws + WS_X2 + (size_t)((cx.g & 1) * 64 + top(cx.g) + tau) * TB + kb * 1024;
    const char* ob2 = (const char*)out + (size_t)((cx.g >> 1) * 1024 + 32 * (31 - tau) + (cx.g & 1)) * (DM * 2) + kb * 64;
    gst16m(u.f2 + 8 * F2ROW, (lane >> 3) * (8 * F2ROW) + (lane & 7) * 16, u32x4{~tag, ~tag, ~tag, ~tag}, 0xFFFFFFFFull);
    auto ask = [&](int step) { reduce_ask(u, l16, iv, pq, vt, tag, cx.dl, step); };
    auto finish = [&](int tau_) {
      f32x4 v0, v1; float x[8];
      reduce_sum(u, sel0, sel1, v0, v1, x);
      adv(u.rp, 0); adv(u.rx, 0); adv(u.ri, 0); adv(u.rf, 0); adv(u.f2, 0); adv(u.ob, 0);
      const int tau = sopq(tau_);
      const char* o = u.ob;
      const unsigned ov = vopq((tau >= 32) ? vrow : l16);
      adv(u.rp, 8 * PB); adv(u.rx, 8 * TB); adv(u.ri, 8 * 128); adv(u.rf, 8 * 128);
      adv(u.f2, (tau < 32) ? 8 * F2ROW : 0);
      u.ob = ((tau >> 3) == 3) ? ob2 : u.ob + ((tau < 32) ? 8l * TB : -8l * 32 * DM * 2); adv(u.ob, 0);
      tie(v0, v1);
      const float sc = u2f(u.i2);
      gst16(o, ov, u32x4{cvt2(fmaf(v0[0], sc, x[0]), fmaf(v0[1], sc, x[1])), cvt2(fmaf(v0[2], sc, x[2]), fmaf(v0[3], sc, x[3])),
                         cvt2(fmaf(v1[0], sc, x[4]), fmaf(v1[1], sc, x[5])), cvt2(fmaf(v1[2], sc, x[6]), fmaf(v1[3], sc, x[7]))});
    };
    int step = -1, tgt = (c < 32) ? c : 99;
    const char* fp = cx.fp - 512;
    bar_bare();
    prime<0>(bq, la[0]);
    wait_lg<0>();
    periods([&](auto jc, int it) {
      constexpr int j = decltype(jc)::value, j1 = (j + 1) & 3;
      stream<(j & 1) * TB, BARG, 63>(bq, la[j >> 1], mm, [&](auto kc) {
        constexpr int k = decltype(kc)::value;
        if constexpr (k == 9 && j == 0) adv(fp, 512);
        if constexpr (k == 10) adv(hb, HB);
        if constexpr (k == BARG - 2) wait_store(step);
      });
      wait_vm<0>();
      flag_by<(j + 1) * 128>(cx.w0, fp, l16, vt);
      asm volatile("s_nop 3");
      u32x4 h = epi();
      gst16m(hb, hv, h, HALF);
      step = 4 * it + j - LAGF - (tau | 1);
      ask(step);
      if (__builtin_expect(step == 2, 0)) finish(tau);
      tau += (step == 3) ? 8 : 0;
      if (__builtin_expect(4 * it + j == tgt, 0)) {
        f32x4 s1 = {0.f, 0.f, 0.f, 0.f}, s2 = s1;
        bf16x8 x[5];
#pragma unroll
        for (int k = 0; k < 5; ++k) x[k] = *(const bf16x8*)(smem + j * TB + (k < 4 ? 4 * (wave - 4) + k : 16 + (wave & 1)) * 1024 + lane * 16);
        if (wave >= 6) x[4] = bf16x8{0, 0, 0, 0, 0, 0, 0, 0};
#pragma unroll
        for (int k = 0; k < 5; ++k) { s1 = MFMA(ones8(), x[k], s1); s2 = MFMA(x[k], x[k], s2); }
        if (g == q) *(f32x2*)(smem + SUMS + (wave - 4) * 128 + r * 8) = f32x2{s1[0], diag(s2, p)};
        tgt += 32;
      }
      prio<0>();
      prime<(j1 & 1) * TB>(bq, la[j1 >> 1]);
      if constexpr (j == 3) wait_lg<0>();
    });
    if (c == 31) bar_bare();
    wait_vm<0>();
    bar_bare();
    flag_by<640>(cx.w0, fp, l16, vt);
    if (step == 2) tau += 8;
#pragma nounroll
    while (tau < 64) {
      ask(0); wait_vm<0>(); ask(1); wait_vm<0>(); ask(2);
      finish(tau); tau += 8;
    }
  }
}

__device__ __forceinline__ void role_dn(char* smem, const u16* wd, const Ctx& cx, int ks, int ns) {
  const int wave = cx.wave, lane = cx.lane;
  const unsigned sm0 = lds_addr(smem);
  unsigned tag;
  const int r = lane & 15, g = lane >> 4, q = r >> 2, p = lane & 3;
  const int kq = wave >> 2, nq = wave & 3;
  constexpr int EX = 4 * HB;
  bf16x8 W[12][3];
  {
    constexpr int BUF = 49152;
    const char* src = (const char*)wd + (size_t)(768 * ks) * (DM * 2) + ns * 384;
    unsigned vo[6];
#pragma unroll
    for (int j = 0; j < 6; ++j) {
      const unsigned ch = (wave * 6 + j) * 64 + lane, row = ch / 24;
      vo[j] = ((row >> 6) * 384 + (row & 63)) * (DM * 2) + (ch - row * 24) * 16;
    }
    const char* rb = smem + (kq * 64 + 8 * g + q) * 384;
    load_w<6, 6>(smem, cx, tag, [&](auto rc) {
      constexpr int rr = decltype(rc)::value;
#pragma unroll
      for (int j = 0; j < 6; ++j) dma_w2(src + rr * 64 * (DM * 2), vo[j], sm0 + (rr % 3) * BUF + (wave * 6 + j) * 1024);
    }, [&](auto rc) {
      constexpr int rr = decltype(rc)::value;
#pragma unroll
      for (int kl = 0; kl < 2; ++kl)
#pragma unroll
        for (int m = 0; m < 3; ++m) {
          const char* a = rb + (rr % 3) * BUF + kl * 32 * 384 + ((m < 2) ? nq * 64 + p * 16 + m * 8 : 256 + nq * 32 + p * 8);
          W[2 * rr + kl][m] = tr_frag(a, a + 4 * 384);
        }
      tie(W[2 * rr][0], W[2 * rr][1], W[2 * rr][2], W[2 * rr + 1][0], W[2 * rr + 1][1], W[2 * rr + 1][2]);
    }, [](auto) {});
  }
  const unsigned l16 = vopq(lane * 16);
  const unsigned la[2] = {vopq(sm0 + l16 + kq * 12288), vopq(sm0 + l16 + kq * 12288 + 2 * HB)};
  const unsigned ea = vopq(sm0 + EX + nq * 3072 + l16);
  f32x4 acc[3];
  auto mm = [&](auto kc, auto jc, const bf16x8& x) {
    constexpr int k = decltype(kc)::value;
    if constexpr (decltype(jc)::value < 3) mf<k == 0>(acc[decltype(jc)::value % 3], W[k][decltype(jc)::value % 3], x);
  };
  if (wave < 4) {
    Ring<12, PDD, 2> bq;
    const char* hs = cx.ws + WS_H + (size_t)(cx.g * 6 + ks) * 64 * HB + nq * 6144;
    const char* fr = cx.fl + TLAG * 128;
    const unsigned l3 = vopq(l16 + 3072), mb = sm0 + nq * 6144, pv = vopq((F_H + 6 * ks + min(lane, 5)) * FSTR);
    auto fetch = [&](auto sc) { dma<decltype(sc)::value * HB, 3>(hs, l16, mb); dma<decltype(sc)::value * HB + 3072, 3>(hs, l3, mb); };
    const unsigned lm[3] = {l16, vopq(l16 + 2048), vopq(l16 + 4096)};
    unsigned dl = cx.dl;
    poll(fr, pv, tag, dl); fetch(IC<0>{});
    poll(fr + 128, pv, tag, dl);
    adv(fr, 256);
    adv(hs, HB); fetch(IC<1>{});
    prio<PR_A>();
    bar_bare();
    periods([&](auto jc, int it) {
      constexpr int j = decltype(jc)::value;
      unsigned fw;
      prime<(j & 1) * HB>(bq, la[j >> 1]);
      gld4<j * 128>(fw, fr, pv);
      stream<(j & 1) * HB, -1, 0>(bq, la[j >> 1], mm, [&](auto kc) {
        constexpr int k = decltype(kc)::value;
        if constexpr (k == 6) adv(hs, HB);
        if constexpr (k == 7 && j == 2) dl = sopq((it == 15) ? 0u : dl);
      });
      prio<3>();
      check_flags<0, j * 128>(fw, fr, pv, tag, dl);
      asm volatile("s_nop 1");
      static_for<0, 3>([&](auto mc) {
        constexpr int m = decltype(mc)::value;
        dma<((j + 2) & 3) * HB + m * 2048, 2>(hs, lm[m], mb);
        lds_wr<(j & 1) * 12288 + m * 1024>(ea, acc[m]);
      });
      prio<PR_A>();
      if constexpr (j == 3) adv(fr, 512);
      bar_raw();
    });
    bar_bare();
    wait_vm<0>();
  } else {
    Ring<12, 12, 12> bq;
    const char* pd = cx.ws + WS_P + (size_t)(cx.g * 18 + 6 * ns + nq) * 64 * PB + ks * 1024 - PB;
    const unsigned hv = vopq((g >> 1) * 256 + r * 16 + (4 + (nq >> 1) - nq) * 64 * PB + (nq & 1) * 512), vt = vopq(tag);
    const char* fp = cx.fp - 512;
    bar_bare();
    prime<0>(bq, la[0]);
    wait_lg<0>();
    periods([&](auto jc, int it) {
      constexpr int j = decltype(jc)::value, j1 = (j + 1) & 3;
      f32x4 e[3];
      stream<(j & 1) * HB, BARD, (2)>(bq, la[j >> 1], mm, [&](auto kc) {
        constexpr int k = decltype(kc)::value;
        if constexpr (k == 5 && j == 0) adv(fp, 512);
        if constexpr (k == 6) adv(pd, PB);
        if constexpr (k == BARD) static_for<0, 3>([&](auto mc) { constexpr int m = decltype(mc)::value; lds_rd<(j & 1) * 12288 + m * 1024>(e[m], ea); });
      });
      flag_by<(j + 1) * 128>(cx.w0, fp, l16, vt);
      asm volatile("s_nop 3");
      wait_lg<0>();
      tie(acc[0], acc[1], acc[2], e[0], e[1], e[2]);
      gst16(pd, l16, pack8(e[0] + acc[0], e[1] + acc[1]));
      e[2] += acc[2];
      gst16m(pd, hv, widen(cvt2(e[2][0], e[2][1]), cvt2(e[2][2], e[2][3])), HALF);
      prio<0>();
      prime<(j1 & 1) * HB>(bq, la[j1 >> 1]);
      if constexpr (j == 3) wait_lg<0>();
    });
    wait_vm<0>();
    bar_bare();
    flag_by<640>(cx.w0, fp, l16, vt); flag_by<768>(cx.w0, fp, l16, vt);
  }
}

template <bool A> __device__ __forceinline__ void role_wo(char* smem, const u16* wo, const Ctx& cx, int jw) {
  const int wave = cx.wave, lane = cx.lane;
  const unsigned sm0 = lds_addr(smem);
  unsigned tag;
  const int r = lane & 15, g = lane >> 4, q = r >> 2, p = lane & 3;
  constexpr int RS = 4 * TB, RT = 10240, EX = RS + 4 * RT;
  bf16x8 W[18][2], WX[9];
  {
    constexpr int BUF = 40960;
    const char* src = (const char*)wo + jw * 576;
    unsigned vo[5], vx[5];
#pragma unroll
    for (int i = 0; i < 5; ++i) {
      const unsigned ch = min((wave * 5 + i) * 64 + lane, 2303), row = ch / 36, cc = (ch - row * 36) * 16;
      const unsigned kl = row >> 5, gg = (row >> 3) & 3, e = row & 7;
      vo[i] = (36 * kl + 20 * (e >> 2) + 4 * gg + (e & 3)) * (DM * 2) + cc;
      vx[i] = (72 * (4 * kl + gg) + 16 + 36 * (e >> 2) + (e & 3)) * (DM * 2) + cc;
    }
    const char* rb = smem + (8 * g + q) * 576 + p * 16;
    load_w<9, 5>(smem, cx, tag, [&](auto rc) {
      constexpr int rr = decltype(rc)::value;
#pragma unroll
      for (int i = 0; i < 5; ++i) dma_w(src + (rr < 8 ? rr * 72 * (DM * 2) : 0), rr < 8 ? vo[i] : vx[i], sm0 + (rr % 3) * BUF + (wave * 5 + i) * 1024);
    }, [&](auto rc) {
      constexpr int rr = decltype(rc)::value;
      bf16x8 wx[2];
#pragma unroll
      for (int kl = 0; kl < 2; ++kl) {
        const char* a = rb + (rr % 3) * BUF + kl * 32 * 576;
#pragma unroll
        for (int m = 0; m < 2; ++m) W[2 * rr + kl][m] = tr_frag(a + wave * 64 + m * 8, a + wave * 64 + m * 8 + 4 * 576);
        if constexpr (A) wx[kl] = tr_frag(a + 512 + (wave & 1) * 8, a + 512 + (wave & 1) * 8 + 4 * 576);
      }
      tie(W[2 * rr][0], W[2 * rr][1], W[2 * rr + 1][0], W[2 * rr + 1][1]);
      if constexpr (A) {
#pragma unroll
        for (int kl = 0; kl < 2; ++kl) {
          const int k = 2 * rr + kl;
          if (k < 9) WX[k % 9] = wx[kl]; else if (wave & 2) WX[k % 9] = wx[kl];
          tie(WX[k % 9]);
        }
      }
    }, [](auto) {});
  }
  const unsigned l16 = vopq(lane * 16), l3 = vopq(l16 + 3072);
  const unsigned la[2] = {vopq(sm0 + l16), vopq(sm0 + l16 + 2 * TB)};
  const unsigned ra = vopq(sm0 + RS + wave * 1024 + l16), mr = sm0 + RS + wave * 1024;
  const char* xo = cx.ws + WS_X1 + (size_t)cx.g * 64 * TB + jw * 9216 + wave * 1024 - TB;
  const char* rs = cx.ws + WS_X0 + (size_t)((cx.g & 1) * 64 + top(cx.g)) * TB + (9 * jw + wave) * 1024;
  constexpr long RSW = TB - 32l * TB - (long)(WS_X0 - WS_X2);
  poll(cx.ws + WS_F2 + ((cx.g & 1) * 72 + top(cx.g)) * F2ROW, F_X0 * 128 + (lane & 1) * F2ROW, tag, cx.dl);
  f32x4 a0, a1, ax;
  Ring<18, PDG, 1> bq;
  auto init = [&](const u32x4& x) {
    a0 = f32x4{blo(x[0]), bhi(x[0]), blo(x[1]), bhi(x[1])}; a1 = f32x4{blo(x[2]), bhi(x[2]), blo(x[3]), bhi(x[3])};
    tie(a0, a1);
  };
  const unsigned l8 = vopq(l16 + (8 - wave) * 1024), r8 = vopq(ra + 8192 + (wave & 1) * 8);
  auto init_x = [&](const u32x2& x) { ax = f32x4{blo(x[0]), bhi(x[0]), blo(x[1]), bhi(x[1])}; tie(ax); };
  dma<0, 1>(rs, l16, mr); if (wave < 2) dma<8192, 1>(rs, l8, mr);
  adv(rs, TB);
  dma<RT, 1>(rs, l16, mr); if (wave < 2) dma<RT + 8192, 1>(rs, l8, mr);
  wait_vm<0>();
  init(*(const u32x4*)(smem + RS + wave * 1024 + lane * 16));
  if constexpr (A) init_x(*(const u32x2*)(smem + RS + 8192 + (wave & 1) * 1032 + lane * 16));
  if constexpr (A) {
    const unsigned ea = vopq(sm0 + EX + wave * 1024 + l16);
    prio<PR_A>();
    bar_bare();
    auto loop = [&](auto hc) {
      constexpr int KH = decltype(hc)::value;
      auto mm = [&](auto kc, auto jc, const bf16x8& x) {
        constexpr int k = decltype(kc)::value;
        if constexpr (decltype(jc)::value == 0) mf<false>(a0, W[k][0], x);
        if constexpr (decltype(jc)::value == 1) mf<false>(a1, W[k][1], x);
        if constexpr (decltype(jc)::value == 2 && k / 9 == KH) mf<k == 9>(ax, WX[k % 9], x);
      };
      periods([&](auto jc, int it) {
        constexpr int j = decltype(jc)::value;
        u32x4 x; u32x2 x8;
        prime<(j & 1) * TB>(bq, la[j >> 1]);
        stream<(j & 1) * TB, -1, 0>(bq, la[j >> 1], mm, [&](auto kc) {
          constexpr int k = decltype(kc)::value;
          if constexpr (k == 12) adv(rs, (j == 2 && it == 7) ? RSW : TB);
          if constexpr (k == 13) adv(xo, TB);
          if constexpr (k == 16) { wait_vm<0>(); lds_rd<((j + 1) & 3) * RT>(x, ra); }
          if constexpr (KH == 0 && k == 17) lds_rd8<((j + 1) & 3) * RT>(x8, r8);
        });
        prio<3>();
        dma<((j + 2) & 3) * RT, 1>(rs, l16, mr);
        if constexpr (KH == 0) dma<((j + 2) & 3) * RT + 8192, 1>(rs, l8, mr);
        asm volatile("s_nop 3" : "+v"(a0), "+v"(a1), "+v"(ax) :: "memory");
        gst16(xo, l16, pack8(a0, a1));
        asm volatile("s_waitcnt lgkmcnt(0)" : "+v"(x) :: "memory");
        init(x);
        prio<0>();
        lds_wr<(j & 1) * 4096>(ea, ax);
        if constexpr (KH == 0) { asm volatile("s_waitcnt lgkmcnt(0)" : "+v"(x8) :: "memory"); init_x(x8); }
        prio<PR_A>();
        bar_raw();
      });
      wait_vm<0>();
      bar_bare();
    };
    if (wave < 2) loop(IC<0>{}); else loop(IC<1>{});
  } else {
    const int b = (wave == 4) ? 0 : wave - 5;
    const char* os = cx.ws + WS_O + (size_t)(cx.g * 68 + 3) * TB + b * 6144;
    const unsigned mb = sm0 + b * 6144, ea = vopq(sm0 + EX + l16), pv = vopq((F_O + min(lane, 23)) * FSTR);
    auto mm = [&](auto kc, auto jc, const bf16x8& x) {
      constexpr int k = decltype(kc)::value;
      if constexpr (decltype(jc)::value == 0) mf<false>(a0, W[k][0], x);
      if constexpr (decltype(jc)::value == 1) mf<false>(a1, W[k][1], x);
    };
    auto loop = [&](auto pc) {
      constexpr bool SH = decltype(pc)::value;
      auto fetch = [&](auto sc) { dma<decltype(sc)::value * TB, 3>(os, l16, mb); dma<decltype(sc)::value * TB + 3072, 3>(os, l3, mb); };
      const char *fr = cx.fl + OLAG * 128, *fp = cx.fp - 512;
      const unsigned vt = vopq(tag);
      unsigned dl = cx.dl;
      if constexpr (!SH) { poll(fr, pv, tag, dl); fetch(IC<0>{}); wait_vm<0>(); }
      bar_bare();
      if constexpr (!SH) { poll(fr + 128, pv, tag, dl); adv(os, TB); fetch(IC<1>{}); }
      adv(fr, 256);
      unsigned fw;
      if constexpr (!SH) gld4<0>(fw, fr, pv);
      prime<0>(bq, la[0]);
      wait_lg<0>();
      periods([&](auto jc, int it) {
        constexpr int j = decltype(jc)::value, j1 = (j + 1) & 3;
        u32x4 x; f32x4 e[4];
        stream<(j & 1) * TB, BARG, (SH ? 0 : 1)>(bq, la[j >> 1], mm, [&](auto kc) {
          constexpr int k = decltype(kc)::value;
          if constexpr (!SH && k == 10 && j == 2) dl = sopq((it == 15) ? 0u : dl);
          if constexpr (SH && k == 10 && j == 0) adv(fp, 512);
          if constexpr (SH && k == BARG) gst4m<1, (j + 1) * 128>(fp, l16, vt);
          if constexpr (k == 11) adv(rs, (j == 2 && it == 7) ? RSW : TB);
          if constexpr (k == 12) adv(xo, TB);
          if constexpr (!SH && k == 9) adv(os, (j == 2 && it == 7) ? 2 * TB : TB);
        });
        if constexpr (!SH) { check_flags<0, j * 128>(fw, fr, pv, tag, dl); fetch(IC<(j + 2) & 3>{}); }
        dma<((j + 2) & 3) * RT, 1>(rs, l16, mr);
        prio<0>();
        if constexpr (SH) {
          static_for<0, 4>([&](auto mc) { constexpr int m = decltype(mc)::value; lds_rd<(j & 1) * 4096 + m * 1024>(e[m], ea); });
        }
        lds_rd<j1 * RT>(x, ra);
        prio<3>();
        tie(a0, a1);
        gst16(xo, l16, pack8(a0, a1));
        asm volatile("s_waitcnt lgkmcnt(0)" : "+v"(x) :: "memory");
        if constexpr (SH) {
          tie(e[0], e[1], e[2], e[3]);
          gst16(xo + 3072, l16, pack8(e[0] + e[2], e[1] + e[3]));
        }
        init(x);
        prio<0>();
        prime<(j1 & 1) * TB>(bq, la[j1 >> 1]);
        if constexpr (j == 1) adv(fr, (it == 7) ? 128 : 0);
        if constexpr (!SH) gld4<(j + 1) * 128>(fw, fr, pv);
        if constexpr (j == 3) { adv(fr, 512); wait_lg<0>(); }
      });
      wait_vm<0>();
      if constexpr (!SH) tie(fw);
      bar_bare();
      if constexpr (SH) gst4m<1, 640>(fp, l16, vt);
    };
    if (wave == 5) loop(std::true_type{}); else loop(std::false_type{});
  }
}

template <class F> __device__ __forceinline__ void qa_periods(F&& f) {
#pragma nounroll
  for (int it = 0; it < 16; ++it) static_for<0, 4>([&](auto jc) { f(jc, IC<0>{}, it); });
  static_for<0, 4>([&](auto jc) { f(jc, IC<1>{}, 16); });
}
template <bool A> __device__ __forceinline__ void role_qa(char* smem, const u16* wq, const Ctx& cx, int jq) {
  const int wave = cx.wave, lane = cx.lane;
  const unsigned sm0 = lds_addr(smem);
  unsigned tag;
  const int r = lane & 15, g = lane >> 4, q = r >> 2, p = lane & 3;
  constexpr int XR = 4 * TB, PP = XR + 2048, ST = PP + 8192;
  const f32x4 z4 = {0.f, 0.f, 0.f, 0.f};
  const unsigned l16 = vopq(lane * 16);
  const bool low = cx.g < 2;
  const char* xs = cx.ws + WS_X0 + (size_t)((cx.g & 1) * 64 + top(cx.g)) * TB + (wave & 3) * 4096;
  const char* fr = cx.ws + WS_F2 + ((cx.g & 1) * 72 + top(cx.g)) * F2ROW;
  const unsigned mb = sm0 + (wave & 3) * 4096, l4 = vopq(l16 + 4096);
  auto fetch = [&](auto sc, auto nc) {
    dma<decltype(sc)::value * TB, 4>(xs, l16, mb);
    if constexpr (decltype(nc)::value == 6) dma<decltype(sc)::value * TB + 4096, 2>(xs, l4, mb);
  };
  bf16x8 W[18][2];
  f32x4 cs[2] = {z4, z4};
  bf16x8 one = ones8();
  tie(one);
  {
    constexpr int BUF = 41472, B0 = 2 * TB;
    const char* src = (const char*)wq + jq * 144 + wave * 12 * (NQKV * 2);
    const unsigned sg = (min(lane, 53) * 57) >> 9, vo = sg * (DM * 2 - 144) + min(lane, 53) * 16;
    int c0, c1;
    if (!A) { c0 = 36 * (wave & 3) + (wave & 1) * 4 + p * 8; c1 = c0 + 144; }
    else if (wave < 2) { c0 = 288 + 72 * wave + p * 8; c1 = c0 + 40; }
    else { c0 = (p >> 1) * 144 + 32 + (p & 1) * 72; c1 = 288 + 32 + (p & 1) * 72; }
    const char* rb = smem + B0 + (8 * g + q) * 432;
    unsigned f0; const unsigned f0v = vopq(F_X0 * 128 + (lane & 3) * F2ROW);
    load_w<6, 6, 5>(smem, cx, tag, [&](auto rc) {
      constexpr int rr = decltype(rc)::value;
      if constexpr (rr == 5) gld4<0>(f0, fr, f0v);
#pragma unroll
      for (int i = 0; i < 6; ++i) dma_w54(src + (rr * 96 + 2 * i) * (NQKV * 2), vo, sm0 + B0 + (rr % 3) * BUF + (wave * 6 + i) * 864);
    }, [&](auto rc) {
      constexpr int rr = decltype(rc)::value;
#pragma unroll
      for (int kl = 0; kl < 3; ++kl)
#pragma unroll
        for (int m = 0; m < 2; ++m) {
          const char* a = rb + (rr % 3) * BUF + kl * 32 * 432 + (m ? c1 : c0);
          W[3 * rr + kl][m] = tr_frag(a, a + 4 * 432);
        }
      tie(W[3 * rr][0], W[3 * rr][1], W[3 * rr + 1][0], W[3 * rr + 1][1], W[3 * rr + 2][0], W[3 * rr + 2][1]);
    }, [&](auto rc) {
      if constexpr (decltype(rc)::value == 3) {
        check_flags<6>(f0, fr, f0v, tag, cx.dl);
        const char* x2 = xs - (wave & 3) * 4096 + wave * 4608;
        dma<0, 4>(x2, l16, sm0 + wave * 4608); dma<4096, 0>(x2, l4, sm0 + wave * 4608);
      }
#pragma unroll
      for (int k = 0; k < 6; ++k) cs[k & 1] = MFMA(one, W[3 * decltype(rc)::value + (k >> 1)][k & 1], cs[k & 1]);
      tie(cs[0], cs[1]);
    });
  }
  auto quad = [&](const f32x4& c) {
    f32x4 o;
#pragma unroll
    for (int j = 0; j < 4; ++j) o[j] = u2f(__builtin_amdgcn_ds_bpermute((4 * g + j) * 4, f2i(c[0])));
    return o;
  };
  const unsigned la[2] = {vopq(sm0 + l16), vopq(sm0 + l16 + 2 * TB)};
  if constexpr (A) {
    Ring<18, PDG, 1> bq;
    unsigned pv = vopq(F_X0 * 128), dl = cx.dl, fw = vopq(tag);
    adv(fr, 2 * F2ROW); adv(xs, 2 * TB);
    auto feed = [&](auto jc, auto nc, int it) {
      constexpr int j = decltype(jc)::value;
      const char* xn = xs + TB;
      adv(fr, F2ROW);
      if constexpr (j == 1) { const bool st = low && it == 7; dl = st ? 0u : dl; xn = st ? xs : xn; }
      if constexpr (j == 2) {
        const bool st = low && it == 15; dl = st ? 0u : cx.dl; xn = st ? xs : xn;
        if (it == 7) { xn = cx.ws + WS_X2 + (size_t)((cx.g & 1) * 64 + top(cx.g)) * TB + wave * 4096; fr -= 33 * F2ROW; pv = vopq(min(lane, 17) * 128); }
      }
      gld4<0>(fw, fr, pv);
      fetch(IC<(j + 2) & 3>{}, nc);
      xs = xn; adv(xs, 0);
    };
    if (wave == 3) {
      f32x4 a0, a1;
      const unsigned sa = vopq(sm0 + ST + ((g == q) ? r * 8 : 256));
      auto mm = [&](auto kc, auto jc, const bf16x8& x) {
        constexpr int k = decltype(kc)::value;
        if constexpr (decltype(jc)::value == 0) mf<k == 0>(a0, one, x);
        if constexpr (decltype(jc)::value == 1) mf<k == 0>(a1, x, x);
      };
      prio<PR_A>();
      wait_vm<0>();
      bar_raw();
      qa_periods([&](auto jc, auto ec, int it) {
        constexpr int j = decltype(jc)::value; constexpr bool END = decltype(ec)::value;
        if constexpr (!END || j < 2) {
          prime<(j & 1) * TB>(bq, la[j >> 1]);
          stream<(j & 1) * TB, -1, 0>(bq, la[j >> 1], mm, [&](auto kc) {
            constexpr int k = decltype(kc)::value;
          });
          prio<0>();
          if constexpr (!END) { check_flags<6>(fw, fr, pv, tag, dl); feed(jc, IC<6>{}, it); }
          else { wait_vm<0>(); tie(fw); settle(); }
          tie(a0, a1);
          lds_wr8<(j & 1) * 128>(sa, row_stat(a0[0], diag(a1, p)));
          prio<PR_A>();
          if constexpr (!END) wait_vm<7>();
        }
        bar_raw();
      });
      if (cx.g == 0 && jq == 0) {
        for (int g2 = 0; g2 < 4; ++g2) poll(cx.ws + WS_FL + g2 * FGRP + 128, lane * FSTR, tag, cx.dl);
        gst4m<1>(cx.ws, l16, vopq((tag - 0x7F4A7C15u) * 244002641u + 1u));
      }
    } else {
      const unsigned sa = vopq(sm0 + ST + g * 32), xa = vopq(sm0 + XR + l16), vt = vopq(tag);
      auto loop = [&](auto xc) {
        constexpr bool X = decltype(xc)::value;
        if constexpr (X) *(f32x4*)(smem + XR + 1024 + lane * 16) = quad(cs[0]);
        const float cv[2] = {cs[0][0], cs[1][0]};
        f32x4 a[2][2] = {{z4, z4}, {z4, z4}};
        u32x4 vw[2] = {{0u, 0u, 0u, 0u}, {0u, 0u, 0u, 0u}};
        const unsigned pa = vopq(sm0 + PP + (X ? 0 : wave * 2048) + l16);
        const char* ob = cx.ws + WS_O + (size_t)cx.g * 68 * TB + (X ? 16384 + jq * 256 : (2 * jq + wave) * 1024) - TB;
        const char* fp = cx.fp + wave * 8 * FSTR;
        prio<PR_A>();
        wait_vm<0>();
        bar_raw();
        qa_periods([&](auto jc, auto ec, int it) {
          constexpr int j = decltype(jc)::value, e = j & 1, o = 1 - e; constexpr bool END = decltype(ec)::value;
          bf16x8 pf[4]; f32x4 d[4], s0, s1;
          auto reads = [&](auto ic) {
            constexpr int i = decltype(ic)::value;
            if constexpr (i < (X ? 4 : 2)) lds_rd<o * 4096 + i * 1024>(pf[i], pa);
            if constexpr (i == 4) lds_rd<o * 128>(s0, sa);
            if constexpr (i == 5) lds_rd<o * 128 + 16>(s1, sa);
          };
          auto mm = [&](auto kc, auto ic, const bf16x8& x) {
            constexpr int k = decltype(kc)::value;
            if constexpr (decltype(ic)::value == 0) { if constexpr (X) mf<k == 0>(a[e][0], W[k][0], x); else mf<k == 0>(a[e][0], x, W[k][0]); }
            if constexpr (decltype(ic)::value == 1) mf<k == 0>(a[e][1], x, W[k][1]);
          };
          if constexpr (!END || j < 2) {
            prime<e * TB>(bq, la[j >> 1]);
            stream<e * TB, -1, 0>(bq, la[j >> 1], mm, [&](auto kc) {
              constexpr int k = decltype(kc)::value;
              if constexpr (k >= 3 && k < 9) reads(IC<k - 3>{});
              if constexpr (k == 13) adv(ob, TB);
            });
          } else { adv(ob, TB); static_for<0, 6>([&](auto ic) { reads(ic); }); }
          if constexpr (END && j >= 2) wait_frag<0>();
          static_for<0, X ? 4 : 2>([&](auto hc) { constexpr int h = decltype(hc)::value; mf<true>(d[h], asb(vw[X ? 1 : h]), pf[h]); });
          asm volatile("s_setprio 0" : "+v"(s0), "+v"(s1) :: "memory");
          if constexpr (!END) check_flags<4>(fw, fr, pv, tag, dl); else { wait_vm<0>(); tie(fw); settle(); }
          gst4m<1, j * 128>(fp, l16, vt);
          if constexpr (X) {
            if constexpr (!END || j < 2) lds_wr<e * 1024>(xa, a[e][0]);
            tie(d[0], d[1], d[2], d[3]);
            unsigned e2 = cvt2(d[2][0], d[2][1]), e3 = cvt2(d[3][2], d[3][3]), e0 = cvt2(d[0][0], d[0][1]), e1 = cvt2(d[1][2], d[1][3]), n2, n3;
            asm volatile("s_nop 0\n\tv_permlane16_swap_b32 %0, %2\n\tv_permlane16_swap_b32 %1, %3" : "+v"(e2), "+v"(e3), "=&v"(n2), "=&v"(n3) : "v"(e0), "v"(e1));
            gst16m(ob, l16, u32x4{e0, e1, n2, n3}, 0xFFFFull);
          } else { tie(d[0], d[1]); gst16(ob, l16, pack8(d[0], d[1])); }
          if constexpr (!END) feed(jc, IC<4>{}, it);
#pragma unroll
          for (int m = X ? 1 : 0; m < 2; ++m) {
            const f32x4 w = a[o][m];
            vw[m][2 * o] = cvt2(fmaf(cv[m], s0[0], w[0]) * s0[1], fmaf(cv[m], s0[2], w[1]) * s0[3]);
            vw[m][2 * o + 1] = cvt2(fmaf(cv[m], s1[0], w[2]) * s1[1], fmaf(cv[m], s1[2], w[3]) * s1[3]);
            tie(vw[m]);
          }
          prio<PR_A>();
          if constexpr (j == 3) adv(fp, 512);
          if constexpr (!END) wait_vm<7>();
          bar_raw();
        });
        wait_vm<0>();
        gst4m<1>(fp, l16, vt);
      };
      if (wave == 2) loop(std::true_type{}); else loop(std::false_type{});
    }
  } else {
    wait_vm<0>();
    bar_bare();
    const int h = wave - 4;
    f32x4 cq = quad(cs[0]), ck = quad(cs[1]);
    tie(cq, ck);
    const float ninf = -__builtin_inff();
    f32x4 bo, bp;
#pragma unroll
    for (int e = 0; e < 4; ++e) { const int m = 4 * g + e; bo[e] = (m <= r && m >= r - 7) ? 0.f : ninf; bp[e] = (m >= r + 9) ? 0.f : ninf; }
    Soft f = {};
#pragma unroll
    for (int e = 0; e < 2; ++e) f.mo[e] = vopq((4 * g + 2 * e <= r ? 0xFFFFu : 0u) | (4 * g + 2 * e + 1 <= r ? 0xFFFF0000u : 0u));
    const unsigned xa = vopq(sm0 + XR + (h >> 1) * 256 + r * 16 + (h & 1) * 8), sa = vopq(sm0 + ST + r * 8), pa = vopq(sm0 + PP + h * 1024 + l16);
    const float g0 = (g == 0) ? 1.f : 0.f;
    f32x2 cxq, cxk;
    lds_rd8<1024>(cxq, xa); lds_rd8<1536>(cxk, xa);
    bf16x8 x[18];
    auto first = [&](auto oc, unsigned a) { static_for<0, 8>([&](auto kc) { lds_rd<decltype(oc)::value + decltype(kc)::value * 1024>(x[decltype(kc)::value], a); }); };
    first(IC<0>{}, la[0]);
    wait_lg<0>();
    tie(cxq, cxk);
    f32x4 a0 = z4, a1 = z4, so = z4;
    u32x4 qf = {0u, 0u, 0u, 0u}, kf = qf;
    float sc = 0.f, scn = 0.f;
    tie(qf, kf, bo, bp);
    qa_periods([&](auto jc, auto ec, int it) {
      constexpr int j = decltype(jc)::value, e = j & 1, j1 = (j + 1) & 3; constexpr bool END = decltype(ec)::value;
      auto put = [&]() { lds_wr<e * 4096>(pa, f32x4{f.s[0], f.s[1], f.s[2], f.s[3]}); };
      if constexpr (END && j >= 2) {
        if constexpr (j == 2) { soft3(f); put(); }
        bar_raw();
      } else {
        prio<PR_B>();
        static_for<0, 5>([&](auto pc) {
          constexpr int k = 2 * decltype(pc)::value;
          wait_frag<6>();
          qk_pair<k / 2, e, e * TB + (k + 8) * 1024>(a0, a1, W[k], W[k + 1], x[k], x[k + 1], x[k + 8], x[k + 9], la[j >> 1], f);
        });
        wait_frag<4>();
        mf<false>(a0, W[10][0], x[10]);
        put();
        mf<false>(a1, W[10][1], x[10]);
        static_for<11, 14>([&](auto kc) {
          constexpr int k = decltype(kc)::value;
          wait_frag<5>();
          mf<false>(a0, W[k][0], x[k]);
          if constexpr (k == 13) asm volatile("s_setprio 3\n\ts_waitcnt lgkmcnt(0)" ::: "memory");
          mf<false>(a1, W[k][1], x[k]);
        });
        bar_bare();
        f32x2 st, xq, xk;
        mf8<e>(a0, a1, W[14], W[15], W[16], W[17], x[14], x[15], x[16], x[17], st, xk, xq, sa, xa);
        asm volatile("s_waitcnt lgkmcnt(0)" : "+v"(st), "+v"(xq), "+v"(xk) :: "memory");
        const float ix = st[1] * g0;
        kf[2] = cvt2(fmaf(cxk[0], st[0], xk[0]) * ix, fmaf(cxk[1], st[0], xk[1]) * ix);
        sc = scn; scn = st[1] * (0.23570226039551584f * 1.4426950408889634f);
        asm volatile("" : "+v"(kf), "+v"(scn), "+v"(a0), "+v"(a1));
        kf[0] = cvt2(fmaf(ck[0], st[0], a1[0]) * st[1], fmaf(ck[1], st[0], a1[1]) * st[1]);
        kf[1] = cvt2(fmaf(ck[2], st[0], a1[2]) * st[1], fmaf(ck[3], st[0], a1[3]) * st[1]);
        f32x4 sp;
        if constexpr ((!END && j == 0) || (END && j == 1)) {
          const float off = (low && (END || it == 8)) ? ninf : 0.f;
          sp = MFMA(asb(kf), asb(qf), bp + off);
        } else sp = MFMA(asb(kf), asb(qf), bp);
        __builtin_amdgcn_sched_barrier(0);
        qf[0] = cvt2(fmaf(cq[0], st[0], a0[0]), fmaf(cq[1], st[0], a0[1]));
        qf[1] = cvt2(fmaf(cq[2], st[0], a0[2]), fmaf(cq[3], st[0], a0[3]));
        qf[2] = cvt2(fmaf(cxq[0], st[0], xq[0]), fmaf(cxq[1], st[0], xq[1]));
        asm volatile("" : "+v"(qf), "+v"(sp));
        max4(f.s, so, sp);
        if constexpr (!(END && j == 1)) mfc(so, asb(kf), asb(qf), bo);
        soft1(f, sc);
        if constexpr (END) tie(bo);
        soft2<(END && j == 1) ? -1 : (j1 & 1) * TB>(f, x, la[j1 >> 1]);
        if constexpr (j == 3) wait_lg<0>();
      }
    });
  }
}

__global__ __launch_bounds__(512, 2) void k_main(const u16* x0, const u16* wqkv, const u16* wo,
                                                 const u16* wg, const u16* wu, const u16* wd,
                                                 char* ws, u16* out) {
  __shared__ __attribute__((aligned(1024))) char smem[SMEM_BYTES];
  Ctx cx;
  const unsigned dm = lds_addr(smem) + 162816;
  {
    unsigned long long pc; unsigned len;
    asm volatile("s_getpc_b64 %0\n.Lkbeg:\n\ts_mov_b32 %1, .Lkend-.Lkbeg" : "=s"(pc), "=s"(len));
    cx.code = (const char*)pc + min(((blockIdx.x & 8) * 64 + threadIdx.x) * 128, len);
    const unsigned i0 = blockIdx.x >> 3;
    if (i0 >= 22 && i0 < 30) touch(cx.code, dm);
    if (i0 < 5 && threadIdx.x < 64) touch((i0 < 4 ? (const char*)wqkv : (const char*)wo) + (threadIdx.x & 1) * 524288, dm);
    if (i0 >= 5 && i0 < 30) {
      const unsigned ln = min((i0 - 5) * 512 + threadIdx.x, 11519u), sg = (ln * 13108u) >> 16;
      const char* bw = (i0 * 512 + (threadIdx.x & 448) < 5 * 512 + 8640) ? (const char*)wqkv : (const char*)wo - 1728 * 1152;
      touch(bw + (sg * (1152 - 640) + ((blockIdx.x & 1) * 4 + ln) * 128), dm);
    }
  }
  cx.ws = ws;
  cx.wave = __builtin_amdgcn_readfirstlane(threadIdx.x >> 6);
  cx.lane = threadIdx.x & 63;
  asm volatile("s_cmp_eq_u32 %1, 4\n\ts_cselect_b64 %0, 1, 0" : "=s"(cx.w0) : "s"(cx.wave) : "scc");
  cx.dl = ((unsigned)__builtin_amdgcn_s_memtime() + SPIN) | 1u;
  const unsigned t0 = (unsigned)__builtin_amdgcn_s_memrealtime();
  const int bid = sopq(blockIdx.x), xh = bid & 1, x4 = bid & 3, qd = (bid >> 2) & 1, i = bid >> 3, lane = cx.lane, wave = cx.wave;
  const int d = i - 23, tri = d / 3;
  const int c = x4 * 9 + ((i - 5) >> 1), ks = (tri < 2) ? x4 : 4 + (x4 >> 1), ns = d - 3 * tri;
  cx.g = (i < 4) ? 2 * (1 - qd) + 1 - (x4 >> 1) : (i == 4) ? 2 * (1 - qd) + (x4 >> 1) : (i < 23) ? 2 * qd + ((i - 5) & 1) : 2 * (1 - qd) + (tri < 2 ? tri : xh);
  cx.fl = ws + WS_FL + cx.g * FGRP;
  cx.fp = cx.fl + (i < 4 ? F_O + xh * 4 + i : i == 4 ? F_X1 + xh : i < 23 ? F_H + c : F_P + ns * 6 + ks) * FSTR;
  const bool fast = i >= 30;
  auto warm = [&]() {
    if (wave < 2) touch(ws + min((size_t)(wave * 64 + lane) << 20, WS_END - 4), dm);
    else if (wave == 2) touch((const char*)out + min(lane << 20, 2359292), dm);
    else if (wave == 3 && i >= 5 && i < 20) {
      const int ln = min(((i - 5) * 2 + xh) * 64 + lane, 26 * 72 - 1);
      touch(ws + WS_FL + (size_t)((bid >> 1) & 3) * FGRP + F_X1 * FSTR + ln * 128, dm);
    }
  };
  if (!fast && i >= 5) warm();
  if (i >= 23) {
    u32x4 v[5];
    const int g0 = (bid >> 1) & 3, n0 = (fast ? i - 30 : 2 + (wave >> 2) * 7 + i - 23) * 2 + xh;
    const int k0 = fast ? 2 * wave : 4 * (wave & 3), nk = fast ? 2 : 4, wl = fast ? wave : wave & 3;
    const char* src = (const char*)x0 + (size_t)((g0 >> 1) * 1024 + 32 * (31 - n0) + (g0 & 1)) * (DM * 2);
    const char* dst = ws + WS_X0 + (size_t)((g0 & 1) * 64 + top(g0) + n0) * TB;
    const unsigned vr = (lane & 15) * 2304 + (lane >> 4) * 16;
    gld16(v[0], src + k0 * 64, vr); gld16(v[1], src + k0 * 64 + 64, vr); gld16(v[4], src + (16 + (wl & 1)) * 64, vr);
    gld16(v[2], src + (k0 + nk - 2) * 64, vr); gld16(v[3], src + (k0 + nk - 1) * 64, vr);
    const unsigned tag = get_tag(ws);
    wait_vm<0>();
    tie(v[0], v[1], v[2], v[3], v[4]);
    gst16(dst + k0 * 1024, lane * 16, v[0]); gst16(dst + k0 * 1024 + 1024, lane * 16, v[1]);
    if (wl < 2) gst16(dst + (16 + wl) * 1024, lane * 16, v[4]);
    if (!fast) { gst16(dst + k0 * 1024 + 2048, lane * 16, v[2]); gst16(dst + k0 * 1024 + 3072, lane * 16, v[3]); }
    wait_vm<0>();
    bar_bare();
    if (wl == 0) gst4m<-1>(ws + WS_F2 + ((g0 & 1) * 72 + top(g0) + n0) * F2ROW + F_X0 * 128, lane * 4, vopq(tag));
    if (fast) warm();
  }
  if (i >= 5) {
    const unsigned z = ~get_tag(ws);
    gst16(cx.fp + wave * 1024, lane * 16, u32x4{z, z, z, z});
    if (wave == 0) gst16(cx.fp + 8192, lane * 16, u32x4{z, z, z, z});
  }
  if (i >= 5) { const unsigned dly = (i < 23) ? DLY_G : DLY_D; while ((unsigned)__builtin_amdgcn_s_memrealtime() - t0 < dly) __builtin_amdgcn_s_sleep(4); }
  if (i < 4) { if (wave < 4) role_qa<true>(smem, wqkv, cx, xh * 4 + i); else role_qa<false>(smem, wqkv, cx, xh * 4 + i); }
  else if (i == 4) { if (wave < 4) role_wo<true>(smem, wo, cx, xh); else role_wo<false>(smem, wo, cx, xh); }
  else if (i < 23) role_gu(smem, wg, wu, out, cx, c);
  else role_dn(smem, wd, cx, ks, ns);
  asm volatile(".subsection 2\n.Lkend:\n\t.fill 64, 4, 0xBF800000\n\t.subsection 0");
}
#if defined(__HIP_DEVICE_COMPILE__)
#pragma clang attribute pop
#endif

extern "C" void kernel_launch(void* const* d_in, const int* in_sizes, int n_in,
                              void* d_out, int out_size, void* d_ws, size_t ws_size,
                              hipStream_t stream) {
  k_main<<<256, 512, 0, stream>>>((const u16*)d_in[0], (const u16*)d_in[1], (const u16*)d_in[2], (const u16*)d_in[4],
                                  (const u16*)d_in[5], (const u16*)d_in[6], (char*)d_ws, (u16*)d_out);
}
```

```cpp
#include <hip/hip_runtime.h>
#include <stdint.h>
#include <type_traits>

#if defined(__HIP_DEVICE_COMPILE__)
#pragma clang attribute push(__attribute__((target("no-packed-fp32-ops"))), apply_to = function)
#endif
typedef __attribute__((ext_vector_type(8))) short bf16x8;
typedef __attribute__((ext_vector_type(4))) short bf16x4;
typedef __attribute__((ext_vector_type(4))) float f32x4;
typedef __attribute__((ext_vector_type(2))) float f32x2;
typedef __attribute__((ext_vector_type(4))) unsigned u32x4;
typedef __attribute__((ext_vector_type(2))) unsigned u32x2;
typedef __bf16 bf2v __attribute__((ext_vector_type(2)));
typedef unsigned short u16;

#define MFMA(b, a, c) __builtin_amdgcn_mfma_f32_16x16x32_bf16(b, a, c, 0, 0, 0)
constexpr int DM = 576, FD = 4608, NQKV = 3 * DM;
constexpr int TB = 18432;
constexpr int HB = 24576;
constexpr int PB = 6144;
constexpr int FSTR = 72 * 128, FGRP = 64 * FSTR, F2ROW = 19 * 128;
constexpr size_t WS_FL = 4096;
constexpr size_t WS_IS = WS_FL + 4 * FGRP;
constexpr size_t WS_F2 = WS_IS + 4 * 64 * 128;
constexpr int SRB = 18 * 128;
constexpr size_t WS_SR = WS_F2 + 2 * 72 * F2ROW;
constexpr size_t WS_O = WS_SR + 260 * SRB;
constexpr size_t WS_X1 = WS_O + (size_t)4 * 68 * TB;
constexpr size_t WS_X2 = WS_X1 + (size_t)256 * TB;
constexpr size_t WS_X0 = WS_X2 + (size_t)128 * TB;
constexpr size_t WS_P = WS_X0 + (size_t)128 * TB;
constexpr size_t WS_H = WS_P + (size_t)72 * 64 * PB;
constexpr size_t WS_END = WS_H + (size_t)(24 * 64 + 4) * HB;
static_assert(WS_O % 1024 == 0 && WS_END <= ((size_t)256 << 20), "workspace");
constexpr int F_H = 0, F_P = 36, F_X1 = 54, F_O = 56, F_X0 = 18;
constexpr int TLAG = 2, PLAG = 3;
constexpr int PV1 = 0;
constexpr int PV2 = 3;
constexpr int OLAG = 4;
constexpr int NA = 3;
constexpr int WORD = 1;
#define SLP ""
constexpr int SMEM_BYTES = 163840;
constexpr int PDG = 6, PDD = 5;
constexpr int BARG = 14, BARD = 10;
constexpr int PR_A = 2, PR_B = 1;
constexpr int DLY_G = 120;
constexpr int DLY_D = 500;
constexpr int LAGF = 8;

__device__ __forceinline__ float u2f(unsigned u) { return __builtin_bit_cast(float, u); }
__device__ __forceinline__ int f2i(float f) { return __builtin_bit_cast(int, f); }
__device__ __forceinline__ float rbf(float f) { __bf16 b = (__bf16)f; return u2f((unsigned)__builtin_bit_cast(u16, b) << 16); }
__device__ __forceinline__ unsigned cvt2(float a, float b) { return __builtin_bit_cast(unsigned, __builtin_convertvector(f32x2{a, b}, bf2v)); }
__device__ __forceinline__ float blo(unsigned w) { return u2f(w << 16); }
__device__ __forceinline__ float bhi(unsigned w) { return u2f(w & 0xffff0000u); }
typedef __attribute__((address_space(3))) bf16x4* lds_b4p;
__device__ __forceinline__ bf16x4 tr_read(const char* p) {
  return __builtin_amdgcn_ds_read_tr16_b64_v4i16((lds_b4p)(__attribute__((address_space(3))) void*)(p));
}
__device__ __forceinline__ bf16x8 tr_frag(const char* lo, const char* hi) {
  return __builtin_shufflevector(tr_read(lo), tr_read(hi), 0, 1, 2, 3, 4, 5, 6, 7);
}
__device__ __forceinline__ unsigned lds_addr(const void* p) { return (unsigned)(size_t)(__attribute__((address_space(3))) const char*)p; }
template <int I, int N, class F> __device__ __forceinline__ void static_for(F&& f) {
  if constexpr (I < N) { f(std::integral_constant<int, I>{}); static_for<I + 1, N>(f); }
}
template <int N> using IC = std::integral_constant<int, N>;
constexpr int imin(int a, int b) { return a < b ? a : b; }
template <int N> __device__ __forceinline__ void wait_vm() { asm volatile("s_waitcnt vmcnt(%0)" ::"n"(N) : "memory"); }
template <int N> __device__ __forceinline__ void wait_lg() { asm volatile("s_waitcnt lgkmcnt(%0)" ::"n"(N) : "memory"); }
template <int N> __device__ __forceinline__ void wait_frag() { __builtin_amdgcn_s_waitcnt(0xC07F | (N << 8)); }
__device__ __forceinline__ void bar_raw() { asm volatile("s_waitcnt lgkmcnt(0)\n\ts_barrier" ::: "memory"); }
__device__ __forceinline__ void bar_bare() { asm volatile("s_barrier" ::: "memory"); }
template <int P> __device__ __forceinline__ void prio() { asm volatile("s_setprio %0" :: "n"(P)); }
__device__ __forceinline__ void settle() { asm volatile("s_nop 7" ::: "memory"); }
__device__ __forceinline__ int sopq(int v) { asm volatile("" : "+s"(v)); return v; }
__device__ __forceinline__ unsigned vopq(unsigned v) { asm volatile("" : "+v"(v)); return v; }
template <class A> __device__ __forceinline__ void tie(A& a) { asm volatile("" : "+v"(a)); }
template <class A, class... R> __device__ __forceinline__ void tie(A& a, R&... r) { tie(a); tie(r...); }
__device__ __forceinline__ void adv(const char*& p, long d) { p += d; asm volatile("" : "+s"(p)); }

__device__ __forceinline__ void dma_w(const void* s, unsigned v, unsigned m) {
  asm volatile("s_mov_b32 m0, %2\n\ts_nop 0\n\tglobal_load_lds_dwordx4 %1, %0" :: "s"(s), "v"(v), "s"(m) : "memory");
}
__device__ __forceinline__ void dma_w54(const void* s, unsigned v, unsigned m) {
  asm volatile("s_mov_b32 m0, %2\n\ts_mov_b32 exec_hi, 0x3fffff\n\tglobal_load_lds_dwordx4 %1, %0\n\ts_mov_b32 exec_hi, -1" :: "s"(s), "v"(v), "s"(m) : "memory");
}
#define DMA_AT(O) "\n\tglobal_load_lds_dwordx4 %1, %0 offset:" #O " sc1"
#define DMA_N(BODY) asm volatile("s_add_u32 m0, %2, %3\n\ts_nop 0" BODY :: "s"(s), "v"(v), "s"(m), "n"(MO) : "memory", "scc")
template <int MO, int N> __device__ __forceinline__ void dma(const void* s, unsigned v, unsigned m) {
  if constexpr (N == 0) DMA_N("\n\ts_mov_b32 exec_hi, 0" DMA_AT(0) "\n\ts_mov_b32 exec_hi, -1");
  if constexpr (N == 1) DMA_N(DMA_AT(0));
  if constexpr (N == 2) DMA_N(DMA_AT(0) DMA_AT(1024));
  if constexpr (N == 3) DMA_N(DMA_AT(0) DMA_AT(1024) DMA_AT(2048));
  if constexpr (N == 4) DMA_N(DMA_AT(0) DMA_AT(1024) DMA_AT(2048) DMA_AT(3072));
}
__device__ __forceinline__ void touch(const void* p, unsigned lds_dummy) {
  asm volatile("s_mov_b32 m0, %1\n\ts_nop 0\n\tglobal_load_lds_dword %0, off" :: "v"(p), "s"(lds_dummy) : "memory");
}
__device__ __forceinline__ void dma_w2(const void* s, unsigned v, unsigned m) {
  asm volatile("s_mov_b32 m0, %2\n\ts_nop 0\n\tglobal_load_lds_dwordx4 %1, %0 nt" :: "s"(s), "v"(v), "s"(m) : "memory");
}
__device__ __forceinline__ void gld16(u32x4& d, const void* s, unsigned v) { asm volatile("global_load_dwordx4 %0, %1, %2 sc1" : "=v"(d) : "v"(v), "s"(s) : "memory"); }
template <int OFF> __device__ __forceinline__ void gld4(unsigned& d, const void* s, unsigned v) { asm volatile("global_load_dword %0, %1, %2 offset:%3 sc1" : "=v"(d) : "v"(v), "s"(s), "n"(OFF) : "memory"); }
__device__ __forceinline__ void gst16(const void* s, unsigned v, u32x4 d) {
  asm volatile("global_store_dwordx4 %0, %1, %2 sc1\n\ts_nop 1" :: "v"(v), "v"(d), "s"(s) : "memory");
}
__device__ __forceinline__ void gst16m(const void* s, unsigned v, u32x4 d, unsigned long long m) {
  asm volatile("s_mov_b64 exec, %3\n\tglobal_store_dwordx4 %0, %1, %2 sc1\n\ts_mov_b64 exec, -1\n\ts_nop 0" :: "v"(v), "v"(d), "s"(s), "s"(m) : "memory");
}
__device__ __forceinline__ void gst8m(const void* s, unsigned v, f32x2 d, unsigned long long m) {
  asm volatile("s_mov_b64 exec, %3\n\tglobal_store_dwordx2 %0, %1, %2 sc1\n\ts_mov_b64 exec, -1" :: "v"(v), "v"(d), "s"(s), "s"(m) : "memory");
}
template <int OFF> __device__ __forceinline__ void gld8(f32x2& d, const void* s, unsigned v) { asm volatile("global_load_dwordx2 %0, %1, %2 offset:%3 sc1" : "=v"(d) : "v"(v), "s"(s), "n"(OFF) : "memory"); }
template <int LO, int OFF = 0> __device__ __forceinline__ void gst4m(const void* s, unsigned v, unsigned d) {
  asm volatile("s_mov_b32 exec_lo, %3\n\ts_mov_b32 exec_hi, 0\n\tglobal_store_dword %0, %1, %2 offset:%4 sc1\n\ts_mov_b64 exec, -1" :: "v"(v), "v"(d), "s"(s), "n"(LO), "n"(OFF) : "memory");
}
#define COLD(L, BACK, BODY) BACK ":\n\t.subsection 1\n" L ":\n\t" BODY "s_branch " BACK "\n\t.subsection 0"
template <int OFF> __device__ __forceinline__ void flag_by(unsigned long long who, const void* s, unsigned l16, unsigned tag) {
  asm volatile("s_mov_b64 exec, %3\n\tglobal_store_dword %0, %1, %2 offset:%4 sc1\n\ts_mov_b64 exec, -1" :: "v"(l16), "v"(tag), "s"(s), "s"(who), "n"(OFF) : "memory");
}
#define TIME_UP(DL, L) "s_memtime vcc\n\ts_waitcnt lgkmcnt(0)\n\ts_sub_u32 vcc_lo, vcc_lo, " DL "\n\ts_cmp_ge_i32 vcc_lo, 0\n\ts_cbranch_scc1 " L "\n\t"
template <int N, int OFF = 0> __device__ __forceinline__ void check_flags(unsigned& v, const void* s, unsigned voff, unsigned tag, unsigned deadline) {
  asm volatile("s_waitcnt vmcnt(%5)\n\tv_cmp_ne_u32 vcc, %4, %0\n\ts_cbranch_vccnz .Lsl%=\n"
               COLD(".Lsl%=", ".Lok%=", "s_cmp_eq_u32 %1, 0\n\ts_cbranch_scc1 .Lok%=\n"
                    ".Lsq%=:\n\ts_mov_b32 m0, 63\n"
                    ".Lsp%=:\n\tglobal_load_dword %0, %2, %3 offset:%6 sc1\n\ts_waitcnt vmcnt(0)\n\tv_cmp_ne_u32 vcc, %4, %0\n\ts_cbranch_vccz .Lok%=\n\t"
                    SLP "s_sub_u32 m0, m0, 1\n\ts_cbranch_scc0 .Lsp%=\n\t" TIME_UP("%1", ".Lok%=") "s_branch .Lsq%=\n\t")
               : "+v"(v) : "s"(deadline), "v"(voff), "s"(s), "s"(tag), "n"(N), "n"(OFF) : "memory", "vcc", "scc");
}
__device__ __forceinline__ void poll(const void* s, unsigned voff, unsigned tag, unsigned deadline) {
  unsigned v; gld4<0>(v, s, voff); check_flags<0>(v, s, voff, tag, deadline);
}
__device__ __forceinline__ unsigned make_tag(unsigned nonce) { return nonce * 2654435761u + 0x7F4A7C15u; }
struct Red { u32x4 pp[6], xp; unsigned i2, fw; const char *rp, *rx, *ri, *rf, *f2, *ob, *sr; };
#define RED_M(D, S, P, C) "v_mfma_f32_16x16x32_bf16 " D ", " S ", " P ", " C "\n\t"
__device__ __forceinline__ void reduce_ask(Red& u, unsigned l16, unsigned iv, unsigned pv, unsigned vt, unsigned tag, unsigned deadline, int step) {
  asm volatile("s_cmp_lt_u32 %20, 4\n\ts_cbranch_scc1 .Ls%=\n"
               COLD(".Ls%=", ".Lz%=", "s_cmp_eq_u32 %20, 1\n\ts_cbranch_scc1 .Lc%=\n\ts_cmp_eq_u32 %20, 2\n\ts_cbranch_scc1 .Lz%=\n\ts_cmp_eq_u32 %20, 3\n\ts_cbranch_scc1 .Lp%=\n\t"
               "global_load_dword %8, %16, %12 sc1\n\ts_branch .Lz%=\n"
               ".Lp%=:\n\ts_mov_b64 exec, 1\n\tglobal_store_dword %14, %17, %13 sc1\n\ts_mov_b64 exec, -1\n\ts_branch .Lz%=\n"
               ".Lc%=:\n\t"
               "s_mov_b32 m0, 63\n.Ld%=:\n\tv_cmp_ne_u32 vcc, %18, %8\n\ts_cbranch_vccz .Lr%=\n\t"
               "global_load_dword %8, %16, %12 sc1\n\ts_sub_u32 m0, m0, 1\n\ts_waitcnt vmcnt(0)\n\ts_cbranch_scc0 .Ld%=\n\t" TIME_UP("%19", ".Lr%=") "s_branch .Lc%=\n"
               ".Lr%=:\n\tglobal_load_dwordx4 %0, %14, %9 offset:-3072 sc1\n\tglobal_load_dwordx4 %1, %14, %9 offset:-2048 sc1\n\t"
               "global_load_dwordx4 %2, %14, %9 offset:-1024 sc1\n\tglobal_load_dwordx4 %3, %14, %9 sc1\n\tglobal_load_dwordx4 %4, %14, %9 offset:1024 sc1\n\t"
               "global_load_dwordx4 %5, %14, %9 offset:2048 sc1\n\tglobal_load_dwordx4 %6, %14, %10 sc1\n\tglobal_load_dword %7, %15, %11 sc1\n\t")
               : "+v"(u.pp[0]), "+v"(u.pp[1]), "+v"(u.pp[2]), "+v"(u.pp[3]), "+v"(u.pp[4]), "+v"(u.pp[5]), "+v"(u.xp), "+v"(u.i2), "+v"(u.fw)
               : "s"(u.rp), "s"(u.rx), "s"(u.ri), "s"(u.rf), "s"(u.f2), "v"(l16), "v"(iv), "v"(pv), "v"(vt), "s"(tag), "s"(deadline), "s"(step) : "memory", "vcc", "scc");
}
__device__ __forceinline__ void reduce_sum(const Red& u, const u32x4& s0, const u32x4& s1, f32x4& v0, f32x4& v1, float (&x)[8]) {
  asm volatile(RED_M("%0", "%10", "%12", "0") RED_M("%1", "%11", "%12", "0") "v_lshlrev_b32 %2, 16, %18\n\tv_and_b32 %3, 0xffff0000, %18\n\t"
               RED_M("%0", "%10", "%13", "%0") RED_M("%1", "%11", "%13", "%1") "v_lshlrev_b32 %4, 16, %19\n\tv_and_b32 %5, 0xffff0000, %19\n\t"
               RED_M("%0", "%10", "%14", "%0") RED_M("%1", "%11", "%14", "%1") "v_lshlrev_b32 %6, 16, %20\n\tv_and_b32 %7, 0xffff0000, %20\n\t"
               RED_M("%0", "%10", "%15", "%0") RED_M("%1", "%11", "%15", "%1") "v_lshlrev_b32 %8, 16, %21\n\tv_and_b32 %9, 0xffff0000, %21\n\t"
               RED_M("%0", "%10", "%16", "%0") RED_M("%1", "%11", "%16", "%1") RED_M("%0", "%10", "%17", "%0") RED_M("%1", "%11", "%17", "%1")
               : "=&v"(v0), "=&v"(v1), "=&v"(x[0]), "=&v"(x[1]), "=&v"(x[2]), "=&v"(x[3]), "=&v"(x[4]), "=&v"(x[5]), "=&v"(x[6]), "=&v"(x[7])
               : "v"(s0), "v"(s1), "v"(u.pp[0]), "v"(u.pp[1]), "v"(u.pp[2]), "v"(u.pp[3]), "v"(u.pp[4]), "v"(u.pp[5]), "v"(u.xp[0]), "v"(u.xp[1]), "v"(u.xp[2]), "v"(u.xp[3]));
}
__device__ __forceinline__ void wait_store(int step) {
  asm volatile("s_cmp_lt_u32 %0, 4\n\ts_cbranch_scc1 .La%=\n\ts_waitcnt vmcnt(0)\n"
               COLD(".La%=", ".Lb%=", "s_waitcnt vmcnt(8)\n\ts_cmp_eq_u32 %0, 1\n\ts_cbranch_scc1 .Lb%=\n\ts_waitcnt vmcnt(2)\n\ts_cmp_eq_u32 %0, 2\n\ts_cbranch_scc1 .Lb%=\n\ts_waitcnt vmcnt(1)\n\t") :: "s"(step) : "memory", "scc");
}

template <bool Z> __device__ __forceinline__ void mf(f32x4& c, const bf16x8& a, const bf16x8& b) {
  if (Z) asm volatile("v_mfma_f32_16x16x32_bf16 %0, %1, %2, 0" : "=&v"(c) : "v"(a), "v"(b));
  else asm volatile("v_mfma_f32_16x16x32_bf16 %0, %1, %2, %0" : "+v"(c) : "v"(a), "v"(b));
}
__device__ __forceinline__ void mfc(f32x4& d, const bf16x8& a, const bf16x8& b, const f32x4& c) {
  asm volatile("v_mfma_f32_16x16x32_bf16 %0, %1, %2, %3" : "=&v"(d) : "v"(a), "v"(b), "v"(c));
}
template <int OFF, class T> __device__ __forceinline__ void lds_rd(T& d, unsigned a) { asm volatile("ds_read_b128 %0, %1 offset:%2" : "=v"(d) : "v"(a), "n"(OFF)); }
template <int OFF, class T> __device__ __forceinline__ void lds_rd8(T& d, unsigned a) { asm volatile("ds_read_b64 %0, %1 offset:%2" : "=v"(d) : "v"(a), "n"(OFF)); }
template <int OFF, class T> __device__ __forceinline__ void lds_wr(unsigned a, const T& d) { asm volatile("ds_write_b128 %0, %1 offset:%2" :: "v"(a), "v"(d), "n"(OFF) : "memory"); }
template <int OFF, class T> __device__ __forceinline__ void lds_wr8(unsigned a, const T& d) { asm volatile("ds_write_b64 %0, %1 offset:%2" :: "v"(a), "v"(d), "n"(OFF) : "memory"); }
template <int NK, int PD, int G> struct Ring { bf16x8 q[NK]; };
template <int O, int NK, int PD, int G> __device__ __forceinline__ void prime(Ring<NK, PD, G>& b, unsigned la) {
  static_for<0, PD>([&](auto kc) { lds_rd<O + decltype(kc)::value * 1024>(b.q[decltype(kc)::value], la); });
}
template <int O, int BAR, int VMB, int NK, int PD, int G, class F, class H> __device__ __forceinline__ void stream(Ring<NK, PD, G>& b, unsigned la, F&& f, H&& hook) {
  constexpr int NT = 36 / NK;
  static_assert((BAR < 0 || BAR + PD >= NK) && NK % G == 0, "barrier before the last read");
  if constexpr (BAR >= 0) prio<PR_B>();
  static_for<0, 36>([&](auto sc) {
    constexpr int s = decltype(sc)::value, c = s / NT, grp = s / (G * NT), in = s % (G * NT);
    constexpr int k = grp * G + in % G, t = (grp & 1) ? NT - 1 - in / G : in / G;
    constexpr int KZ = (BAR >= 0) ? BAR : NK - PD + 2;
    if constexpr (s == BAR * NT - 1) asm volatile("s_setprio 3\n\ts_waitcnt vmcnt(%0) lgkmcnt(0)" :: "n"(VMB) : "memory");
    if constexpr (s == BAR * NT) { asm volatile("" ::: "memory"); __builtin_amdgcn_s_barrier(); asm volatile("" ::: "memory"); }
    if constexpr (BAR < 0 && s == KZ * NT) wait_frag<0>();
    if constexpr (c < KZ && in < G) wait_frag<imin(NK - 1, PD - 1 + (s + NT - 1) / NT) - k>();
    f(IC<k>{}, IC<t>{}, b.q[k]);
    if constexpr (s % NT == 0 && c + PD < NK) lds_rd<O + (c + PD) * 1024>(b.q[c + PD], la);
    if constexpr (in == G * NT - 1) static_for<grp * G, grp * G + G>([&](auto kc) { f(kc, IC<NT>{}, b.q[decltype(kc)::value]); });
    if constexpr (s % NT == NT - 1) hook(IC<c>{});
  });
}
template <class F> __device__ __forceinline__ void periods(F&& f) {
#pragma nounroll
  for (int it = 0; it < 16; ++it) static_for<0, 4>([&](auto jc) { f(jc, it); });
}

__device__ __forceinline__ void max4(float (&s)[4], const f32x4& a, const f32x4& b) {
  asm volatile("v_max_f32 %0, %4, %8\n\tv_max_f32 %1, %5, %9\n\tv_max_f32 %2, %6, %10\n\tv_max_f32 %3, %7, %11" : "=&v"(s[0]), "=&v"(s[1]), "=&v"(s[2]), "=&v"(s[3])
               : "v"(a[0]), "v"(a[1]), "v"(a[2]), "v"(a[3]), "v"(b[0]), "v"(b[1]), "v"(b[2]), "v"(b[3]));
}
struct Soft { float s[4], t0, t1; unsigned mo[2]; };
__device__ __forceinline__ void soft1(Soft& f, float sc) {
  asm volatile("v_max3_f32 %4, %0, %1, %2\n\tv_max_f32 %5, %4, %3\n\tv_max_f32 %4, %4, %3\n\ts_nop 1\n\tv_permlane16_swap_b32 %4, %5\n\t"
               "v_max_f32 %4, %4, %5\n\tv_mov_b32 %5, %4\n\ts_nop 1\n\tv_permlane32_swap_b32 %4, %5\n\tv_max_f32 %4, %4, %5\n\tv_mul_f32 %4, %4, %6\n\t"
               "v_fma_f32 %0, %0, %6, -%4\n\tv_fma_f32 %1, %1, %6, -%4\n\tv_fma_f32 %2, %2, %6, -%4\n\tv_fma_f32 %3, %3, %6, -%4"
               : "+v"(f.s[0]), "+v"(f.s[1]), "+v"(f.s[2]), "+v"(f.s[3]), "=&v"(f.t0), "=&v"(f.t1) : "v"(sc));
}
#define SOFT2(R0, R1, R2, R3, R4, R5, R6, R7, PAD) \
  "v_exp_f32 %0, %0\n\t" R0 "v_exp_f32 %1, %1\n\t" R1 "v_exp_f32 %2, %2\n\t" R2 "v_exp_f32 %3, %3\n\t" R3 \
  "v_add_f32 %4, %0, %1\n\tv_add_f32 %5, %2, %3\n\t" R4 "v_add_f32 %4, %4, %5\n\tv_mov_b32 %5, %4\n\t" R5 PAD \
  "v_permlane16_swap_b32 %4, %5\n\t" R6 "v_add_f32 %4, %4, %5\n\tv_mov_b32 %5, %4\n\t" R7 PAD \
  "v_permlane32_swap_b32 %4, %5\n\tv_add_f32 %4, %4, %5\n\tv_rcp_f32 %4, %4"
#define SOFT_RD(N, K) "ds_read_b128 %" #N ", %14 offset:%15+" #K "\n\t"
template <int OFF> __device__ __forceinline__ void soft2(Soft& f, bf16x8 (&x)[18], unsigned la) {
  if constexpr (OFF < 0)
    asm volatile(SOFT2("", "", "", "", "", "", "", "", "s_nop 1\n\t") : "+v"(f.s[0]), "+v"(f.s[1]), "+v"(f.s[2]), "+v"(f.s[3]), "=&v"(f.t0), "=&v"(f.t1));
  else
    asm volatile(SOFT2(SOFT_RD(6, 0), SOFT_RD(7, 1024), SOFT_RD(8, 2048), SOFT_RD(9, 3072), SOFT_RD(10, 4096), SOFT_RD(11, 5120), SOFT_RD(12, 6144), SOFT_RD(13, 7168), "s_nop 0\n\t")
                 : "+v"(f.s[0]), "+v"(f.s[1]), "+v"(f.s[2]), "+v"(f.s[3]), "=&v"(f.t0), "=&v"(f.t1),
                   "=&v"(x[0]), "=&v"(x[1]), "=&v"(x[2]), "=&v"(x[3]), "=&v"(x[4]), "=&v"(x[5]), "=&v"(x[6]), "=&v"(x[7]) : "v"(la), "n"(OFF < 0 ? 0 : OFF));
}
#define QK_M(D, W, X, C) "v_mfma_f32_16x16x32_bf16 " D ", " W ", " X ", " C "\n\t"
#define QK_G3 "v_mul_f32 %4, %4, %8\n\t"
#define QK_G4 "v_mul_f32 %5, %5, %8\n\t"
#define QK_G5 "v_mul_f32 %6, %6, %8\n\t"
#define QK_G6 "v_mul_f32 %7, %7, %8\n\t"
#define QK_G7 "v_cvt_pk_bf16_f32 %8, %4, %5\n\t"
#define QK_G8 "v_cvt_pk_bf16_f32 %9, %6, %7\n\t"
#define QK_G9 "v_and_b32 %4, %8, %17\n\t"
#define QK_GA "v_and_b32 %5, %9, %18\n\t"
#define QK_GB "v_xor_b32 %6, %8, %4\n\t"
#define QK_GC "v_xor_b32 %7, %9, %5\n\t"
#define QK_OUT(A, B, C, D) "+v"(f.s[A]), "+v"(f.s[B]), "+v"(f.s[C]), "+v"(f.s[D]), "+v"(f.t0), "+v"(f.t1)
#define QK_PAIR(C0, C1, GA, GB, S) asm volatile(QK_M("%0", "%10", "%14", C0) "ds_read_b128 %2, %16 offset:%19\n\t" QK_M("%1", "%11", "%14", C1) GA \
    QK_M("%1", "%13", "%15", "%1") "ds_read_b128 %3, %16 offset:%20\n\t" QK_M("%0", "%12", "%15", "%0") GB \
    : "+v"(a0), "+v"(a1), "=&v"(y0), "=&v"(y1), QK_OUT(S, S + 1, 2 - S, 3 - S) \
    : "v"(w0[0]), "v"(w0[1]), "v"(w1[0]), "v"(w1[1]), "v"(x0), "v"(x1), "v"(la), "v"(f.mo[0]), "v"(f.mo[1]), "n"(OFF), "n"(OFF + 1024))
template <int P, int E, int OFF> __device__ __forceinline__ void qk_pair(f32x4& a0, f32x4& a1, const bf16x8 (&w0)[2], const bf16x8 (&w1)[2], bf16x8 x0, bf16x8 x1,
                                                                         bf16x8& y0, bf16x8& y1, unsigned la, Soft& f) {
  if constexpr (P == 0) QK_PAIR("0", "0", QK_G3, QK_G4, 0);
  if constexpr (P == 1) QK_PAIR("%0", "%1", QK_G5, QK_G6, 0);
  if constexpr (P == 2) QK_PAIR("%0", "%1", QK_G7, QK_G8, 0);
  if constexpr (P == 3) QK_PAIR("%0", "%1", QK_G9, QK_GA, 2 * E);
  if constexpr (P == 4) QK_PAIR("%0", "%1", QK_GB, QK_GC, 2 * E);
}
__device__ __forceinline__ void soft3(Soft& f) {
  int u = 0;
  asm volatile("s_nop 0\n\t" QK_G3 QK_G4 QK_G5 QK_G6 QK_G7 QK_G8 QK_G9 QK_GA QK_GB QK_GC : "+s"(u), "+s"(u), "+s"(u), "+s"(u), QK_OUT(0, 1, 2, 3)
               : "n"(0), "n"(0), "n"(0), "n"(0), "n"(0), "n"(0), "n"(0), "v"(f.mo[0]), "v"(f.mo[1]));
}
template <int J> __device__ __forceinline__ void mf8(f32x4& a0, f32x4& a1, const bf16x8 (&w0)[2], const bf16x8 (&w1)[2], const bf16x8 (&w2)[2], const bf16x8 (&w3)[2],
                                                     const bf16x8& x0, const bf16x8& x1, const bf16x8& x2, const bf16x8& x3, f32x2& st, unsigned& xk, unsigned& xq,
                                                     unsigned sa, unsigned ka, unsigned qa) {
  asm volatile(QK_M("%0", "%5", "%13", "%0") "ds_read_b64 %2, %17 offset:%20\n\tds_read_b32 %3, %18 offset:%21\n\tds_read_b32 %4, %19 offset:%21\n\t"
               QK_M("%1", "%6", "%13", "%1") QK_M("%1", "%8", "%14", "%1") QK_M("%1", "%10", "%15", "%1") QK_M("%1", "%12", "%16", "%1")
               QK_M("%0", "%7", "%14", "%0") QK_M("%0", "%9", "%15", "%0") QK_M("%0", "%11", "%16", "%0")
               : "+v"(a0), "+v"(a1), "=&v"(st), "=&v"(xk), "=&v"(xq)
               : "v"(w0[0]), "v"(w0[1]), "v"(w1[0]), "v"(w1[1]), "v"(w2[0]), "v"(w2[1]), "v"(w3[0]), "v"(w3[1]), "v"(x0), "v"(x1), "v"(x2), "v"(x3),
                 "v"(sa), "v"(ka), "v"(qa), "n"(J * 128), "n"((J & 1) * 1024));
}

constexpr unsigned SPIN = 400000000u;
struct Ctx { char* ws; int g, wave, lane; unsigned dl; const char *fl, *fp, *code; unsigned long long w0; };
constexpr unsigned long long HALF = 0x0000FFFF0000FFFFull, DIAG = 0xF0000F0000F0000Full;

__device__ __forceinline__ unsigned get_tag(const char* ws) {
  unsigned nonce;
  asm volatile("s_load_dword %0, %1, 0x0\n\ts_waitcnt lgkmcnt(0)" : "=s"(nonce) : "s"(ws) : "memory");
  return make_tag(nonce);
}
template <int R, int NP, int NX = 0, class I, class Q, class P> __device__ __forceinline__ void load_w(char* smem, const Ctx& cx, unsigned& tag, I&& issue, Q&& rd, P&& then) {
  const int wave = cx.wave;
  static_for<0, 3>([&](auto rc) { issue(rc); });
  tag = get_tag(cx.ws);
  static_for<0, R>([&](auto rc) {
    constexpr int r = decltype(rc)::value;
    wait_vm<imin(2, R - 1 - r) * NP + (r >= R - 2 ? NX : 0)>();
    bar_bare();
    rd(rc);
    if constexpr (r + 3 < R || r + 1 == R) bar_raw();
    if constexpr (r + 3 < R) issue(IC<r + 3>{});
    if constexpr (r + 3 == R && NX == 0) { if (wave == ((blockIdx.x >> 4) & 7)) touch(cx.code, lds_addr(smem) + 162816); }
    then(rc);
  });
}
__device__ __forceinline__ bf16x8 asb(u32x4 v) { return __builtin_bit_cast(bf16x8, v); }
__device__ __forceinline__ bf16x8 ones8() { return asb(u32x4{0x3F803F80u, 0x3F803F80u, 0x3F803F80u, 0x3F803F80u}); }
__device__ __forceinline__ u32x4 widen(unsigned w0, unsigned w1) {
  unsigned n0, n1;
  asm volatile("s_nop 1\n\tv_permlane16_swap_b32 %0, %2\n\tv_permlane16_swap_b32 %1, %3" : "+v"(w0), "+v"(w1), "=&v"(n0), "=&v"(n1));
  return u32x4{w0, w1, n0, n1};
}
__device__ __forceinline__ u32x4 pack8(const f32x4& a, const f32x4& b) { return u32x4{cvt2(a[0], a[1]), cvt2(a[2], a[3]), cvt2(b[0], b[1]), cvt2(b[2], b[3])}; }
__device__ __forceinline__ f32x2 row_stat(float sum, float sq) {
  const float mean = sum * (1.0f / 576.0f);
  const float var = fmaxf(sq * (1.0f / 576.0f) - mean * mean, 0.0f);
  return f32x2{-rbf(mean), __builtin_amdgcn_rcpf(rbf(__builtin_amdgcn_sqrtf(rbf(rbf(var) + 1e-5f))))};
}
__device__ __forceinline__ float diag(const f32x4& a, int p) { return (p == 0) ? a[0] : (p == 1) ? a[1] : (p == 2) ? a[2] : a[3]; }
__device__ __forceinline__ float sum_g(float v) {
  const auto a = __builtin_amdgcn_permlane16_swap(f2i(v), f2i(v), false, false);
  const float h = u2f(a[0]) + u2f(a[1]);
  const auto b = __builtin_amdgcn_permlane32_swap(f2i(h), f2i(h), false, false);
  return u2f(b[0]) + u2f(b[1]);
}
__device__ __forceinline__ int top(int g) { return (1 - (g >> 1)) * 32; }

__device__ __forceinline__ void role_gu(char* smem, const u16* wg, const u16* wu, u16* out, const Ctx& cx, int c) {
  const int wave = cx.wave, lane = cx.lane;
  const unsigned sm0 = lds_addr(smem);
  unsigned tag;
  const int r = lane & 15, g = lane >> 4, q = r >> 2, p = lane & 3;
  constexpr int SUMS = 4 * TB;
  bf16x8 W[18][2];
  u32x4 sel0, sel1;
#pragma unroll
  for (int e = 0; e < 2; ++e) {
    sel0[e] = sel1[2 + e] = (r == 4 * g + 2 * e ? 0x3F80u : 0u) | (r == 4 * g + 2 * e + 1 ? 0x3F800000u : 0u);
    sel0[2 + e] = sel1[e] = 0u;
  }
  tie(sel0, sel1);
  {
    f32x4 cs[2] = {{0.f, 0.f, 0.f, 0.f}, {0.f, 0.f, 0.f, 0.f}};
    constexpr int BUF = 48 * 1056;
    const char* src = (const char*)((wave >> 2) ? wu : wg) + c * 256;
    const unsigned vo = (4 * (g & 1) + 16 * (g >> 1)) * (FD * 2) + r * 16;
    const char* rb = smem + ((g & 1) * 4 + q) * 1056 + (g >> 1) * 512 + wave * 32 + p * 8;
    load_w<6, 6>(smem, cx, tag, [&](auto rc) {
      constexpr int rr = decltype(rc)::value;
#pragma unroll
      for (int j = 0; j < 6; ++j) {
        const int idx = 6 * (wave & 3) + j, id = idx & 7;
        dma_w2(src + (size_t)(32 * (3 * rr + (idx >> 3)) + 8 * (id >> 2) + (id & 3)) * (FD * 2), vo, sm0 + (rr % 3) * BUF + ((wave >> 2) * 24 + idx) * 1056);
      }
    }, [&](auto rc) {
      constexpr int rr = decltype(rc)::value;
#pragma unroll
      for (int kl = 0; kl < 3; ++kl)
#pragma unroll
        for (int m = 0; m < 2; ++m) { const char* a = rb + (rr % 3) * BUF + (m * 24 + kl * 8) * 1056; W[3 * rr + kl][m] = tr_frag(a, a + 256); }
      tie(W[3 * rr][0], W[3 * rr][1], W[3 * rr + 1][0], W[3 * rr + 1][1], W[3 * rr + 2][0], W[3 * rr + 2][1]);
    }, [&](auto rc) {
#pragma unroll
      for (int k = 0; k < 6; ++k) cs[k & 1] = MFMA(ones8(), W[3 * decltype(rc)::value + (k >> 1)][k & 1], cs[k & 1]);
      tie(cs[0], cs[1]);
    });
#pragma unroll
    for (int m = 0; m < 2; ++m) {
      const float cm = cs[m][0] * (-1.0f / 576.0f);
      const f32x4 c4 = {cm, cm, cm, cm};
#pragma unroll
      for (int k = 0; k < 18; ++k) { W[k][m] = asb(pack8(MFMA(asb(sel0), W[k][m], c4), MFMA(asb(sel1), W[k][m], c4))); tie(W[k][m]); }
    }
  }
  const char* hb = cx.ws + WS_H + (size_t)(cx.g * 6 + c / 6) * 64 * HB + (c % 6) * 4096 + wave * 512 - HB;
  const unsigned l16 = vopq(lane * 16), hv = vopq((g >> 1) * 256 + r * 16);
  const unsigned la[2] = {vopq(sm0 + l16), vopq(sm0 + l16 + 2 * TB)};
  f32x4 ag, au;
  Ring<18, PDG, 2> bq;
  auto mm = [&](auto kc, auto jc, const bf16x8& x) {
    constexpr int k = decltype(kc)::value;
    if constexpr (decltype(jc)::value == 0) mf<k == 0>(ag, W[k][0], x);
    if constexpr (decltype(jc)::value == 1) mf<k == 0>(au, W[k][1], x);
  };
  auto epi = [&]() {
    tie(ag, au);
    return widen(cvt2(ag[0] * au[0], ag[1] * au[1]), cvt2(ag[2] * au[2], ag[3] * au[3]));
  };
  if (wave < 4) {
    const char* xs = cx.ws + WS_X1 + (size_t)(cx.g * 64 + (wave >> 1)) * TB + (wave & 1) * 9216;
    const char* fr = cx.fl + TLAG * 128;
    const unsigned pv = vopq((F_X1 + (lane & 1)) * FSTR), mb = sm0 + (wave & 1) * 9216;
    const unsigned lm[3] = {l16, vopq(l16 + 3072), vopq(l16 + 6144)};
    auto fetch = [&](auto sc) { static_for<0, 3>([&](auto mc) { dma<decltype(sc)::value * TB + decltype(mc)::value * 3072, 3>(xs, lm[decltype(mc)::value], mb); }); };
    int tgt = (wave == 1 && c < 32) ? c + 2 : 99;
    auto scale = [&]() {
      const f32x2 t = *(const f32x2*)(smem + SUMS + lane * 8);
      const float is = row_stat(sum_g(t[0]), sum_g(t[1]))[1];
      gst4m<-1>(cx.ws + WS_IS + (cx.g * 64 + tgt - 2) * 128, lane * 4, f2i(is * is));
      tgt += 32;
    };
    auto loop = [&](auto oc) {
      constexpr int ODD = decltype(oc)::value;
      unsigned dl = cx.dl;
      poll(fr + ODD * 128, pv, tag, dl); fetch(oc);
      if constexpr (!ODD) wait_vm<0>();
      adv(fr, 256);
      prio<PR_A>();
      bar_bare();
      periods([&](auto jc, int it) {
        constexpr int j = decltype(jc)::value;
        constexpr bool MINE = (j & 1) == ODD;
        unsigned fw;
        prime<(j & 1) * TB>(bq, la[j >> 1]);
        if constexpr (MINE) gld4<j * 128>(fw, fr, pv);
        stream<(j & 1) * TB, -1, 0>(bq, la[j >> 1], mm, [&](auto kc) {
          constexpr int k = decltype(kc)::value;
          if constexpr (MINE && k == 11) adv(xs, 2 * TB);
          if constexpr (k == 12 && j == 2) dl = sopq((it == 15) ? 0u : dl);
          if constexpr (k == 13) adv(hb, HB);
        });
        prio<3>();
        if constexpr (MINE) { check_flags<0, j * 128>(fw, fr, pv, tag, dl); fetch(IC<(j + 2) & 3>{}); }
        else settle();
        gst16m(hb, hv, epi(), HALF);
        if constexpr (j == 3) adv(fr, 512);
        if constexpr (!MINE) wait_vm<1>();
        if constexpr (!ODD) { if (__builtin_expect(4 * it + j == tgt, 0)) scale(); }
        prio<PR_A>();
        bar_bare();
      });
    };
    if (wave < 2) loop(IC<0>{}); else loop(IC<1>{});
    if (c == 31) bar_bare();
    if (tgt < 66) scale();
    wait_vm<0>();
    bar_bare();
  } else {
    const int kb = 2 * (c % 9) + (wave & 1);
    int tau = 2 * (c / 9) + ((wave >> 1) & 1);
    Red u = {};
    const unsigned vt = vopq(tag), iv = vopq(r * 4), pq = vopq((F_P + (kb / 6) * 6 + min(lane, 5)) * FSTR), vrow = vopq(r * 2304 + g * 16);
    u.rp = cx.ws + WS_P + (size_t)((cx.g * 18 + kb) * 64 + tau) * PB + 3072;
    u.rx = cx.ws + WS_X1 + (size_t)(cx.g * 64 + tau) * TB + kb * 1024;
    u.ri = cx.ws + WS_IS + (cx.g * 64 + tau) * 128;
    u.rf = cx.fl + (tau + PLAG) * 128;
    u.f2 = cx.ws + WS_F2 + ((cx.g & 1) * 72 + top(cx.g) + tau - 8) * F2ROW + kb * 128;
    u.ob = cx.ws + WS_X2 + (size_t)((cx.g & 1) * 64 + top(cx.g) + tau) * TB + kb * 1024;
    u.sr = cx.ws + WS_SR + (128 + (cx.g & 1) * 64 + top(cx.g) + tau) * SRB + kb * 128;
    const char* ob2 = (const char*)out + (size_t)((cx.g >> 1) * 1024 + 32 * (31 - tau) + (cx.g & 1)) * (DM * 2) + kb * 64;
    gst16m(u.f2 + 8 * F2ROW, (lane >> 3) * (8 * F2ROW) + (lane & 7) * 16, u32x4{~tag, ~tag, ~tag, ~tag}, 0xFFFFFFFFull);
    auto ask = [&](int step) { reduce_ask(u, l16, iv, pq, vt, tag, cx.dl, step); };
    auto finish = [&](int tau_) {
      f32x4 v0, v1; float x[8];
      reduce_sum(u, sel0, sel1, v0, v1, x);
      adv(u.rp, 0); adv(u.rx, 0); adv(u.ri, 0); adv(u.rf, 0); adv(u.f2, 0); adv(u.ob, 0); adv(u.sr, 0);
      const int tau = sopq(tau_);
      const char *o = u.ob, *so = u.sr;
      const unsigned ov = vopq((tau >= 32) ? vrow : l16);
      adv(u.rp, 8 * PB); adv(u.rx, 8 * TB); adv(u.ri, 8 * 128); adv(u.rf, 8 * 128);
      adv(u.f2, (tau < 32) ? 8 * F2ROW : 0);
      u.ob = ((tau >> 3) == 3) ? ob2 : u.ob + ((tau < 32) ? 8l * TB : -8l * 32 * DM * 2); adv(u.ob, 0);
      u.sr = (tau < 24) ? u.sr + 8 * SRB : cx.ws + WS_SR + (256 + cx.g) * SRB + kb * 128; adv(u.sr, 0);
      tie(v0, v1);
      const float sc = u2f(u.i2);
      const u32x4 y = {cvt2(fmaf(v0[0], sc, x[0]), fmaf(v0[1], sc, x[1])), cvt2(fmaf(v0[2], sc, x[2]), fmaf(v0[3], sc, x[3])),
                       cvt2(fmaf(v1[0], sc, x[4]), fmaf(v1[1], sc, x[5])), cvt2(fmaf(v1[2], sc, x[6]), fmaf(v1[3], sc, x[7]))};
      gst16(o, ov, y);
      const f32x4 z = {0.f, 0.f, 0.f, 0.f};
      gst8m(so, iv * 2, f32x2{MFMA(ones8(), asb(y), z)[0], diag(MFMA(asb(y), asb(y), z), p)}, DIAG);
    };
    int step = -1, tgt = (c < 32) ? c : 99;
    const char* fp = cx.fp - 512;
    bar_bare();
    prime<0>(bq, la[0]);
    wait_lg<0>();
    periods([&](auto jc, int it) {
      constexpr int j = decltype(jc)::value, j1 = (j + 1) & 3;
      stream<(j & 1) * TB, BARG, 63>(bq, la[j >> 1], mm, [&](auto kc) {
        constexpr int k = decltype(kc)::value;
        if constexpr (k == 9 && j == 0) adv(fp, 512);
        if constexpr (k == 10) adv(hb, HB);
        if constexpr (k == BARG - 2) wait_store(step);
      });
      wait_vm<0>();
      flag_by<(j + 1) * 128>(cx.w0, fp, l16, vt);
      asm volatile("s_nop 3");
      u32x4 h = epi();
      gst16m(hb, hv, h, HALF);
      step = 4 * it + j - LAGF - (tau | 1);
      ask(step);
      if (__builtin_expect(step == 2, 0)) finish(tau);
      tau += (step == 3) ? 8 : 0;
      if (__builtin_expect(4 * it + j == tgt, 0)) {
        f32x4 s1 = {0.f, 0.f, 0.f, 0.f}, s2 = s1;
        bf16x8 x[5];
#pragma unroll
        for (int k = 0; k < 5; ++k) x[k] = *(const bf16x8*)(smem + j * TB + (k < 4 ? 4 * (wave - 4) + k : 16 + (wave & 1)) * 1024 + lane * 16);
        if (wave >= 6) x[4] = bf16x8{0, 0, 0, 0, 0, 0, 0, 0};
#pragma unroll
        for (int k = 0; k < 5; ++k) { s1 = MFMA(ones8(), x[k], s1); s2 = MFMA(x[k], x[k], s2); }
        if (g == q) *(f32x2*)(smem + SUMS + (wave - 4) * 128 + r * 8) = f32x2{s1[0], diag(s2, p)};
        tgt += 32;
      }
      prio<0>();
      prime<(j1 & 1) * TB>(bq, la[j1 >> 1]);
      if constexpr (j == 3) wait_lg<0>();
    });
    if (c == 31) bar_bare();
    wait_vm<0>();
    bar_bare();
    flag_by<640>(cx.w0, fp, l16, vt);
    if (step == 2) tau += 8;
#pragma nounroll
    while (tau < 64) {
      ask(0); wait_vm<0>(); ask(1); wait_vm<0>(); ask(2);
      finish(tau); tau += 8;
    }
  }
}

__device__ __forceinline__ void role_dn(char* smem, const u16* wd, const Ctx& cx, int ks, int ns) {
  const int wave = cx.wave, lane = cx.lane;
  const unsigned sm0 = lds_addr(smem);
  unsigned tag;
  const int r = lane & 15, g = lane >> 4, q = r >> 2, p = lane & 3;
  const int kq = wave >> 2, nq = wave & 3;
  constexpr int EX = 4 * HB;
  bf16x8 W[12][3];
  {
    constexpr int BUF = 49152;
    const char* src = (const char*)wd + (size_t)(768 * ks) * (DM * 2) + ns * 384;
    unsigned vo[6];
#pragma unroll
    for (int j = 0; j < 6; ++j) {
      const unsigned ch = (wave * 6 + j) * 64 + lane, row = ch / 24;
      vo[j] = ((row >> 6) * 384 + (row & 63)) * (DM * 2) + (ch - row * 24) * 16;
    }
    const char* rb = smem + (kq * 64 + 8 * g + q) * 384;
    load_w<6, 6>(smem, cx, tag, [&](auto rc) {
      constexpr int rr = decltype(rc)::value;
#pragma unroll
      for (int j = 0; j < 6; ++j) dma_w2(src + rr * 64 * (DM * 2), vo[j], sm0 + (rr % 3) * BUF + (wave * 6 + j) * 1024);
    }, [&](auto rc) {
      constexpr int rr = decltype(rc)::value;
#pragma unroll
      for (int kl = 0; kl < 2; ++kl)
#pragma unroll
        for (int m = 0; m < 3; ++m) {
          const char* a = rb + (rr % 3) * BUF + kl * 32 * 384 + ((m < 2) ? nq * 64 + p * 16 + m * 8 : 256 + nq * 32 + p * 8);
          W[2 * rr + kl][m] = tr_frag(a, a + 4 * 384);
        }
      tie(W[2 * rr][0], W[2 * rr][1], W[2 * rr][2], W[2 * rr + 1][0], W[2 * rr + 1][1], W[2 * rr + 1][2]);
    }, [](auto) {});
  }
  const unsigned l16 = vopq(lane * 16);
  const unsigned la[2] = {vopq(sm0 + l16 + kq * 12288), vopq(sm0 + l16 + kq * 12288 + 2 * HB)};
  const unsigned ea = vopq(sm0 + EX + nq * 3072 + l16);
  f32x4 acc[3];
  auto mm = [&](auto kc, auto jc, const bf16x8& x) {
    constexpr int k = decltype(kc)::value;
    if constexpr (decltype(jc)::value < 3) mf<k == 0>(acc[decltype(jc)::value % 3], W[k][decltype(jc)::value % 3], x);
  };
  if (wave < 4) {
    Ring<12, PDD, 2> bq;
    const char* hs = cx.ws + WS_H + (size_t)((cx.g * 6 + ks) * 64 + (nq >> 1)) * HB + (nq & 1) * 12288;
    const char* fr = cx.fl + TLAG * 128;
    const unsigned mb = sm0 + (nq & 1) * 12288, pv = vopq((F_H + 6 * ks + min(lane, 5)) * FSTR);
    const unsigned lm[3] = {l16, vopq(l16 + 4096), vopq(l16 + 8192)};
    auto loop = [&](auto oc) {
      constexpr int ODD = decltype(oc)::value;
      unsigned dl = cx.dl;
      poll(fr + ODD * 128, pv, tag, dl);
      static_for<0, 3>([&](auto mc) { dma<ODD * HB + decltype(mc)::value * 4096, 4>(hs, lm[decltype(mc)::value], mb); });
      if constexpr (!ODD) wait_vm<0>();
      adv(fr, 256);
      prio<PR_A>();
      bar_bare();
      periods([&](auto jc, int it) {
        constexpr int j = decltype(jc)::value;
        constexpr bool MINE = (j & 1) == ODD;
        unsigned fw;
        prime<(j & 1) * HB>(bq, la[j >> 1]);
        if constexpr (MINE) gld4<j * 128>(fw, fr, pv);
        stream<(j & 1) * HB, -1, 0>(bq, la[j >> 1], mm, [&](auto kc) {
          constexpr int k = decltype(kc)::value;
          if constexpr (MINE && k == 6) adv(hs, 2 * HB);
          if constexpr (k == 7 && j == 2) dl = sopq((it == 15) ? 0u : dl);
        });
        prio<3>();
        if constexpr (MINE) { check_flags<0, j * 128>(fw, fr, pv, tag, dl); dma<((j + 2) & 3) * HB, 4>(hs, lm[0], mb); } else settle();
        static_for<0, 3>([&](auto mc) { lds_wr<(j & 1) * 12288 + decltype(mc)::value * 1024>(ea, acc[decltype(mc)::value]); });
        if constexpr (MINE) { dma<((j + 2) & 3) * HB + 4096, 4>(hs, lm[1], mb); dma<((j + 2) & 3) * HB + 8192, 4>(hs, lm[2], mb); }
        prio<PR_A>();
        if constexpr (j == 3) adv(fr, 512);
        if constexpr (!MINE) wait_vm<0>();
        bar_raw();
      });
    };
    if (nq < 2) loop(IC<0>{}); else loop(IC<1>{});
    bar_bare();
    wait_vm<0>();
  } else {
    Ring<12, 12, 12> bq;
    const char* pd = cx.ws + WS_P + (size_t)(cx.g * 18 + 6 * ns + nq) * 64 * PB + ks * 1024 - PB;
    const unsigned hv = vopq((g >> 1) * 256 + r * 16 + (4 + (nq >> 1) - nq) * 64 * PB + (nq & 1) * 512), vt = vopq(tag);
    const char* fp = cx.fp - 512;
    bar_bare();
    prime<0>(bq, la[0]);
    wait_lg<0>();
    periods([&](auto jc, int it) {
      constexpr int j = decltype(jc)::value, j1 = (j + 1) & 3;
      f32x4 e[3];
      stream<(j & 1) * HB, BARD, (2)>(bq, la[j >> 1], mm, [&](auto kc) {
        constexpr int k = decltype(kc)::value;
        if constexpr (k == 5 && j == 0) adv(fp, 512);
        if constexpr (k == 6) adv(pd, PB);
        if constexpr (k == BARD) static_for<0, 3>([&](auto mc) { constexpr int m = decltype(mc)::value; lds_rd<(j & 1) * 12288 + m * 1024>(e[m], ea); });
      });
      flag_by<(j + 1) * 128>(cx.w0, fp, l16, vt);
      asm volatile("s_nop 3");
      wait_lg<0>();
      tie(acc[0], acc[1], acc[2], e[0], e[1], e[2]);
      gst16(pd, l16, pack8(e[0] + acc[0], e[1] + acc[1]));
      e[2] += acc[2];
      gst16m(pd, hv, widen(cvt2(e[2][0], e[2][1]), cvt2(e[2][2], e[2][3])), HALF);
      prio<0>();
      prime<(j1 & 1) * HB>(bq, la[j1 >> 1]);
      if constexpr (j == 3) wait_lg<0>();
    });
    wait_vm<0>();
    bar_bare();
    flag_by<640>(cx.w0, fp, l16, vt); flag_by<768>(cx.w0, fp, l16, vt);
  }
}

template <bool A> __device__ __forceinline__ void role_wo(char* smem, const u16* wo, const Ctx& cx, int jw) {
  const int wave = cx.wave, lane = cx.lane;
  const unsigned sm0 = lds_addr(smem);
  unsigned tag;
  const int r = lane & 15, g = lane >> 4, q = r >> 2, p = lane & 3;
  constexpr int RS = 4 * TB, RT = 10240, EX = RS + 4 * RT;
  bf16x8 W[18][2], WX[9];
  {
    constexpr int BUF = 40960;
    const char* src = (const char*)wo + jw * 576;
    unsigned vo[5], vx[5];
#pragma unroll
    for (int i = 0; i < 5; ++i) {
      const unsigned ch = min((wave * 5 + i) * 64 + lane, 2303), row = ch / 36, cc = (ch - row * 36) * 16;
      const unsigned kl = row >> 5, gg = (row >> 3) & 3, e = row & 7;
      vo[i] = (36 * kl + 20 * (e >> 2) + 4 * gg + (e & 3)) * (DM * 2) + cc;
      vx[i] = (72 * (4 * kl + gg) + 16 + 36 * (e >> 2) + (e & 3)) * (DM * 2) + cc;
    }
    const char* rb = smem + (8 * g + q) * 576 + p * 16;
    load_w<9, 5>(smem, cx, tag, [&](auto rc) {
      constexpr int rr = decltype(rc)::value;
#pragma unroll
      for (int i = 0; i < 5; ++i) dma_w(src + (rr < 8 ? rr * 72 * (DM * 2) : 0), rr < 8 ? vo[i] : vx[i], sm0 + (rr % 3) * BUF + (wave * 5 + i) * 1024);
    }, [&](auto rc) {
      constexpr int rr = decltype(rc)::value;
      bf16x8 wx[2];
#pragma unroll
      for (int kl = 0; kl < 2; ++kl) {
        const char* a = rb + (rr % 3) * BUF + kl * 32 * 576;
#pragma unroll
        for (int m = 0; m < 2; ++m) W[2 * rr + kl][m] = tr_frag(a + wave * 64 + m * 8, a + wave * 64 + m * 8 + 4 * 576);
        if constexpr (A) wx[kl] = tr_frag(a + 512 + (wave & 1) * 8, a + 512 + (wave & 1) * 8 + 4 * 576);
      }
      tie(W[2 * rr][0], W[2 * rr][1], W[2 * rr + 1][0], W[2 * rr + 1][1]);
      if constexpr (A) {
#pragma unroll
        for (int kl = 0; kl < 2; ++kl) {
          const int k = 2 * rr + kl;
          if (k < 9) WX[k % 9] = wx[kl]; else if (wave & 2) WX[k % 9] = wx[kl];
          tie(WX[k % 9]);
        }
      }
    }, [](auto) {});
  }
  const unsigned l16 = vopq(lane * 16), l3 = vopq(l16 + 3072);
  const unsigned la[2] = {vopq(sm0 + l16), vopq(sm0 + l16 + 2 * TB)};
  const unsigned ra = vopq(sm0 + RS + wave * 1024 + l16), mr = sm0 + RS + wave * 1024;
  const char* xo = cx.ws + WS_X1 + (size_t)cx.g * 64 * TB + jw * 9216 + wave * 1024 - TB;
  const char* rs = cx.ws + WS_X0 + (size_t)((cx.g & 1) * 64 + top(cx.g)) * TB + (9 * jw + wave) * 1024;
  constexpr long RSW = TB - 32l * TB - (long)(WS_X0 - WS_X2);
  poll(cx.ws + WS_F2 + ((cx.g & 1) * 72 + top(cx.g)) * F2ROW, F_X0 * 128 + (lane & 1) * F2ROW, tag, cx.dl);
  f32x4 a0, a1, ax;
  Ring<18, PDG, 1> bq;
  auto init = [&](const u32x4& x) {
    a0 = f32x4{blo(x[0]), bhi(x[0]), blo(x[1]), bhi(x[1])}; a1 = f32x4{blo(x[2]), bhi(x[2]), blo(x[3]), bhi(x[3])};
    tie(a0, a1);
  };
  const unsigned l8 = vopq(l16 + (8 - wave) * 1024), r8 = vopq(ra + 8192 + (wave & 1) * 8);
  auto init_x = [&](const u32x2& x) { ax = f32x4{blo(x[0]), bhi(x[0]), blo(x[1]), bhi(x[1])}; tie(ax); };
  dma<0, 1>(rs, l16, mr); if (wave < 2) dma<8192, 1>(rs, l8, mr);
  adv(rs, TB);
  dma<RT, 1>(rs, l16, mr); if (wave < 2) dma<RT + 8192, 1>(rs, l8, mr);
  wait_vm<0>();
  init(*(const u32x4*)(smem + RS + wave * 1024 + lane * 16));
  if constexpr (A) init_x(*(const u32x2*)(smem + RS + 8192 + (wave & 1) * 1032 + lane * 16));
  if constexpr (A) {
    const unsigned ea = vopq(sm0 + EX + wave * 1024 + l16);
    prio<PR_A>();
    bar_bare();
    auto loop = [&](auto hc) {
      constexpr int KH = decltype(hc)::value;
      auto mm = [&](auto kc, auto jc, const bf16x8& x) {
        constexpr int k = decltype(kc)::value;
        if constexpr (decltype(jc)::value == 0) mf<false>(a0, W[k][0], x);
        if constexpr (decltype(jc)::value == 1) mf<false>(a1, W[k][1], x);
        if constexpr (decltype(jc)::value == 2 && k / 9 == KH) mf<k == 9>(ax, WX[k % 9], x);
      };
      periods([&](auto jc, int it) {
        constexpr int j = decltype(jc)::value;
        u32x4 x; u32x2 x8;
        prime<(j & 1) * TB>(bq, la[j >> 1]);
        stream<(j & 1) * TB, -1, 0>(bq, la[j >> 1], mm, [&](auto kc) {
          constexpr int k = decltype(kc)::value;
          if constexpr (k == 12) adv(rs, (j == 2 && it == 7) ? RSW : TB);
          if constexpr (k == 13) adv(xo, TB);
          if constexpr (k == 16) { wait_vm<0>(); lds_rd<((j + 1) & 3) * RT>(x, ra); }
          if constexpr (KH == 0 && k == 17) lds_rd8<((j + 1) & 3) * RT>(x8, r8);
        });
        prio<3>();
        auto resid = [&]() { dma<((j + 2) & 3) * RT, 1>(rs, l16, mr); if constexpr (KH == 0) dma<((j + 2) & 3) * RT + 8192, 1>(rs, l8, mr); };
        if constexpr (!WORD) resid();
        asm volatile("s_nop %3" : "+v"(a0), "+v"(a1), "+v"(ax) : "n"(WORD ? 6 : 3) : "memory");
        gst16(xo, l16, pack8(a0, a1));
        asm volatile("s_waitcnt lgkmcnt(0)" : "+v"(x) :: "memory");
        init(x);
        prio<0>();
        lds_wr<(j & 1) * 4096>(ea, ax);
        if constexpr (WORD) resid();
        if constexpr (KH == 0) { asm volatile("s_waitcnt lgkmcnt(0)" : "+v"(x8) :: "memory"); init_x(x8); }
        prio<PR_A>();
        bar_raw();
      });
      wait_vm<0>();
      bar_bare();
    };
    if (wave < 2) loop(IC<0>{}); else loop(IC<1>{});
  } else {
    const int b = (wave == 4) ? 0 : wave - 5;
    const char* os = cx.ws + WS_O + (size_t)(cx.g * 68 + 3) * TB + b * 6144;
    const unsigned mb = sm0 + b * 6144, ea = vopq(sm0 + EX + l16), pv = vopq((F_O + (lane & 7)) * FSTR);
    auto mm = [&](auto kc, auto jc, const bf16x8& x) {
      constexpr int k = decltype(kc)::value;
      if constexpr (decltype(jc)::value == 0) mf<false>(a0, W[k][0], x);
      if constexpr (decltype(jc)::value == 1) mf<false>(a1, W[k][1], x);
    };
    auto loop = [&](auto pc) {
      constexpr bool SH = decltype(pc)::value;
      auto fetch = [&](auto sc) { dma<decltype(sc)::value * TB, 3>(os, l16, mb); dma<decltype(sc)::value * TB + 3072, 3>(os, l3, mb); };
      const char *fr = cx.fl + OLAG * 128, *fp = cx.fp - 512;
      const unsigned vt = vopq(tag);
      unsigned dl = cx.dl;
      if constexpr (!SH) { poll(fr, pv, tag, dl); fetch(IC<0>{}); wait_vm<0>(); }
      bar_bare();
      if constexpr (!SH) { poll(fr + 128, pv, tag, dl); adv(os, TB); fetch(IC<1>{}); }
      adv(fr, 256);
      unsigned fw;
      if constexpr (!SH) gld4<0>(fw, fr, pv);
      prime<0>(bq, la[0]);
      wait_lg<0>();
      periods([&](auto jc, int it) {
        constexpr int j = decltype(jc)::value, j1 = (j + 1) & 3;
        u32x4 x; f32x4 e[4];
        stream<(j & 1) * TB, BARG, (SH ? 0 : 1)>(bq, la[j >> 1], mm, [&](auto kc) {
          constexpr int k = decltype(kc)::value;
          if constexpr (!SH && k == 10 && j == 2) dl = sopq((it == 15) ? 0u : dl);
          if constexpr (SH && k == 10 && j == 0) adv(fp, 512);
          if constexpr (SH && k == BARG) gst4m<1, (j + 1) * 128>(fp, l16, vt);
          if constexpr (k == 11) adv(rs, (j == 2 && it == 7) ? RSW : TB);
          if constexpr (k == 12) adv(xo, TB);
          if constexpr (!SH && k == 9) adv(os, (j == 2 && it == 7) ? 2 * TB : TB);
        });
        if constexpr (!SH) { check_flags<0, j * 128>(fw, fr, pv, tag, dl); fetch(IC<(j + 2) & 3>{}); }
        dma<((j + 2) & 3) * RT, 1>(rs, l16, mr);
        prio<0>();
        if constexpr (SH) {
          static_for<0, 4>([&](auto mc) { constexpr int m = decltype(mc)::value; lds_rd<(j & 1) * 4096 + m * 1024>(e[m], ea); });
        }
        lds_rd<j1 * RT>(x, ra);
        prio<3>();
        tie(a0, a1);
        gst16(xo, l16, pack8(a0, a1));
        asm volatile("s_waitcnt lgkmcnt(0)" : "+v"(x) :: "memory");
        if constexpr (SH) {
          tie(e[0], e[1], e[2], e[3]);
          gst16(xo + 3072, l16, pack8(e[0] + e[2], e[1] + e[3]));
        }
        init(x);
        prio<0>();
        prime<(j1 & 1) * TB>(bq, la[j1 >> 1]);
        if constexpr (j == 1) adv(fr, (it == 7) ? 128 : 0);
        if constexpr (!SH) gld4<(j + 1) * 128>(fw, fr, pv);
        if constexpr (j == 3) { adv(fr, 512); wait_lg<0>(); }
      });
      wait_vm<0>();
      if constexpr (!SH) tie(fw);
      bar_bare();
      if constexpr (SH) gst4m<1, 640>(fp, l16, vt);
    };
    if (wave == 5) loop(std::true_type{}); else loop(std::false_type{});
  }
}

template <class F> __device__ __forceinline__ void qa_periods(F&& f) {
#pragma nounroll
  for (int it = 0; it < 16; ++it) static_for<0, 4>([&](auto jc) { f(jc, IC<0>{}, it); });
  static_for<0, 4>([&](auto jc) { f(jc, IC<1>{}, 16); });
}
template <bool A> __device__ __forceinline__ void role_qa(char* smem, const u16* wq, const Ctx& cx, int jq) {
  const int wave = cx.wave, lane = cx.lane;
  const unsigned sm0 = lds_addr(smem);
  unsigned tag;
  const int r = lane & 15, g = lane >> 4, q = r >> 2, p = lane & 3;
  constexpr int XR = 4 * TB, PP = XR + 2048, VB = PP + 8192, ST = VB + 6144;
  const f32x4 z4 = {0.f, 0.f, 0.f, 0.f};
  const unsigned l16 = vopq(lane * 16);
  const bool low = cx.g < 2;
  const char* xs = cx.ws + WS_X0 + (size_t)((cx.g & 1) * 64 + top(cx.g)) * TB;
  const char* fr = cx.ws + WS_F2 + ((cx.g & 1) * 72 + top(cx.g)) * F2ROW;
  const char* sr = cx.ws + WS_SR + ((cx.g & 1) * 64 + top(cx.g)) * SRB;
  f32x2 sv[5];
  const unsigned l8 = vopq(lane * 8), l8e = vopq((lane & 31) * 8);
  auto sums = [&]() { static_for<0, 4>([&](auto ic) { gld8<decltype(ic)::value * 512>(sv[decltype(ic)::value], sr, l8); }); gld8<2048>(sv[4], sr, l8e); };
  bf16x8 W[18][2];
  f32x4 cs[2] = {z4, z4};
  bf16x8 one = ones8();
  tie(one);
  {
    constexpr int BUF = 41472, B0 = 2 * TB;
    const char* src = (const char*)wq + jq * 144 + wave * 12 * (NQKV * 2);
    const unsigned sg = (min(lane, 53) * 57) >> 9, vo = sg * (DM * 2 - 144) + min(lane, 53) * 16;
    int c0, c1;
    if (!A) { c0 = 36 * (wave & 3) + (wave & 1) * 4 + p * 8; c1 = c0 + 144; }
    else if (wave < 2) { c0 = 288 + 72 * wave + p * 8; c1 = c0 + 40; }
    else { c0 = (p >> 1) * 144 + 32 + (p & 1) * 72; c1 = 288 + 32 + (p & 1) * 72; }
    const char* rb = smem + B0 + (8 * g + q) * 432;
    unsigned f0; const unsigned f0v = vopq(F_X0 * 128 + (lane & 3) * F2ROW);
    load_w<6, 6, A ? 10 : 5>(smem, cx, tag, [&](auto rc) {
      constexpr int rr = decltype(rc)::value;
      if constexpr (rr == 5) gld4<0>(f0, fr, f0v);
#pragma unroll
      for (int i = 0; i < 6; ++i) dma_w54(src + (rr * 96 + 2 * i) * (NQKV * 2), vo, sm0 + B0 + (rr % 3) * BUF + (wave * 6 + i) * 864);
    }, [&](auto rc) {
      constexpr int rr = decltype(rc)::value;
#pragma unroll
      for (int kl = 0; kl < 3; ++kl)
#pragma unroll
        for (int m = 0; m < 2; ++m) {
          const char* a = rb + (rr % 3) * BUF + kl * 32 * 432 + (m ? c1 : c0);
          W[3 * rr + kl][m] = tr_frag(a, a + 4 * 432);
        }
      tie(W[3 * rr][0], W[3 * rr][1], W[3 * rr + 1][0], W[3 * rr + 1][1], W[3 * rr + 2][0], W[3 * rr + 2][1]);
    }, [&](auto rc) {
      if constexpr (decltype(rc)::value == 3) {
        check_flags<6>(f0, fr, f0v, tag, cx.dl);
        dma<0, 4>(xs + wave * 4608, l16, sm0 + wave * 4608); dma<4096, 0>(xs + wave * 4608, l16 + 4096, sm0 + wave * 4608);
        if constexpr (A) sums();
      }
#pragma unroll
      for (int k = 0; k < 6; ++k) cs[k & 1] = MFMA(one, W[3 * decltype(rc)::value + (k >> 1)][k & 1], cs[k & 1]);
      tie(cs[0], cs[1]);
    });
  }
  auto quad = [&](const f32x4& c) {
    f32x4 o;
#pragma unroll
    for (int j = 0; j < 4; ++j) o[j] = u2f(__builtin_amdgcn_ds_bpermute((4 * g + j) * 4, f2i(c[0])));
    return o;
  };
  const unsigned la[2] = {vopq(sm0 + l16), vopq(sm0 + l16 + 2 * TB)};
  if constexpr (A) {
    wait_vm<0>();
    tie(sv[0], sv[1], sv[2], sv[3], sv[4]);
    if (wave == 3) {
      const unsigned sa = vopq(sm0 + ST + r * 8), pa = vopq(sm0 + PP + l16), va = vopq(sm0 + VB + l16), vt = vopq(tag);
      const float g2 = (g < 2) ? 1.f : 0.f;
      auto stats = [&](auto jc, f32x2 t) { tie(t); lds_wr8<decltype(jc)::value * 128>(sa, row_stat(sum_g(t[0]), sum_g(t[1]))); };
      constexpr int NB = 14 - 4 * NA;
      const unsigned pv2 = vopq(min(lane, 17) * 128);
      unsigned pv = vopq(F_X0 * 128), dl = cx.dl, fw = vopq(tag), vf[5];
#pragma unroll
      for (int i = 0; i < 5; ++i) vf[i] = vopq(l16 + i * 4096);
      adv(fr, 2 * F2ROW); adv(xs, 2 * TB);
      u32x4 vw[5] = {};
      const char* ob = cx.ws + WS_O + (size_t)cx.g * 68 * TB + jq * 2048;
      const char* fp = cx.fp;
      const unsigned l1 = vopq(l16 + 1024), lx = vopq(l16 + 16384 - jq * 1792);
      f32x2 t0 = (sv[0] + sv[1]) + (sv[2] + sv[3]) + sv[4] * g2;
      tie(t0);
      adv(sr, SRB); sums(); adv(sr, SRB);
      stats(IC<0>{}, t0);
      prio<PR_A>();
      bar_raw();
      qa_periods([&](auto jc, auto ec, int it) {
        constexpr int j = decltype(jc)::value, o = 1 - (j & 1); constexpr bool END = decltype(ec)::value, MORE = !END || j == 0;
        bf16x8 pf[4]; u32x2 vn[5]; f32x4 d[8];
        int ds = SRB, dx = TB;
        auto rows = [&](auto ic) { constexpr int i = decltype(ic)::value; if constexpr (!END) dma<((j + 2) & 3) * TB + i * 4096, (i < 4) ? 4 : 2>(xs, vf[i], sm0); };
        wait_vm<MORE ? NB + 5 : 0>();
        gst4m<1, j * 128>(fp, l16, vt);
        if constexpr (END) tie(fw);
        else {
          check_flags<63>(fw, fr, pv, tag, dl);
          adv(fr, F2ROW);
          if constexpr (j == 1) { const bool stay = low && it == 7; ds = stay ? 0 : SRB; dx = stay ? 0 : TB; dl = stay ? 0u : dl; }
          if constexpr (j == 2) {
            const bool stay = low && it == 15; ds = stay ? 0 : SRB; dx = stay ? 0 : TB; dl = stay ? 0u : cx.dl;
            const bool hop = it == 7;
            ds = hop ? (low ? 97 * SRB : 96 * SRB) : ds; dx = hop ? (low ? -159 * TB : -160 * TB) : dx; adv(fr, hop ? -33 * F2ROW : 0);
            pv = vopq(it >= 7 ? pv2 : pv);
          }
        }
        static_for<0, 4>([&](auto hc) { constexpr int h = decltype(hc)::value; lds_rd<o * 4096 + h * 1024>(pf[h], pa); });
        static_for<0, 5>([&](auto hc) { constexpr int h = decltype(hc)::value; lds_rd8<o * 3072 + (h >> 1) * 1024 + (h & 1) * 8>(vn[h], va); });
        if constexpr (!END) gld4<0>(fw, fr, pv);
        static_for<0, NA>([&](auto ic) { rows(ic); });
        wait_frag<5>();
        static_for<0, 8>([&](auto hc) { constexpr int h = decltype(hc)::value; mf<true>(d[h], asb(vw[h < 4 ? h : 4]), pf[h & 3]); });
        rows(IC<NA>{});
        asm volatile("s_nop 1" : "+v"(d[0]), "+v"(d[1]));
        gst16(ob, l16, pack8(d[0], d[1]));
        tie(d[2], d[3]);
        gst16(ob, l1, pack8(d[2], d[3]));
        tie(d[4], d[5], d[6], d[7]);
        const u32x4 n = widen(cvt2(d[6][0], d[6][1]), cvt2(d[7][2], d[7][3]));
        gst16m(ob, lx, u32x4{cvt2(d[4][0], d[4][1]), cvt2(d[5][2], d[5][3]), n[2], n[3]}, 0xFFFFull);
        static_for<NA + 1, 5>([&](auto ic) { rows(ic); });
        wait_lg<0>();
        static_for<0, 5>([&](auto hc) { constexpr int h = decltype(hc)::value; tie(vn[h]); vw[h][2 * o] = vn[h][0]; vw[h][2 * o + 1] = vn[h][1]; tie(vw[h]); });
        adv(ob, TB);
        if constexpr (MORE) {
          wait_vm<END ? 4 : 23>();
          tie(sv[0], sv[1], sv[2], sv[3], sv[4]);
          f32x2 t = (sv[0] + sv[1]) + (sv[2] + sv[3]) + sv[4] * g2;
          tie(t);
          if constexpr (!END) { sums(); adv(sr, ds); adv(xs, dx); }
          stats(IC<(j + 1) & 3>{}, t);
        }
        if constexpr (j == 3) adv(fp, 512);
        bar_raw();
      });
      wait_vm<0>();
      gst4m<1>(fp, l16, vt);
      if (cx.g == 0 && jq == 0) {
        for (int g2 = 0; g2 < 4; ++g2) poll(cx.ws + WS_FL + g2 * FGRP + 128, lane * FSTR, tag, cx.dl);
        gst4m<1>(cx.ws, l16, vopq((tag - 0x7F4A7C15u) * 244002641u + 1u));
      }
    } else {
      Ring<18, PDG, 1> bq;
      const unsigned sa = vopq(sm0 + ST + g * 32), va = vopq(sm0 + VB + wave * 1024 + l16);
      auto loop = [&](auto xc) {
        constexpr bool X = decltype(xc)::value;
        const float cv[2] = {cs[0][0], cs[1][0]};
        f32x4 cx4 = z4, a0, a1;
        unsigned sx = 0, xa = 0;
        if constexpr (X) {
          cx4 = quad(cs[0]); tie(cx4);
          sx = vopq(sm0 + ST + r * 8); xa = vopq(sm0 + XR + l8);
          *(unsigned*)(smem + XR + 512 + (lane & 1) * 1024) = 0u;
        }
        prio<PR_A>();
        bar_raw();
        qa_periods([&](auto jc, auto ec, int it) {
          constexpr int j = decltype(jc)::value, e = j & 1; constexpr bool END = decltype(ec)::value;
          if constexpr (!END || j < 2) {
            f32x4 s0, s1; f32x2 st;
            auto mm = [&](auto kc, auto ic, const bf16x8& x) {
              constexpr int k = decltype(kc)::value;
              if constexpr (decltype(ic)::value == 0) { if constexpr (X) mf<k == 0>(a0, W[k][0], x); else mf<k == 0>(a0, x, W[k][0]); }
              if constexpr (decltype(ic)::value == 1) mf<k == 0>(a1, x, W[k][1]);
            };
            prime<e * TB>(bq, la[j >> 1]);
            stream<e * TB, -1, 0>(bq, la[j >> 1], mm, [&](auto kc) {
              constexpr int k = decltype(kc)::value;
              if constexpr (k == 4) lds_rd<j * 128>(s0, sa);
              if constexpr (k == 5) lds_rd<j * 128 + 16>(s1, sa);
              if constexpr (k == 6 && X) lds_rd8<j * 128>(st, sx);
            });
            prio<PV1>();
            settle();
            tie(a0, a1, s0, s1);
            auto vals = [&](const f32x4& w, float c) {
              return u32x2{cvt2(fmaf(c, s0[0], w[0]) * s0[1], fmaf(c, s0[2], w[1]) * s0[3]), cvt2(fmaf(c, s1[0], w[2]) * s1[1], fmaf(c, s1[2], w[3]) * s1[3])};
            };
            if constexpr (X) {
              tie(st);
              const float is = (g < 2) ? 1.f : st[1];
              u32x2 v0 = {cvt2(fmaf(cx4[0], st[0], a0[0]) * is, fmaf(cx4[1], st[0], a0[1]) * is), cvt2(fmaf(cx4[2], st[0], a0[2]) * is, fmaf(cx4[3], st[0], a0[3]) * is)}, v1 = vals(a1, cv[1]);
              tie(v0, v1);
              prio<PV2>();
              lds_wr8<e * 1024>(xa, v0);
              lds_wr8<e * 3072>(va, v1);
            } else {
              const u32x2 v0 = vals(a0, cv[0]), v1 = vals(a1, cv[1]);
              u32x4 v = {v0[0], v0[1], v1[0], v1[1]};
              tie(v);
              prio<PV2>();
              lds_wr<e * 3072>(va, v);
            }
            prio<PR_A>();
          }
          bar_raw();
        });
      };
      if (wave == 2) loop(std::true_type{}); else loop(std::false_type{});
    }
  } else {
    wait_vm<0>();
    bar_bare();
    const int h = wave - 4;
    f32x4 cq = quad(cs[0]), ck = quad(cs[1]);
    tie(cq, ck);
    const float ninf = -__builtin_inff();
    f32x4 bo, bp;
#pragma unroll
    for (int e = 0; e < 4; ++e) { const int m = 4 * g + e; bo[e] = (m <= r && m >= r - 7) ? 0.f : ninf; bp[e] = (m >= r + 9) ? 0.f : ninf; }
    Soft f = {};
#pragma unroll
    for (int e = 0; e < 2; ++e) f.mo[e] = vopq((4 * g + 2 * e <= r ? 0xFFFFu : 0u) | (4 * g + 2 * e + 1 <= r ? 0xFFFF0000u : 0u));
    const unsigned qa = vopq(sm0 + XR + (h >> 1) * 128 + r * 8 + (h & 1) * 4), ka = vopq((g == 0) ? qa + 256 : sm0 + XR + 512);
    const unsigned sa = vopq(sm0 + ST + r * 8), pa = vopq(sm0 + PP + h * 1024 + l16);
    bf16x8 x[18];
    auto first = [&](auto oc, unsigned a) { static_for<0, 8>([&](auto kc) { lds_rd<decltype(oc)::value + decltype(kc)::value * 1024>(x[decltype(kc)::value], a); }); };
    first(IC<0>{}, la[0]);
    wait_lg<0>();
    f32x4 a0 = z4, a1 = z4, so = z4;
    u32x4 qf = {0u, 0u, 0u, 0u}, kf = qf;
    float sc = 0.f, scn = 0.f;
    tie(qf, kf, bo, bp);
    qa_periods([&](auto jc, auto ec, int it) {
      constexpr int j = decltype(jc)::value, e = j & 1, j1 = (j + 1) & 3; constexpr bool END = decltype(ec)::value;
      auto put = [&]() { lds_wr<e * 4096>(pa, f32x4{f.s[0], f.s[1], f.s[2], f.s[3]}); };
      if constexpr (END && j >= 2) {
        if constexpr (j == 2) { soft3(f); put(); }
        bar_raw();
      } else {
        prio<PR_B>();
        static_for<0, 5>([&](auto pc) {
          constexpr int k = 2 * decltype(pc)::value;
          wait_frag<6>();
          qk_pair<k / 2, e, e * TB + (k + 8) * 1024>(a0, a1, W[k], W[k + 1], x[k], x[k + 1], x[k + 8], x[k + 9], la[j >> 1], f);
        });
        wait_frag<4>();
        mf<false>(a0, W[10][0], x[10]);
        put();
        mf<false>(a1, W[10][1], x[10]);
        static_for<11, 14>([&](auto kc) {
          constexpr int k = decltype(kc)::value;
          wait_frag<5>();
          mf<false>(a0, W[k][0], x[k]);
          if constexpr (k == 13) asm volatile("s_setprio 3\n\ts_waitcnt lgkmcnt(0)" ::: "memory");
          mf<false>(a1, W[k][1], x[k]);
        });
        bar_bare();
        f32x2 st; unsigned xq, xk;
        mf8<j>(a0, a1, W[14], W[15], W[16], W[17], x[14], x[15], x[16], x[17], st, xk, xq, sa, ka, qa);
        asm volatile("s_nop 2\n\ts_waitcnt lgkmcnt(0)" : "+v"(st), "+v"(xq), "+v"(xk) :: "memory");
        kf[2] = xk;
        sc = scn; scn = st[1] * (0.23570226039551584f * 1.4426950408889634f);
        asm volatile("" : "+v"(kf), "+v"(scn), "+v"(a1));
        kf[0] = cvt2(fmaf(ck[0], st[0], a1[0]) * st[1], fmaf(ck[1], st[0], a1[1]) * st[1]);
        kf[1] = cvt2(fmaf(ck[2], st[0], a1[2]) * st[1], fmaf(ck[3], st[0], a1[3]) * st[1]);
        f32x4 sp;
        if constexpr ((!END && j == 0) || (END && j == 1)) {
          const float off = (low && (END || it == 8)) ? ninf : 0.f;
          sp = MFMA(asb(kf), asb(qf), bp + off);
        } else sp = MFMA(asb(kf), asb(qf), bp);
        __builtin_amdgcn_sched_barrier(0);
        tie(a0);
        qf[0] = cvt2(fmaf(cq[0], st[0], a0[0]), fmaf(cq[1], st[0], a0[1]));
        qf[1] = cvt2(fmaf(cq[2], st[0], a0[2]), fmaf(cq[3], st[0], a0[3]));
        qf[2] = xq;
        asm volatile("s_nop 1" : "+v"(qf), "+v"(sp));
        max4(f.s, so, sp);
        if constexpr (!(END && j == 1)) mfc(so, asb(kf), asb(qf), bo);
        soft1(f, sc);
        if constexpr (END) tie(bo);
        soft2<(END && j == 1) ? -1 : (j1 & 1) * TB>(f, x, la[j1 >> 1]);
        if constexpr (j == 3) wait_lg<0>();
      }
    });
  }
}

__global__ __launch_bounds__(512, 2) void k_main(const u16* x0, const u16* wqkv, const u16* wo,
                                                 const u16* wg, const u16* wu, const u16* wd,
                                                 char* ws, u16* out) {
  __shared__ __attribute__((aligned(1024))) char smem[SMEM_BYTES];
  Ctx cx;
  const unsigned dm = lds_addr(smem) + 162816;
  {
    unsigned long long pc; unsigned len;
    asm volatile("s_getpc_b64 %0\n.Lkbeg:\n\ts_mov_b32 %1, .Lkend-.Lkbeg" : "=s"(pc), "=s"(len));
    cx.code = (const char*)pc + min(((blockIdx.x & 8) * 64 + threadIdx.x) * 128, len);
    const unsigned i0 = blockIdx.x >> 3;
    if (i0 >= 22 && i0 < 30) touch(cx.code, dm);
    if (i0 < 5 && threadIdx.x < 64) touch((i0 < 4 ? (const char*)wqkv : (const char*)wo) + (threadIdx.x & 1) * 524288, dm);
    if (i0 >= 5 && i0 < 30) {
      const unsigned ln = min((i0 - 5) * 512 + threadIdx.x, 11519u), sg = (ln * 13108u) >> 16;
      const char* bw = (i0 * 512 + (threadIdx.x & 448) < 5 * 512 + 8640) ? (const char*)wqkv : (const char*)wo - 1728 * 1152;
      touch(bw + (sg * (1152 - 640) + ((blockIdx.x & 1) * 4 + ln) * 128), dm);
    }
  }
  cx.ws = ws;
  cx.wave = __builtin_amdgcn_readfirstlane(threadIdx.x >> 6);
  cx.lane = threadIdx.x & 63;
  asm volatile("s_cmp_eq_u32 %1, 4\n\ts_cselect_b64 %0, 1, 0" : "=s"(cx.w0) : "s"(cx.wave) : "scc");
  cx.dl = ((unsigned)__builtin_amdgcn_s_memtime() + SPIN) | 1u;
  const unsigned t0 = (unsigned)__builtin_amdgcn_s_memrealtime();
  const int bid = sopq(blockIdx.x), xh = bid & 1, x4 = bid & 3, qd = (bid >> 2) & 1, i = bid >> 3, lane = cx.lane, wave = cx.wave;
  const int d = i - 23, tri = d / 3;
  const int c = x4 * 9 + ((i - 5) >> 1), ks = (tri < 2) ? x4 : 4 + (x4 >> 1), ns = d - 3 * tri;
  cx.g = (i < 4) ? 2 * (1 - qd) + 1 - (x4 >> 1) : (i == 4) ? 2 * (1 - qd) + (x4 >> 1) : (i < 23) ? 2 * qd + ((i - 5) & 1) : 2 * (1 - qd) + (tri < 2 ? tri : xh);
  cx.fl = ws + WS_FL + cx.g * FGRP;
  cx.fp = cx.fl + (i < 4 ? F_O + xh * 4 + i : i == 4 ? F_X1 + xh : i < 23 ? F_H + c : F_P + ns * 6 + ks) * FSTR;
  const bool fast = i >= 30;
  auto warm = [&]() {
    if (wave < 2) touch(ws + min((size_t)(wave * 64 + lane) << 20, WS_END - 4), dm);
    else if (wave == 2) touch((const char*)out + min(lane << 20, 2359292), dm);
    else if (wave == 3 && i >= 5 && i < 11) {
      const int ln = min(((i - 5) * 2 + xh) * 64 + lane, 10 * 72 - 1);
      touch(ws + WS_FL + (size_t)((bid >> 1) & 3) * FGRP + F_X1 * FSTR + ln * 128, dm);
    }
  };
  if (!fast && i >= 5) warm();
  if (i >= 23) {
    u32x4 v[5];
    const int g0 = (bid >> 1) & 3, n0 = (fast ? i - 30 : 2 + (wave >> 2) * 7 + i - 23) * 2 + xh;
    const int k0 = fast ? 2 * wave : 4 * (wave & 3), nk = fast ? 2 : 4, wl = fast ? wave : wave & 3;
    const char* src = (const char*)x0 + (size_t)((g0 >> 1) * 1024 + 32 * (31 - n0) + (g0 & 1)) * (DM * 2);
    const char* dst = ws + WS_X0 + (size_t)((g0 & 1) * 64 + top(g0) + n0) * TB;
    const unsigned vr = (lane & 15) * 2304 + (lane >> 4) * 16;
    gld16(v[0], src + k0 * 64, vr); gld16(v[1], src + k0 * 64 + 64, vr); gld16(v[4], src + (16 + (wl & 1)) * 64, vr);
    gld16(v[2], src + (k0 + nk - 2) * 64, vr); gld16(v[3], src + (k0 + nk - 1) * 64, vr);
    const unsigned tag = get_tag(ws);
    wait_vm<0>();
    tie(v[0], v[1], v[2], v[3], v[4]);
    gst16(dst + k0 * 1024, lane * 16, v[0]); gst16(dst + k0 * 1024 + 1024, lane * 16, v[1]);
    if (wl < 2) gst16(dst + (16 + wl) * 1024, lane * 16, v[4]);
    if (!fast) { gst16(dst + k0 * 1024 + 2048, lane * 16, v[2]); gst16(dst + k0 * 1024 + 3072, lane * 16, v[3]); }
    const char* sr = ws + WS_SR + ((g0 & 1) * 64 + top(g0) + n0) * SRB;
#pragma unroll
    for (int k = 0; k < 5; ++k) {
      const f32x4 z = {0.f, 0.f, 0.f, 0.f};
      const f32x2 t = {MFMA(ones8(), asb(v[k]), z)[0], diag(MFMA(asb(v[k]), asb(v[k]), z), lane & 3)};
      if (k < 2 || (k < 4 ? !fast : wl < 2)) gst8m(sr + (k < 4 ? k0 + k : 16 + wl) * 128, (lane & 15) * 8, t, DIAG);
    }
    wait_vm<0>();
    bar_bare();
    if (wl == 0) gst4m<-1>(ws + WS_F2 + ((g0 & 1) * 72 + top(g0) + n0) * F2ROW + F_X0 * 128, lane * 4, vopq(tag));
    if (fast) warm();
  }
  if (i >= 5) {
    const unsigned z = ~get_tag(ws);
    gst16(cx.fp + wave * 1024, lane * 16, u32x4{z, z, z, z});
    if (wave == 0) gst16(cx.fp + 8192, lane * 16, u32x4{z, z, z, z});
  }
  if (i >= 5) { const unsigned dly = (i < 23) ? DLY_G : DLY_D; while ((unsigned)__builtin_amdgcn_s_memrealtime() - t0 < dly) __builtin_amdgcn_s_sleep(4); }
  if (i < 4) { if (wave < 4) role_qa<true>(smem, wqkv, cx, xh * 4 + i); else role_qa<false>(smem, wqkv, cx, xh * 4 + i); }
  else if (i == 4) { if (wave < 4) role_wo<true>(smem, wo, cx, xh); else role_wo<false>(smem, wo, cx, xh); }
  else if (i < 23) role_gu(smem, wg, wu, out, cx, c);
  else role_dn(smem, wd, cx, ks, ns);
  asm volatile(".subsection 2\n.Lkend:\n\t.fill 64, 4, 0xBF800000\n\t.subsection 0");
}
#if defined(__HIP_DEVICE_COMPILE__)
#pragma clang attribute pop
#endif

extern "C" void kernel_launch(void* const* d_in, const int* in_sizes, int n_in,
                              void* d_out, int out_size, void* d_ws, size_t ws_size,
                              hipStream_t stream) {
  k_main<<<256, 512, 0, stream>>>((const u16*)d_in[0], (const u16*)d_in[1], (const u16*)d_in[2], (const u16*)d_in[4],
                                  (const u16*)d_in[5], (const u16*)d_in[6], (char*)d_ws, (u16*)d_out);
}
```
